```python
import torch

def _softmax(x, axis=-1):
    return torch.softmax(x, dim=axis)
def _gelu(x):
    return torch.nn.functional.gelu(x, approximate="tanh")
def _silu(x):
    return torch.nn.functional.silu(x)
def _sigmoid(x):
    return torch.sigmoid(x)
def _layernorm(x, axis=-1, eps=1e-5):
    m = x.mean(dim=axis, keepdim=True)
    v = x.var(dim=axis, keepdim=True, unbiased=False)
    return (x - m) / torch.sqrt(v + eps)
def _scatter_add(tgt, idx, src, axis=0):
    return tgt.clone().index_add_(axis, idx, src)
def _segment_sum(src, seg, n):
    out = torch.zeros((n,) + src.shape[1:], dtype=src.dtype)
    return out.index_add_(0, seg, src)
def _pad(x, pw):
    flat = tuple(v for pair in reversed(pw) for v in pair)
    return torch.nn.functional.pad(x, flat)

kernel_name = 'recurrent_scan__spectral_fnet__mixer_channel_mlp__prompt'

def setup_inputs():

    _w = (lambda a: (lambda t: t.to(torch.bfloat16) if t.is_floating_point() else t)(torch.as_tensor(a)))
    def _A_setup():
        B, S, D = 1, 2048, 512
        ns = torch.arange(S)[:, None] * torch.arange(S)[None, :]
        nd = torch.arange(D)[:, None] * torch.arange(D)[None, :]
        Ws_re = torch.cos(-2.0 * torch.pi * ns / S).to(float)
        Ws_im = torch.sin(-2.0 * torch.pi * ns / S).to(float)
        Wd_re = torch.cos(-2.0 * torch.pi * nd / D).to(float)
        Wd_im = torch.sin(-2.0 * torch.pi * nd / D).to(float)
        return {
            "x": _w((lambda *s: torch.randn(*s, dtype=torch.bfloat16) * 0.05)(B, S, D)),
            "dft_seq_re": _w(Ws_re),
            "dft_seq_im": _w(Ws_im),
            "dft_hid_re": _w(Wd_re),
            "dft_hid_im": _w(Wd_im),
        }
    _A_in = _A_setup()

    def _B_setup():
        B, S, D, F = 1, 2048, 512, 1376
        return {
            "x": _w((lambda *s: torch.randn(*s, dtype=torch.bfloat16) * 0.05)(B, S, D)),
            "w1": _w((lambda *s: torch.randn(*s, dtype=torch.bfloat16) * 0.8)(D, F) * (D ** -0.5)),
            "b1": _w((lambda *s: torch.randn(*s, dtype=torch.bfloat16) * 0.8)(F)),
            "w2": _w((lambda *s: torch.randn(*s, dtype=torch.bfloat16) * 0.05)(F, D) * (F ** -0.5)),
            "b2": _w((lambda *s: torch.randn(*s, dtype=torch.bfloat16) * 0.05)(D)),
        }
    _B_in = _B_setup()

    return {
        "A_x": _A_in["x"],
        "A_dft_seq_re": _A_in["dft_seq_re"],
        "A_dft_seq_im": _A_in["dft_seq_im"],
        "A_dft_hid_re": _A_in["dft_hid_re"],
        "A_dft_hid_im": _A_in["dft_hid_im"],
        "B_x": _B_in["x"],
        "B_w1": _B_in["w1"],
        "B_b1": _B_in["b1"],
        "B_w2": _B_in["w2"],
        "B_b2": _B_in["b2"],
    }

def reference(A_x, A_dft_seq_re, A_dft_seq_im, A_dft_hid_re, A_dft_hid_im, B_x, B_w1, B_b1, B_w2, B_b2):
    def _A_ref(x, dft_seq_re, dft_seq_im, dft_hid_re, dft_hid_im):
        a = torch.einsum("bsd,ed->bse", x, dft_hid_re)
        b = torch.einsum("bsd,ed->bse", x, dft_hid_im)
        y = torch.einsum("ts,bse->bte", dft_seq_re, a) - torch.einsum("ts,bse->bte", dft_seq_im, b)
        return y

    def _B_ref(x, w1, b1, w2, b2):
        y = _layernorm(x, axis=-1)
        h = _gelu(torch.einsum("bsd,df->bsf", y, w1) + b1)
        return torch.einsum("bsf,fd->bsd", h, w2) + b2

    x = A_x
    xn = _layernorm(x, axis=-1)
    s = _A_ref(xn, A_dft_seq_re, A_dft_seq_im, A_dft_hid_re, A_dft_hid_im)
    g = _silu(_B_ref(xn, B_w1, B_b1, B_w2, B_b2))
    return x + s * g
```

```cpp
#include <hip/hip_runtime.h>
#include <hip/hip_bf16.h>
#include <stdint.h>
typedef unsigned short u16;
__device__ __forceinline__ float bf2f(u16 v){ return __uint_as_float(((unsigned)v)<<16); }
__device__ __forceinline__ u16 f2bf(float f){ unsigned u=__float_as_uint(f); u += 0x7FFF + ((u>>16)&1); return (u16)(u>>16); }
__device__ __forceinline__ float rbf(float f){ return bf2f(f2bf(f)); }

__global__ void ln_kernel(const u16* x, u16* xn, u16* y){
  int r = blockIdx.x; int l = threadIdx.x;
  float v[8];
  for(int j=0;j<8;j++) v[j]=bf2f(x[r*512+l*8+j]);
  for(int pass=0;pass<2;pass++){
    float s=0; for(int j=0;j<8;j++) s+=v[j];
    for(int o=32;o>0;o>>=1) s+=__shfl_xor(s,o);
    float m=s/512.f;
    float q=0; for(int j=0;j<8;j++){ float d=v[j]-m; q+=d*d; }
    for(int o=32;o>0;o>>=1) q+=__shfl_xor(q,o);
    float var=rbf(q/512.f); float mb=rbf(m);
    float sd=rbf(sqrtf(rbf(var+1e-5f)));
    for(int j=0;j<8;j++){ v[j]=rbf(rbf(v[j]-mb)/sd); }
    u16* o = pass==0? xn : y;
    for(int j=0;j<8;j++) o[r*512+l*8+j]=f2bf(v[j]);
  }
}
__global__ void gemm_naive(const u16* A, const u16* B, float* C, int M,int N,int K,int bt){
  int n = blockIdx.x*64+threadIdx.x; int m = blockIdx.y;
  if(n>=N) return;
  float acc=0;
  if(bt){ for(int k=0;k<K;k++) acc+=bf2f(A[(size_t)m*K+k])*bf2f(B[(size_t)n*K+k]); }
  else { for(int k=0;k<K;k++) acc+=bf2f(A[(size_t)m*K+k])*bf2f(B[(size_t)k*N+n]); }
  C[(size_t)m*N+n]=acc;
}
__global__ void tobf(const float* a, u16* b, int n){ int i=blockIdx.x*256+threadIdx.x; if(i<n) b[i]=f2bf(a[i]); }
__global__ void gelu_k(const float* a, const u16* b1, u16* h, int n, int F){ int i=blockIdx.x*256+threadIdx.x; if(i<n){ float v=rbf(rbf(a[i])+bf2f(b1[i%F])); float u=0.7978845608f*(v+0.044715f*v*v*v); h[i]=f2bf(0.5f*v*(1.f+tanhf(u))); } }
__global__ void fin_k(const u16* x, const float* y1, const float* y2, const float* o, const u16* b2, u16* out, int n){ int i=blockIdx.x*256+threadIdx.x; if(i<n){ float s=rbf(rbf(y1[i])-rbf(y2[i])); float v=rbf(rbf(o[i])+bf2f(b2[i%512])); float g=rbf(v/(1.f+expf(-v))); out[i]=f2bf(bf2f(x[i])+rbf(s*g)); } }

extern "C" void kernel_launch(void* const* d_in, const int* in_sizes, int n_in, void* d_out, int out_size, void* d_ws, size_t ws_size, hipStream_t stream){
  const u16* x=(const u16*)d_in[0]; const u16* cre=(const u16*)d_in[1]; const u16* cim=(const u16*)d_in[2];
  const u16* wre=(const u16*)d_in[3]; const u16* wim=(const u16*)d_in[4];
  const u16* w1=(const u16*)d_in[6]; const u16* b1=(const u16*)d_in[7]; const u16* w2=(const u16*)d_in[8]; const u16* b2=(const u16*)d_in[9];
  char* ws=(char*)d_ws;
  u16* xn=(u16*)ws; u16* y=(u16*)(ws+(2<<20)); u16* a=(u16*)(ws+(4<<20)); u16* b=(u16*)(ws+(6<<20)); u16* h=(u16*)(ws+(8<<20));
  float* t0=(float*)(ws+(16<<20)); float* t1=(float*)(ws+(32<<20)); float* t2=(float*)(ws+(48<<20));
  ln_kernel<<<2048,64,0,stream>>>(x,xn,y);
  gemm_naive<<<dim3(8,2048),64,0,stream>>>(xn,wre,t0,2048,512,512,1);
  tobf<<<4096,256,0,stream>>>(t0,a,2048*512);
  gemm_naive<<<dim3(8,2048),64,0,stream>>>(xn,wim,t0,2048,512,512,1);
  tobf<<<4096,256,0,stream>>>(t0,b,2048*512);
  gemm_naive<<<dim3(22,2048),64,0,stream>>>(y,w1,t0,2048,1376,512,0);
  gelu_k<<<(2048*1376+255)/256,256,0,stream>>>(t0,b1,h,2048*1376,1376);
  gemm_naive<<<dim3(8,2048),64,0,stream>>>(cre,a,t0,2048,512,2048,0);
  gemm_naive<<<dim3(8,2048),64,0,stream>>>(cim,b,t1,2048,512,2048,0);
  gemm_naive<<<dim3(8,2048),64,0,stream>>>(h,w2,t2,2048,512,1376,0);
  fin_k<<<4096,256,0,stream>>>(x,t0,t1,t2,b2,(u16*)d_out,2048*512);
}
```

```cpp
#include <hip/hip_runtime.h>
#include <hip/hip_bf16.h>
#include <stdint.h>

typedef unsigned short u16;
typedef short v4s __attribute__((ext_vector_type(4)));
typedef short bf16x8 __attribute__((ext_vector_type(8)));
typedef __bf16 v2bf __attribute__((ext_vector_type(2)));
typedef float f32x4 __attribute__((ext_vector_type(4)));
typedef float f32x16 __attribute__((ext_vector_type(16)));
typedef unsigned u32x4 __attribute__((ext_vector_type(4)));
typedef unsigned u32x2 __attribute__((ext_vector_type(2)));
#define LAS __attribute__((address_space(3)))
#define GAS __attribute__((address_space(1)))

constexpr int S_ = 2048, D_ = 512, F_ = 1376;
constexpr int NKK_B = 86;
constexpr int NKK_H = 22;
constexpr size_t WS_B    = 0;
constexpr size_t WS_H    = 8u << 20;
constexpr size_t WS_SLAB = 16u << 20;
constexpr size_t WS_CNT  = 40u << 20;

__device__ __forceinline__ float bf2f(u16 v) { return __uint_as_float(((unsigned)v) << 16); }
__device__ __forceinline__ float bflo(unsigned v) { return __uint_as_float(v << 16); }
__device__ __forceinline__ float bfhi(unsigned v) { return __uint_as_float(v & 0xffff0000u); }
__device__ __forceinline__ unsigned pk_bf16(float lo, float hi) {
  typedef float f32x2_ __attribute__((ext_vector_type(2)));
  v2bf r = __builtin_convertvector((f32x2_){lo, hi}, v2bf);
  return __builtin_bit_cast(unsigned, r);
}
__device__ __forceinline__ float rbf(float f) { return bflo(pk_bf16(f, 0.f)); }
__device__ __forceinline__ int swap23(int r) { return (r & ~12) | ((r & 4) << 1) | ((r & 8) >> 1); }
__device__ __forceinline__ float dot2(unsigned a, unsigned b, float c) {
  return __builtin_amdgcn_fdot2_f32_bf16(__builtin_bit_cast(v2bf, a), __builtin_bit_cast(v2bf, b), c, false);
}
#define GLDS16(g, l) __builtin_amdgcn_global_load_lds((const GAS void*)(g), (LAS void*)(l), 16, 0, 0)
#define WAIT_V(n) asm volatile("s_waitcnt vmcnt(%0)" ::"n"(n) : "memory")
#define WAIT_L0() asm volatile("s_waitcnt lgkmcnt(0)" ::: "memory")
#define BAR() __builtin_amdgcn_s_barrier()
#define ST(k, i)

__global__ __launch_bounds__(512, 2) void k1(const u16* __restrict__ x, const u16* __restrict__ wre, const u16* __restrict__ wim,
                                             const u16* __restrict__ w1, const u16* __restrict__ b1, const u16* __restrict__ w2,
                                             char* __restrict__ ws) {
  extern __shared__ __attribute__((aligned(16))) char lds[];
  const int tid = threadIdx.x, lane = tid & 63, wid = __builtin_amdgcn_readfirstlane(tid >> 6);
  const int b = blockIdx.x, xcd = b & 7, idx = b >> 3;
  const int rt = (xcd >> 1) * 4 + (idx & 3);
  const int cg = (xcd & 1) * 8 + (idx >> 2);
  const bool typeA = cg < 7;
  int u0, ucnt;
  if (typeA) { u0 = cg < 4 ? cg * 5 : 20 + (cg - 4) * 4; ucnt = cg < 4 ? 5 : 4; }
  else { int g = cg - 7; u0 = g < 7 ? g * 5 : 35 + (g - 7) * 4; ucnt = g < 7 ? 5 : 4; }
  const int m = wid & 3, p = wid >> 2;
  const int r31 = lane & 31, hh = lane >> 5;

  ST(0, 0);
  u16 w2v[8];
  int w2chunk = b * 352 + tid;
  if (tid < 352) {
    int c = w2chunk;
    int ln = c & 63, sub = (c >> 6) & 3, nt = (c >> 8) & 3, rest = c >> 10;
    int j = rest / 22, kkw = rest - j * 22;
    int d = 128 * j + 32 * nt + swap23(ln & 31);
    int fb = 64 * kkw + 16 * sub + 4 * (ln >> 5);
#pragma unroll
    for (int jj = 0; jj < 8; ++jj) { int f = fb + 8 * (jj >> 2) + (jj & 3); w2v[jj] = f < F_ ? w2[(size_t)f * D_ + d] : (u16)0; }
  }
  if (b == 0 && tid < 64) *(unsigned*)(ws + WS_CNT + tid * 256) = 0u;

  {
    const u16* xb = x + (size_t)(rt * 128 + wid * 16) * D_;
#pragma unroll
    for (int i = 0; i < 16; ++i) GLDS16(xb + i * D_ + ((lane ^ i) * 8), lds + (wid * 16 + i) * 1024);
  }
  auto stage_panel = [&](int u, int slot_off) {
    if (typeA) {
      const u16* wb = (u < 16 ? wre + (size_t)u * 32 * D_ : wim + (size_t)(u - 16) * 32 * D_) + (size_t)(wid * 4) * D_;
#pragma unroll
      for (int i = 0; i < 4; ++i) GLDS16(wb + i * D_ + ((lane ^ ((wid * 4 + i) & 15)) * 8), lds + slot_off + (wid * 4 + i) * 1024);
    } else {
      const u16* wb = w1 + (size_t)(wid * 64 + (lane >> 2)) * F_ + u * 32 + (lane & 3) * 8;
#pragma unroll
      for (int i = 0; i < 4; ++i) GLDS16(wb + (size_t)i * 16 * F_, lds + slot_off + (wid * 4 + i) * 1024);
    }
  };
  stage_panel(u0, 131072);
  WAIT_V(0);
  ST(0, 1);
  if (tid < 352) {
    u32x4 v;
    v.x = w2v[0] | ((unsigned)w2v[1] << 16); v.y = w2v[2] | ((unsigned)w2v[3] << 16);
    v.z = w2v[4] | ((unsigned)w2v[5] << 16); v.w = w2v[6] | ((unsigned)w2v[7] << 16);
    int c = w2chunk; int rest = c >> 10; int j = rest / 22, kkw = rest - j * 22;
    *(u32x4*)(ws + WS_B + ((size_t)(j * NKK_B + 64 + kkw) << 14) + (size_t)(c & 1023) * 16) = v;
  }
  BAR();
  bf16x8 xf[32];
  {
    int row = m * 32 + (typeA ? swap23(r31) : r31);
    unsigned base = row * 1024 + ((hh ^ (row & 15)) * 16);
#pragma unroll
    for (int ks = 0; ks < 32; ++ks)
      xf[ks] = *(const bf16x8*)(lds + ((base ^ ((ks & 7) * 32)) + (ks >> 3) * 256));
  }
  WAIT_L0();
  __builtin_amdgcn_sched_barrier(0);
  BAR();
  ST(0, 2);
  for (int i = 1; i < ucnt; ++i) stage_panel(u0 + i, (i - 1) * 32768);

  float s0 = 0.f, s1 = 0.f, q0 = 0.f, q1 = 0.f;
#pragma unroll
  for (int ks = 0; ks < 32; ++ks) {
    u32x4 v = __builtin_bit_cast(u32x4, xf[ks]);
    s0 = dot2(v.x, 0x3f803f80u, s0); s1 = dot2(v.y, 0x3f803f80u, s1); s0 = dot2(v.z, 0x3f803f80u, s0); s1 = dot2(v.w, 0x3f803f80u, s1);
    q0 = dot2(v.x, v.x, q0); q1 = dot2(v.y, v.y, q1); q0 = dot2(v.z, v.z, q0); q1 = dot2(v.w, v.w, q1);
  }
  float sum = s0 + s1, sq = q0 + q1;
  sum += __shfl_xor(sum, 32); sq += __shfl_xor(sq, 32);
  const float mean = sum * (1.f / 512.f);
  const float var = fmaxf(sq * (1.f / 512.f) - mean * mean, 0.f);
  const float m_bf = rbf(mean);
  const float sd = rbf(sqrtf(rbf(rbf(var) + 1e-5f)));
  float scale, center;
  if (typeA) { scale = 1.f / sd; center = m_bf; }
  else {
    const float inv = 1.f / sd;
    const float m2 = rbf((mean - m_bf) * inv);
    const float v2 = rbf(var * inv * inv);
    const float sd2 = rbf(sqrtf(rbf(v2 + 1e-5f)));
    scale = inv / sd2; center = m_bf + m2 * sd;
  }
  ST(0, 3);
  WAIT_V(0);
  BAR();
  ST(0, 4);

  if (typeA) {
    float sc[16], cs_[16];
#pragma unroll
    for (int rg = 0; rg < 16; ++rg) { int rho = (rg & 3) + 8 * (rg >> 2) + 4 * hh; sc[rg] = __shfl(scale, rho); cs_[rg] = __shfl(center, rho); }
    const int wrow = swap23(r31);
    const unsigned wbase = wrow * 1024 + ((hh ^ (wrow & 15)) * 16);
    for (int i = p; i < ucnt; i += 2) {
      const int u = u0 + i;
      const char* pan = lds + (i == 0 ? 131072 : (i - 1) * 32768);
      f32x16 acc = {};
      float c0 = 0.f, c1 = 0.f;
      bf16x8 wf[2][4];
#pragma unroll
      for (int e = 0; e < 4; ++e) wf[0][e] = *(const bf16x8*)(pan + ((wbase ^ (e * 32))));
#pragma unroll
      for (int g = 0; g < 8; ++g) {
        if (g < 7) {
#pragma unroll
          for (int e = 0; e < 4; ++e) { const int ks = g * 4 + 4 + e; wf[(g + 1) & 1][e] = *(const bf16x8*)(pan + ((wbase ^ ((ks & 7) * 32)) + (ks >> 3) * 256)); }
        }
#pragma unroll
        for (int e = 0; e < 4; ++e) {
          acc = __builtin_amdgcn_mfma_f32_32x32x16_bf16(xf[g * 4 + e], wf[g & 1][e], acc, 0, 0, 0);
          u32x4 v = __builtin_bit_cast(u32x4, wf[g & 1][e]);
          c0 = dot2(v.x, 0x3f803f80u, c0); c1 = dot2(v.y, 0x3f803f80u, c1); c0 = dot2(v.z, 0x3f803f80u, c0); c1 = dot2(v.w, 0x3f803f80u, c1);
        }
        __builtin_amdgcn_sched_barrier(0);
      }
      float cs = c0 + c1; cs += __shfl_xor(cs, 32);
      const float sg = u < 16 ? 1.f : -1.f;
      float o[16];
#pragma unroll
      for (int rg = 0; rg < 16; ++rg) o[rg] = (acc[rg] - cs_[rg] * cs) * (sc[rg] * sg);
      const int et = u & 15, j = et >> 2, nt = et & 3, s0r = rt * 128 + m * 32;
      const int kk = (u >> 4) * 32 + (s0r >> 6), sub = (s0r & 63) >> 4;
      char* dst = ws + WS_B + ((size_t)((j * NKK_B + kk) * 16 + nt * 4 + sub) << 10) + lane * 16;
      u32x4 v0 = {pk_bf16(o[0], o[1]), pk_bf16(o[2], o[3]), pk_bf16(o[4], o[5]), pk_bf16(o[6], o[7])};
      u32x4 v1 = {pk_bf16(o[8], o[9]), pk_bf16(o[10], o[11]), pk_bf16(o[12], o[13]), pk_bf16(o[14], o[15])};
      *(u32x4*)dst = v0; *(u32x4*)(dst + 1024) = v1;
    }
  } else {
    const unsigned tbase = (8 * hh + ((lane & 15) >> 2)) * 64 + ((lane >> 4) & 1) * 32 + (lane & 3) * 8;
    for (int i = p; i < ucnt; i += 2) {
      const int u = u0 + i;
      const char* pan = lds + (i == 0 ? 131072 : (i - 1) * 32768) + tbase;
      f32x16 acc = {};
      float c0 = 0.f, c1 = 0.f;
      bf16x8 wf[2][4];
      auto trd = [&](int ks) {
        v4s t0 = __builtin_amdgcn_ds_read_tr16_b64_v4i16((LAS v4s*)(pan + ks * 1024));
        v4s t1 = __builtin_amdgcn_ds_read_tr16_b64_v4i16((LAS v4s*)(pan + ks * 1024 + 256));
        return (bf16x8)__builtin_shufflevector(t0, t1, 0, 1, 2, 3, 4, 5, 6, 7);
      };
#pragma unroll
      for (int e = 0; e < 4; ++e) wf[0][e] = trd(e);
#pragma unroll
      for (int g = 0; g < 8; ++g) {
        if (g < 7) {
#pragma unroll
          for (int e = 0; e < 4; ++e) wf[(g + 1) & 1][e] = trd(g * 4 + 4 + e);
        }
#pragma unroll
        for (int e = 0; e < 4; ++e) {
          acc = __builtin_amdgcn_mfma_f32_32x32x16_bf16(wf[g & 1][e], xf[g * 4 + e], acc, 0, 0, 0);
          u32x4 v = __builtin_bit_cast(u32x4, wf[g & 1][e]);
          c0 = dot2(v.x, 0x3f803f80u, c0); c1 = dot2(v.y, 0x3f803f80u, c1); c0 = dot2(v.z, 0x3f803f80u, c0); c1 = dot2(v.w, 0x3f803f80u, c1);
        }
        __builtin_amdgcn_sched_barrier(0);
      }
      float cs = c0 + c1; cs += __shfl_xor(cs, 32);
      float o[16];
#pragma unroll
      for (int g4 = 0; g4 < 4; ++g4) {
        const u32x2 bb = *(const u32x2*)(b1 + u * 32 + 8 * g4 + 4 * hh);
        const float bv[4] = {bflo(bb.x), bfhi(bb.x), bflo(bb.y), bfhi(bb.y)};
#pragma unroll
        for (int e = 0; e < 4; ++e) {
          const int rg = g4 * 4 + e;
          const float csf = __shfl(cs, 8 * g4 + 4 * hh + e);
          const float v = (acc[rg] - center * csf) * scale + bv[e];
          const float t = v * (-2.302208198f - 0.1029432397f * v * v);
          o[rg] = v * __builtin_amdgcn_rcpf(1.f + __builtin_amdgcn_exp2f(t));
        }
      }
      const int kk = u >> 1, sub = 2 * (u & 1);
      char* dst = ws + WS_H + ((size_t)((rt * NKK_H + kk) * 16 + m * 4 + sub) << 10) + lane * 16;
      u32x4 v0 = {pk_bf16(o[0], o[1]), pk_bf16(o[2], o[3]), pk_bf16(o[4], o[5]), pk_bf16(o[6], o[7])};
      u32x4 v1 = {pk_bf16(o[8], o[9]), pk_bf16(o[10], o[11]), pk_bf16(o[12], o[13]), pk_bf16(o[14], o[15])};
      *(u32x4*)dst = v0; *(u32x4*)(dst + 1024) = v1;
      if (u == 42) { u32x4 z = {0u, 0u, 0u, 0u}; *(u32x4*)(dst + 2048) = z; *(u32x4*)(dst + 3072) = z; }
    }
  }
  ST(0, 5);
}

__global__ __launch_bounds__(256, 1) void k2(const u16* __restrict__ x, const u16* __restrict__ cre, const u16* __restrict__ cim,
                                             const u16* __restrict__ b2, char* __restrict__ ws, u16* __restrict__ out) {
  extern __shared__ __attribute__((aligned(16))) char lds[];
  const int tid = threadIdx.x, lane = tid & 63, wid = __builtin_amdgcn_readfirstlane(tid >> 6);
  const int b = blockIdx.x, xcd = b & 7, idx = b >> 3;
  const int c = xcd >> 1, rt = (xcd & 1) * 8 + (idx >> 2), j = idx & 3;
  const int r31 = lane & 31, hh = lane >> 5;
  const int wm = wid >> 1, wn = wid & 1;
  const int kk0 = c == 0 ? 0 : (c == 1 ? 22 : (c == 2 ? 43 : 64));
  const int nk = (c == 0 || c == 3) ? 22 : 21;

  ST(1, 0);
  int aoff[4];
  if (c < 3) {
#pragma unroll
    for (int i = 0; i < 4; ++i) { int row = wid * 32 + i * 8 + (lane >> 3); aoff[i] = row * 4096 + (((lane & 7) ^ ((row >> 1) & 7)) * 16) - i * 1024 + 4096; }
  } else {
#pragma unroll
    for (int i = 0; i < 4; ++i) aoff[i] = wid * 4096 + lane * 16 + 4096;
  }
  const int boff = wid * 4096 + lane * 16;
  const char* a_re = (c < 3 ? (const char*)cre + (size_t)rt * 128 * 4096 : ws + WS_H + ((size_t)(rt * NKK_H) << 14)) - 4096;
  const char* a_im = (const char*)cim + (size_t)rt * 128 * 4096 - 4096;
  const char* b_base = ws + WS_B + ((size_t)(j * NKK_B + kk0) << 14);
  const unsigned ldsw = (unsigned)(size_t)(LAS char*)lds + wid * 4096;
#define ROTM 0
  const int rot = ((rt & 7) * ROTM) % 21;
  auto dma_setup = [&](int t, __amdgpu_buffer_rsrc_t& ra, __amdgpu_buffer_rsrc_t& rb, int& sa, int& sb, unsigned& dst) {
    const bool live = t < nk;
    int tr = t + rot; if (tr >= nk) tr -= nk;
    const int kk = kk0 + tr;
    const bool im = (c < 3) && kk >= 32;
    ra = __builtin_amdgcn_make_buffer_rsrc((void*)(im ? a_im : a_re), 0, live ? 0x7fffffff : 0, 0x00020000);
    rb = __builtin_amdgcn_make_buffer_rsrc((void*)b_base, 0, live ? 0x7fffffff : 0, 0x00020000);
    sa = c < 3 ? (kk & 31) * 128 : tr << 14;
    sb = tr << 14;
    dst = ldsw + (t & 3) * 32768;
  };
#define DMA_A(i) __builtin_amdgcn_raw_ptr_buffer_load_lds(ra, (LAS void*)(size_t)dst, 16, aoff[i], sa, (i) * 1024, 0)
#define DMA_B(i) __builtin_amdgcn_raw_ptr_buffer_load_lds(rb, (LAS void*)(size_t)(dst + 16384), 16, boff, sb, (i) * 1024, 0)
  unsigned ab[4];
#pragma unroll
  for (int s = 0; s < 4; ++s)
    ab[s] = wm * 8192 + (c < 3 ? (unsigned)(r31 * 128 + (((2 * s + hh) ^ ((r31 >> 1) & 7)) * 16)) : (unsigned)(s * 1024 + lane * 16));
  const unsigned bb = 16384 + wn * 8192 + lane * 16;

  f32x16 acc[2][2] = {};
  bf16x8 F0[16], F1[16];
#define SB() __builtin_amdgcn_sched_barrier(0)
#define RD(F, s) do { F[4 * (s) + 0] = *(const bf16x8*)(sl + ab[s]); F[4 * (s) + 1] = *(const bf16x8*)(sl + ab[s] + 4096); \
                      F[4 * (s) + 2] = *(const bf16x8*)(sl + bb + (s) * 1024); F[4 * (s) + 3] = *(const bf16x8*)(sl + bb + (s) * 1024 + 4096); } while (0)
#define MM0(F, s) acc[0][0] = __builtin_amdgcn_mfma_f32_32x32x16_bf16(F[4 * (s) + 2], F[4 * (s) + 0], acc[0][0], 0, 0, 0)
#define MM1(F, s) acc[0][1] = __builtin_amdgcn_mfma_f32_32x32x16_bf16(F[4 * (s) + 3], F[4 * (s) + 0], acc[0][1], 0, 0, 0)
#define MM2(F, s) acc[1][0] = __builtin_amdgcn_mfma_f32_32x32x16_bf16(F[4 * (s) + 2], F[4 * (s) + 1], acc[1][0], 0, 0, 0)
#define MM3(F, s) acc[1][1] = __builtin_amdgcn_mfma_f32_32x32x16_bf16(F[4 * (s) + 3], F[4 * (s) + 1], acc[1][1], 0, 0, 0)
#define PERIOD(u, FR, FM) do { \
    __amdgpu_buffer_rsrc_t ra, rb; int sa, sb; unsigned dst; dma_setup((u) + 3, ra, rb, sa, sb, dst); \
    const char* sl = lds + ((u) & 3) * 32768; \
    WAIT_V(16); BAR(); SB(); \
    RD(FR, 0); SB(); MM0(FM, 0); SB(); DMA_A(0); SB(); MM1(FM, 0); SB(); MM2(FM, 0); SB(); DMA_A(1); SB(); MM3(FM, 0); SB(); \
    RD(FR, 1); SB(); MM0(FM, 1); SB(); DMA_A(2); SB(); MM1(FM, 1); SB(); MM2(FM, 1); SB(); DMA_A(3); SB(); MM3(FM, 1); SB(); \
    RD(FR, 2); SB(); MM0(FM, 2); SB(); DMA_B(0); SB(); MM1(FM, 2); SB(); MM2(FM, 2); SB(); DMA_B(1); SB(); MM3(FM, 2); SB(); \
    RD(FR, 3); SB(); MM0(FM, 3); SB(); DMA_B(2); SB(); MM1(FM, 3); SB(); MM2(FM, 3); SB(); DMA_B(3); SB(); MM3(FM, 3); SB(); \
    WAIT_L0(); SB(); \
  } while (0)
#define MMALL(FM) do { MM0(FM, 0); MM1(FM, 0); MM2(FM, 0); MM3(FM, 0); MM0(FM, 1); MM1(FM, 1); MM2(FM, 1); MM3(FM, 1); \
                       MM0(FM, 2); MM1(FM, 2); MM2(FM, 2); MM3(FM, 2); MM0(FM, 3); MM1(FM, 3); MM2(FM, 3); MM3(FM, 3); } while (0)
#pragma unroll
  for (int t = 0; t < 3; ++t) {
    __amdgpu_buffer_rsrc_t ra, rb; int sa, sb; unsigned dst; dma_setup(t, ra, rb, sa, sb, dst);
    DMA_A(0); DMA_A(1); DMA_A(2); DMA_A(3); DMA_B(0); DMA_B(1); DMA_B(2); DMA_B(3);
  }
  {
    __amdgpu_buffer_rsrc_t ra, rb; int sa, sb; unsigned dst; dma_setup(3, ra, rb, sa, sb, dst);
    const char* sl = lds;
    WAIT_V(16); BAR(); SB(); ST(1, 1);
    RD(F0, 0); RD(F0, 1); RD(F0, 2); RD(F0, 3); SB();
    DMA_A(0); DMA_A(1); DMA_A(2); DMA_A(3); DMA_B(0); DMA_B(1); DMA_B(2); DMA_B(3); SB();
    WAIT_L0(); SB();
  }
  for (int u = 1; u < 21; u += 2) {
    PERIOD(u, F1, F0);
    PERIOD(u + 1, F0, F1);
  }
  if (nk == 22) {
    PERIOD(21, F1, F0);
    MMALL(F1);
  } else {
    MMALL(F0);
  }

  ST(1, 2);
  const int tile = rt * 4 + j;
  const auto rs = __builtin_amdgcn_make_buffer_rsrc(ws + WS_SLAB + ((size_t)tile << 18), 0, 1 << 18, 0x00020000);
  const unsigned lo = wid * 4096 + lane * 16;
#pragma unroll
  for (int q = 0; q < 4; ++q) {
    if (q != c) {
      const f32x16 v = acc[q >> 1][q & 1];
#pragma unroll
      for (int r4 = 0; r4 < 4; ++r4) {
        f32x4 w = {v[r4 * 4], v[r4 * 4 + 1], v[r4 * 4 + 2], v[r4 * 4 + 3]};
        __builtin_amdgcn_raw_buffer_store_b128(__builtin_bit_cast(u32x4, w), rs, (q * 4 + c) * 16384 + lo + r4 * 1024, 0, 16);
      }
    }
  }
  WAIT_V(0);
  ST(1, 3);
  __syncthreads();
  unsigned* cnt = (unsigned*)(ws + WS_CNT + tile * 256);
  if (tid == 0) {
    __hip_atomic_fetch_add(cnt, 1u, __ATOMIC_RELAXED, __HIP_MEMORY_SCOPE_AGENT);
    unsigned spins = 0;
    while (__hip_atomic_load(cnt, __ATOMIC_RELAXED, __HIP_MEMORY_SCOPE_AGENT) < 4u) { __builtin_amdgcn_s_sleep(1); if (++spins > (1u << 22)) break; }
  }
  __syncthreads();
  ST(1, 4);
  f32x16 own = c == 0 ? acc[0][0] : (c == 1 ? acc[0][1] : (c == 2 ? acc[1][0] : acc[1][1]));
  f32x4 pv[4][4];
#pragma unroll
  for (int s = 0; s < 4; ++s)
#pragma unroll
    for (int r4 = 0; r4 < 4; ++r4)
      pv[s][r4] = __builtin_bit_cast(f32x4, __builtin_amdgcn_raw_buffer_load_b128(rs, (c * 4 + s) * 16384 + lo + r4 * 1024, 0, 16));
  const int trow = rt * 128 + wm * 64 + (c >> 1) * 32 + r31;
  const int ecol = j * 128 + wn * 64 + (c & 1) * 32 + 8 * hh;
#pragma unroll
  for (int half = 0; half < 2; ++half) {
    const u32x4 xv = *(const u32x4*)(x + (size_t)trow * D_ + ecol + 16 * half);
    const u32x4 bv = *(const u32x4*)(b2 + ecol + 16 * half);
    float o[8];
#pragma unroll
    for (int e = 0; e < 8; ++e) {
      const int rg = half * 8 + e;
      const float p0 = c == 0 ? own[rg] : pv[0][rg >> 2][rg & 3];
      const float p1 = c == 1 ? own[rg] : pv[1][rg >> 2][rg & 3];
      const float p2 = c == 2 ? own[rg] : pv[2][rg >> 2][rg & 3];
      const float p3 = c == 3 ? own[rg] : pv[3][rg >> 2][rg & 3];
      const float s = (p0 + p1) + p2;
      const unsigned xw = xv[e >> 1], bw = bv[e >> 1];
      const float xx = (e & 1) ? bfhi(xw) : bflo(xw), bb2 = (e & 1) ? bfhi(bw) : bflo(bw);
      const float v = p3 + bb2;
      const float g = v * __builtin_amdgcn_rcpf(1.f + __builtin_amdgcn_exp2f(-1.442695041f * v));
      o[e] = xx + s * g;
    }
    u32x4 ov = {pk_bf16(o[0], o[1]), pk_bf16(o[2], o[3]), pk_bf16(o[4], o[5]), pk_bf16(o[6], o[7])};
    *(u32x4*)(out + (size_t)trow * D_ + ecol + 16 * half) = ov;
  }
  ST(1, 5);
}

extern "C" void kernel_launch(void* const* d_in, const int* in_sizes, int n_in, void* d_out, int out_size, void* d_ws, size_t ws_size,
                              hipStream_t stream) {
  const u16* x = (const u16*)d_in[0]; const u16* cre = (const u16*)d_in[1]; const u16* cim = (const u16*)d_in[2];
  const u16* wre = (const u16*)d_in[3]; const u16* wim = (const u16*)d_in[4];
  const u16* w1 = (const u16*)d_in[6]; const u16* b1 = (const u16*)d_in[7]; const u16* w2 = (const u16*)d_in[8]; const u16* b2 = (const u16*)d_in[9];
  (void)hipFuncSetAttribute((const void*)k1, hipFuncAttributeMaxDynamicSharedMemorySize, 163840);
  (void)hipFuncSetAttribute((const void*)k2, hipFuncAttributeMaxDynamicSharedMemorySize, 131072);
  k1<<<256, 512, 163840, stream>>>(x, wre, wim, w1, b1, w2, (char*)d_ws);
  k2<<<256, 256, 131072, stream>>>(x, cre, cim, b2, (char*)d_ws, (u16*)d_out);
}
```

```cpp
#include <hip/hip_runtime.h>
#include <hip/hip_bf16.h>
#include <stdint.h>

typedef unsigned short u16;
typedef short v4s __attribute__((ext_vector_type(4)));
typedef short bf16x8 __attribute__((ext_vector_type(8)));
typedef __bf16 v2bf __attribute__((ext_vector_type(2)));
typedef float f32x4 __attribute__((ext_vector_type(4)));
typedef float f32x16 __attribute__((ext_vector_type(16)));
typedef unsigned u32x4 __attribute__((ext_vector_type(4)));
typedef unsigned u32x2 __attribute__((ext_vector_type(2)));
#define LAS __attribute__((address_space(3)))
#define GAS __attribute__((address_space(1)))

constexpr int S_ = 2048, D_ = 512, F_ = 1376;
constexpr int NKK_B = 64;
constexpr int NSUB = 88;
constexpr size_t WS_B    = 0;
constexpr size_t WS_W2   = 4u << 20;
constexpr size_t WS_H    = 8u << 20;
constexpr size_t WS_SLAB = 16u << 20;
constexpr size_t WS_CNT  = 40u << 20;

__device__ __forceinline__ float bf2f(u16 v) { return __uint_as_float(((unsigned)v) << 16); }
__device__ __forceinline__ float bflo(unsigned v) { return __uint_as_float(v << 16); }
__device__ __forceinline__ float bfhi(unsigned v) { return __uint_as_float(v & 0xffff0000u); }
__device__ __forceinline__ unsigned pk_bf16(float lo, float hi) {
  typedef float f32x2_ __attribute__((ext_vector_type(2)));
  v2bf r = __builtin_convertvector((f32x2_){lo, hi}, v2bf);
  return __builtin_bit_cast(unsigned, r);
}
__device__ __forceinline__ float rbf(float f) { return bflo(pk_bf16(f, 0.f)); }
__device__ __forceinline__ int swap23(int r) { return (r & ~12) | ((r & 4) << 1) | ((r & 8) >> 1); }
__device__ __forceinline__ float dot2(unsigned a, unsigned b, float c) {
  return __builtin_amdgcn_fdot2_f32_bf16(__builtin_bit_cast(v2bf, a), __builtin_bit_cast(v2bf, b), c, false);
}
#define GLDS16(g, l) __builtin_amdgcn_global_load_lds((const GAS void*)(g), (LAS void*)(l), 16, 0, 0)
typedef int i32x4 __attribute__((ext_vector_type(4)));
__device__ __forceinline__ i32x4 mk_rsrc(const void* p) {
  const unsigned long long a = (unsigned long long)p;
  return (i32x4){(int)(unsigned)a, (int)((unsigned)(a >> 32) & 0xffffu), 0x7fffffff, 0x00020000};
}
__device__ __forceinline__ void dma16(i32x4 rsrc, unsigned lds_dst, int voff, int soff) {
  unsigned keep;
  asm volatile("s_mov_b32 %0, m0\n\ts_mov_b32 m0, %1\n\ts_nop 0\n\tbuffer_load_dwordx4 %2, %3, %4 offen lds\n\ts_mov_b32 m0, %0"
               : "=&s"(keep) : "s"(lds_dst), "v"(voff), "s"(rsrc), "s"(soff) : "memory");
}
__device__ __forceinline__ void touch4_nt(i32x4 rsrc, unsigned lds_dst, int voff, int soff) {
  unsigned keep;
  asm volatile("s_mov_b32 %0, m0\n\ts_mov_b32 m0, %1\n\ts_nop 0\n\tbuffer_load_dword %2, %3, %4 offen nt lds\n\ts_mov_b32 m0, %0"
               : "=&s"(keep) : "s"(lds_dst), "v"(voff), "s"(rsrc), "s"(soff) : "memory");
}
#define WAIT_V(n) asm volatile("s_waitcnt vmcnt(%0)" ::"n"(n) : "memory")
#define WAIT_L0() asm volatile("s_waitcnt lgkmcnt(0)" ::: "memory")
#define BAR() __builtin_amdgcn_s_barrier()
#define ST_DECL
#define ST(k, i)
#define ST_FLUSH(k)

#define AUX_PF 2
#define PF_ON 1
#define PF_AT 1
#define AUX_ST 16
#define TOUCH 2
typedef float f32x2 __attribute__((ext_vector_type(2)));
constexpr int SL1 = 0, SL2 = 33280, SL3 = 98304, SL4 = 0, SLP = 131072, AROW = 1040;
constexpr int SCR = 66560;
constexpr int SC_PART = SCR;
constexpr int SC_HS   = SCR + 2048;
constexpr int SC_RS   = SCR + 3072;
constexpr int SC_MRS  = SCR + 3584;
constexpr int SC_CS   = SCR + 4096;
constexpr int SC_B1   = SCR + 7168;
constexpr int SC_DUM  = SCR + 8192;
#define PIN(x) asm volatile("" : "+v"(x))
#define SBAR() __builtin_amdgcn_sched_barrier(0)

template <int K> __device__ __forceinline__ void eh_op(const f32x16& ap, float (&v)[16], float (&t)[16], unsigned (&ow)[8], float scale, float center) {
  constexpr int st = K / 16, e = K % 16;
  if constexpr (st == 0) { v[e] = fmaf(-center, t[e], v[e]); PIN(v[e]); }
  else if constexpr (st == 1) { v[e] = fmaf(ap[e], scale, v[e]); PIN(v[e]); }
  else if constexpr (st == 2) { t[e] = v[e] * v[e]; PIN(t[e]); }
  else if constexpr (st == 3) { t[e] = fmaf(t[e], -0.1029432397f, -2.302208198f); PIN(t[e]); }
  else if constexpr (st == 4) { t[e] = t[e] * v[e]; PIN(t[e]); }
  else if constexpr (st == 5) { t[e] = __builtin_amdgcn_exp2f(t[e]); PIN(t[e]); }
  else if constexpr (st == 6) { t[e] = 1.f + t[e]; PIN(t[e]); }
  else if constexpr (st == 7) { t[e] = __builtin_amdgcn_rcpf(t[e]); PIN(t[e]); }
  else if constexpr (st == 8) { v[e] = v[e] * t[e]; PIN(v[e]); }
  else if constexpr (K < 152) { ow[e] = pk_bf16(v[2 * e], v[2 * e + 1]); PIN(ow[e]); }
}
template <int K> __device__ __forceinline__ void ea_op(const f32x16& ap, float (&v)[16], float (&t)[16], unsigned (&ow)[8], float cs) {
  constexpr int st = K / 16, e = K % 16;
  if constexpr (st == 0) { t[e] = ap[e] * t[e]; PIN(t[e]); }
  else if constexpr (st == 1) { t[e] = fmaf(-v[e], cs, t[e]); PIN(t[e]); }
  else if constexpr (K < 40) { ow[e] = pk_bf16(t[2 * e], t[2 * e + 1]); PIN(ow[e]); }
}
template <int ET, int K0, int K1> __device__ __forceinline__ void e_ops(const f32x16& ap, float (&v)[16], float (&t)[16], unsigned (&ow)[8], float s0, float s1) {
  if constexpr (K0 < K1) {
    if constexpr (ET == 1) eh_op<K0>(ap, v, t, ow, s0, s1); else if constexpr (ET == 2) ea_op<K0>(ap, v, t, ow, s0);
    e_ops<ET, K0 + 1, K1>(ap, v, t, ow, s0, s1);
  }
}
template <int MT> __device__ __forceinline__ bf16x8 wread(const char* pan, int ks) {
  if constexpr (MT == 1) { return *(const bf16x8*)(pan + ks * 32); }
  else {
    v4s t0 = __builtin_amdgcn_ds_read_tr16_b64_v4i16((LAS v4s*)(pan + ks * 1024));
    v4s t1 = __builtin_amdgcn_ds_read_tr16_b64_v4i16((LAS v4s*)(pan + ks * 1024 + 256));
    return (bf16x8)__builtin_shufflevector(t0, t1, 0, 1, 2, 3, 4, 5, 6, 7);
  }
}
__device__ __forceinline__ void row_stats(float sum, float sq, float& mean, float& inv, float& m_bf, float& scale, float& center) {
  mean = sum * (1.f / 512.f);
  const float var = fmaxf(sq * (1.f / 512.f) - mean * mean, 0.f);
  m_bf = rbf(mean);
  const float sd = rbf(sqrtf(rbf(rbf(var) + 1e-5f)));
  inv = 1.f / sd;
  const float m2 = rbf((mean - m_bf) * inv);
  const float v2 = rbf(var * inv * inv);
  const float sd2 = rbf(sqrtf(rbf(v2 + 1e-5f)));
  scale = inv / sd2; center = (m_bf + m2 * sd) * scale;
}
constexpr int WR = 6;
template <int MT, int ET, int KS> __device__ __forceinline__ void unit_steps(const char* pan, const bf16x8 (&xf)[32], bf16x8 (&wf)[WR], f32x16& acc,
                                                                          const f32x16& ap, float (&v)[16], float (&t)[16], unsigned (&ow)[8], float s0, float s1) {
  if constexpr (KS < 32) {
    constexpr int NOPS = ET == 1 ? 152 : (ET == 2 ? 40 : 0);
    constexpr int G0 = 2;
    if constexpr (MT == 1) acc = __builtin_amdgcn_mfma_f32_32x32x16_bf16(xf[KS], wf[KS % WR], acc, 0, 0, 0);
    else acc = __builtin_amdgcn_mfma_f32_32x32x16_bf16(wf[KS % WR], xf[KS], acc, 0, 0, 0);
    PIN(acc);
    SBAR();
    if constexpr (KS + WR - 1 < 32) wf[(KS + WR - 1) % WR] = wread<MT>(pan, KS + WR - 1);
    SBAR();
    constexpr int k0 = KS < G0 ? 0 : (KS - G0) * NOPS / (32 - G0), k1 = KS < G0 ? 0 : (KS + 1 - G0) * NOPS / (32 - G0);
    e_ops<ET, k0, k1>(ap, v, t, ow, s0, s1);
    SBAR();
    unit_steps<MT, ET, KS + 1>(pan, xf, wf, acc, ap, v, t, ow, s0, s1);
  }
}

__global__ __launch_bounds__(512, 2) void k1(const u16* __restrict__ x, const u16* __restrict__ wre, const u16* __restrict__ wim,
                                             const u16* __restrict__ w1, const u16* __restrict__ b1, const u16* __restrict__ w2,
                                             const u16* __restrict__ cre, const u16* __restrict__ cim, char* __restrict__ ws) {
  extern __shared__ __attribute__((aligned(16))) char lds[];
  const int tid = threadIdx.x, lane = tid & 63, wid = __builtin_amdgcn_readfirstlane(tid >> 6);
  const int b = blockIdx.x, xcd = b & 7, idx = b >> 3;
  const int rt = (xcd >> 1) * 4 + (idx & 3);
  const int cg = (xcd & 1) * 8 + (idx >> 2);
  const int h0 = cg < 11 ? cg * 3 : 33 + (cg - 11) * 2;
  const bool h3 = cg < 11;
  const int a0 = cg * 2;
  const int m = wid & 3;
  const bool helper = wid >= 4;
  const int r31 = lane & 31, hh = lane >> 5;
  const unsigned lds0 = (unsigned)(size_t)(LAS char*)lds;
  const int prot = idx & 3;
  ST_DECL;
  ST(0, 0);

  const int hoff = (lane >> 2) * (F_ * 2) + (lane & 3) * 16;
  const i32x4 rw1 = mk_rsrc(w1);
  auto piece_h = [&](int u, int q, int slot_off) { dma16(rw1, lds0 + slot_off + q * 1024, hoff, q * 16 * (F_ * 2) + u * 64); };
  const i32x4 rx = mk_rsrc(x + (size_t)(rt * 128 + m * 32) * D_);
  int xo[4];
#pragma unroll
  for (int i = 0; i < 4; ++i) { const int r = i * 4 + (lane >> 4); xo[i] = r * 1024 + (((lane & 15) ^ r) * 16); }
  auto quarter = [&](int q) {
#pragma unroll
    for (int i = 0; i < 8; ++i) dma16(rx, lds0 + q * 32768 + m * 8192 + i * 1024, xo[i & 3], q * 256 + (i >> 2) * 16384);
    piece_h(h0, q * 8 + m * 2, SLP); piece_h(h0, q * 8 + m * 2 + 1, SLP);
  };
  if (helper) { quarter(0); quarter(1); }

  unsigned w2v[8], b1v;
  const int w2chunk = b * 352 + tid;
  {
    i32x4 rw2 = mk_rsrc(w2); rw2.z = F_ * D_ * 2;
    const int c = w2chunk;
    const int ln = c & 63, rest = c >> 6;
    const int ct = rest / NSUB, sub = rest - ct * NSUB;
    const int d = 32 * ct + swap23(ln & 31);
    const int fb = 16 * sub + 4 * (ln >> 5);
    const int off = tid < 352 ? (fb * D_ + d) * 2 : 0x7ff00000;
    const int bo = h0 * 32 + lane * 2;
    const i32x4 rb1 = mk_rsrc(b1);
    asm volatile("buffer_load_ushort %0, %9, %11, 0 offen\n\tbuffer_load_ushort %1, %9, %11, 0 offen offset:1024\n\t"
                 "buffer_load_ushort %2, %9, %11, 0 offen offset:2048\n\tbuffer_load_ushort %3, %9, %11, 0 offen offset:3072\n\t"
                 "buffer_load_ushort %4, %10, %11, 0 offen\n\tbuffer_load_ushort %5, %10, %11, 0 offen offset:1024\n\t"
                 "buffer_load_ushort %6, %10, %11, 0 offen offset:2048\n\tbuffer_load_ushort %7, %10, %11, 0 offen offset:3072\n\t"
                 "buffer_load_dword %8, %12, %13, 0 offen"
                 : "=&v"(w2v[0]), "=&v"(w2v[1]), "=&v"(w2v[2]), "=&v"(w2v[3]), "=&v"(w2v[4]), "=&v"(w2v[5]), "=&v"(w2v[6]), "=&v"(w2v[7]), "=&v"(b1v)
                 : "v"(off), "v"(off + 8192), "s"(rw2), "v"((bo < F_ - 2 ? bo : F_ - 2) * 2), "s"(rb1) : "memory");
  }
#define REGS_LANDED() asm volatile("" : "+v"(w2v[0]), "+v"(w2v[1]), "+v"(w2v[2]), "+v"(w2v[3]), "+v"(w2v[4]), "+v"(w2v[5]), "+v"(w2v[6]), "+v"(w2v[7]), "+v"(b1v))
  const auto rws = __builtin_amdgcn_make_buffer_rsrc((void*)ws, 0, 0x7fffffff, 0x00020000);
  const int xrow = m * 32 + swap23(r31);
  const unsigned xbase = xrow * 256 + ((hh ^ (xrow & 15)) * 16);
#define QWAIT(q) WAIT_V(9 + 10 * (3 - (q)))

  if (helper) {
    const i32x4 rwa = mk_rsrc((a0 < 16 ? wre : wim) + (size_t)(a0 & 15) * 32 * D_);
    auto panel_h = [&](int u, int slot_off) {
#pragma unroll
      for (int k = 0; k < 8; ++k) piece_h(u, m * 8 + ((k + 2 * prot) & 7), slot_off);
    };
    auto panel_a = [&](int w, int slot_off) {
#pragma unroll
      for (int k = 0; k < 8; ++k) { const int q = m * 8 + ((k + 2 * prot) & 7); dma16(rwa, lds0 + slot_off + q * AROW, lane * 16, (w * 32 + q) * 1024); }
    };
    const v4s ones = {0x3f80, 0x3f80, 0x3f80, 0x3f80};
    const char* pzh = lds + ((lane >> 2) & 15) * 64 + (m & 1) * 32 + (lane & 3) * 8;
    auto cs_h_part = [&](f32x4 (&c)[4], int slot_off, int j0, int n) {
#pragma unroll
      for (int j = 0; j < n; ++j)
        c[j & 3] = __builtin_amdgcn_mfma_f32_4x4x4bf16_1k(ones, __builtin_amdgcn_ds_read_tr16_b64_v4i16((LAS v4s*)(pzh + slot_off + (j0 + j) * 1024)), c[j & 3], 0, 0, 0);
    };
    auto cs_h_fin = [&](f32x4 (&c)[4], int i) {
      float r = (c[0][0] + c[1][0]) + (c[2][0] + c[3][0]);
      r += __shfl_xor(r, 16); r += __shfl_xor(r, 32);
      if (lane < 16) *(float*)(lds + SC_CS + i * 512 + (m & 1) * 64 + lane * 4) = r;
    };
    auto cs_h = [&](int i, int slot_off) {
      if ((m >> 1) == (i & 1)) { f32x4 c[4] = {}; cs_h_part(c, slot_off, 0, 32); cs_h_fin(c, i); }
    };
    auto cs_a = [&](int i, int slot_off) {
      const char* pz = lds + slot_off + swap23(r31) * AROW + hh * 16 + m * 256;
      f32x4 c0 = {}, c1 = {};
#pragma unroll
      for (int j = 0; j < 8; ++j) {
        const bf16x8 w = *(const bf16x8*)(pz + j * 32);
        c0 = __builtin_amdgcn_mfma_f32_4x4x4bf16_1k(ones, __builtin_shufflevector(w, w, 0, 1, 2, 3), c0, 0, 0, 0);
        c1 = __builtin_amdgcn_mfma_f32_4x4x4bf16_1k(ones, __builtin_shufflevector(w, w, 4, 5, 6, 7), c1, 0, 0, 0);
      }
      float r = c0[0] + c1[0]; r += __shfl_xor(r, 32);
      if (lane < 32) *(float*)(lds + SC_CS + i * 512 + m * 128 + lane * 4) = r;
    };
    __builtin_amdgcn_s_setprio(2);
    {
      f32x4 c[4] = {};
      WAIT_V(19); ST(0, 1); BAR();
      quarter(2);
      if (m < 2) cs_h_part(c, SLP, 0, 8);
      WAIT_V(10); BAR();
      quarter(3);
      panel_h(h0 + 1, SL1);
      if (m < 2) cs_h_part(c, SLP, 8, 8);
      WAIT_V(18); BAR();
      if (m < 2) cs_h_part(c, SLP, 16, 8);
      WAIT_V(8); BAR();
      ST(0, 2);
      panel_a(0, SL2);
      if (m < 2) { cs_h_part(c, SLP, 24, 8); cs_h_fin(c, 0); }
    }
    REGS_LANDED();
    if (m == 3 && lane < 48) *(f32x2*)(lds + SC_B1 + lane * 8) = (f32x2){bflo(b1v), bfhi(b1v)};
    if (tid < 352) {
      u32x4 v = {w2v[0] | (w2v[1] << 16), w2v[2] | (w2v[3] << 16), w2v[4] | (w2v[5] << 16), w2v[6] | (w2v[7] << 16)};
      __builtin_amdgcn_raw_buffer_store_b128(v, rws, w2chunk * 16, (int)WS_W2, AUX_ST);
    } else { __builtin_amdgcn_raw_buffer_store_b128((u32x4){0u, 0u, 0u, 0u}, rws, 0x7ffffff0, 0x7ffffff0, AUX_ST); }
    ST(0, 3);
    WAIT_V(9); WAIT_L0(); BAR();
    if (h3) panel_h(h0 + 2, SL3); else panel_a(1, SL3);
    cs_h(1, SL1);
    {
      float mean, inv, m_bf, sc_, ce_;
      const f32x2 pr = *(const f32x2*)(lds + SC_PART + (m * 32 + r31) * 8);
      row_stats(pr.x, pr.y, mean, inv, m_bf, sc_, ce_);
      const float sg = a0 < 16 ? inv : -inv;
      if (lane < 32) { *(float*)(lds + SC_RS + (m * 32 + lane) * 4) = sg; *(float*)(lds + SC_MRS + (m * 32 + lane) * 4) = m_bf * sg; }
    }
    WAIT_V(8); WAIT_L0(); BAR();
    if (h3) panel_a(1, SL4);
    cs_a(2, SL2);
    if (h3) WAIT_V(8); else WAIT_V(0);
    WAIT_L0(); BAR();
    {
      const int c2 = xcd >> 1;
      const int row = (xcd & 1) * 1024 + idx * 32 + m * 8 + (lane >> 4);
      const i32x4 rc = mk_rsrc(c2 < 2 ? cre : cim);
      const int vo = row * 4096 + (lane & 15) * 128;
      touch4_nt(rc, lds0 + SC_DUM + m * 256, vo, (c2 & 1) * 2048);
      touch4_nt(rc, lds0 + SC_DUM + m * 256, vo, (c2 & 1) * 2048 + 4 * 4096);
    }
    if (h3) {
      cs_h(3, SL3);
      WAIT_V(PF_ON ? 2 : 0); WAIT_L0(); BAR();
      cs_a(4, SL4);
    } else cs_a(3, SL3);
    WAIT_L0(); BAR();
    WAIT_V(0);
    ST(0, 5); ST_FLUSH(0);
    return;
  }

  bf16x8 xf[32];
  unsigned long long clk0 = 0; (void)clk0;
  const int wrow = swap23(r31);
  const unsigned wlA = (unsigned)(wrow * AROW + hh * 16);
  const unsigned wlH = (unsigned)((8 * hh + ((lane & 15) >> 2)) * 64 + ((lane >> 4) & 1) * 32 + (lane & 3) * 8);
  f32x16 acc, ap = {};
  bf16x8 wf[WR];
  float v[16], t[16]; unsigned ow[8];
  float scale = 0.f, center = 0.f;
  const int hstore = (swap23(r31) + 32 * hh) * 16;
  const int s0r = rt * 128 + m * 32;

  auto tables_h = [&](int i, int hu) {
#pragma unroll
    for (int g4 = 0; g4 < 4; ++g4) {
      const f32x4 c = *(const f32x4*)(lds + SC_CS + i * 512 + (8 * g4 + 4 * hh) * 4), bq = *(const f32x4*)(lds + SC_B1 + hu * 128 + (8 * g4 + 4 * hh) * 4);
#pragma unroll
      for (int e = 0; e < 4; ++e) { t[g4 * 4 + e] = c[e]; v[g4 * 4 + e] = bq[e]; }
    }
  };
  auto tables_a = [&](int i) -> float {
#pragma unroll
    for (int g4 = 0; g4 < 4; ++g4) {
      const f32x4 c = *(const f32x4*)(lds + SC_RS + (m * 32 + 8 * g4 + 4 * hh) * 4), bq = *(const f32x4*)(lds + SC_MRS + (m * 32 + 8 * g4 + 4 * hh) * 4);
#pragma unroll
      for (int e = 0; e < 4; ++e) { t[g4 * 4 + e] = c[e]; v[g4 * 4 + e] = bq[e]; }
    }
    const char* cq = lds + SC_CS + i * 512 + r31 * 4;
    return (*(const float*)cq + *(const float*)(cq + 128)) + (*(const float*)(cq + 256) + *(const float*)(cq + 384));
  };
  auto store_h = [&](int hu) {
    const int dst = (int)WS_H + (((rt * 4 + m) * NSUB + 2 * (h0 + hu)) << 10);
    __builtin_amdgcn_raw_buffer_store_b128((u32x4){ow[0], ow[1], ow[2], ow[3]}, rws, hstore, dst, AUX_ST);
    __builtin_amdgcn_raw_buffer_store_b128((u32x4){ow[4], ow[5], ow[6], ow[7]}, rws, hstore + 1024, dst, AUX_ST);
    if (h0 + hu == 42) { u32x4 z = {0u, 0u, 0u, 0u}; __builtin_amdgcn_raw_buffer_store_b128(z, rws, lane * 16 + 2048, dst, AUX_ST); __builtin_amdgcn_raw_buffer_store_b128(z, rws, lane * 16 + 3072, dst, AUX_ST); }
  };
  auto store_a = [&](int w) {
    const int u = a0 + w, et = u & 15, j = et >> 2, nt = et & 3;
    const int kk = (u >> 4) * 32 + (s0r >> 6), sub = (s0r & 63) >> 4;
    const int dst = (int)WS_B + (((j * NKK_B + kk) * 16 + nt * 4 + sub) << 10);
    __builtin_amdgcn_raw_buffer_store_b128((u32x4){ow[0], ow[1], ow[2], ow[3]}, rws, lane * 16, dst, AUX_ST);
    __builtin_amdgcn_raw_buffer_store_b128((u32x4){ow[4], ow[5], ow[6], ow[7]}, rws, lane * 16 + 1024, dst, AUX_ST);
  };
#define UNIT(MT, ET, slot, s0, s1) do { const char* pan = lds + (slot) + (MT ? wlA : wlH); acc = (f32x16){}; \
    _Pragma("unroll") for (int e = 0; e < WR - 1; ++e) wf[e] = wread<MT>(pan, e); \
    unit_steps<MT, ET, 0>(pan, xf, wf, acc, ap, v, t, ow, s0, s1); } while (0)
  {
    float ss0 = 0.f, ss1 = 0.f, sq0 = 0.f, sq1 = 0.f;
    acc = (f32x16){};
    const char* pan = lds + SLP + wlH;
#pragma unroll
    for (int q = 0; q < 4; ++q) {
      if (q == 0) ST(0, 1);
      BAR();
      if (q == 3) ST(0, 2);
      bf16x8 w8[8];
#pragma unroll
      for (int k = 0; k < 8; ++k) xf[q * 8 + k] = *(const bf16x8*)(lds + q * 32768 + (xbase ^ (k * 32)));
#pragma unroll
      for (int k = 0; k < 8; ++k) w8[k] = wread<0>(pan, q * 8 + k);
      SBAR();
#pragma unroll
      for (int k = 0; k < 8; ++k) {
        const u32x4 v4 = __builtin_bit_cast(u32x4, xf[q * 8 + k]);
        ss0 = dot2(v4.x, 0x3f803f80u, ss0); sq0 = dot2(v4.x, v4.x, sq0); ss1 = dot2(v4.y, 0x3f803f80u, ss1); sq1 = dot2(v4.y, v4.y, sq1);
        ss0 = dot2(v4.z, 0x3f803f80u, ss0); sq0 = dot2(v4.z, v4.z, sq0); ss1 = dot2(v4.w, 0x3f803f80u, ss1); sq1 = dot2(v4.w, v4.w, sq1);
      }
      asm volatile("" : "+v"(ss0), "+v"(ss1), "+v"(sq0), "+v"(sq1));
      SBAR();
#pragma unroll
      for (int k = 0; k < 8; ++k) { acc = __builtin_amdgcn_mfma_f32_32x32x16_bf16(w8[k], xf[q * 8 + k], acc, 0, 0, 0); PIN(acc); }
      WAIT_L0(); SBAR();
    }
    float sum = ss0 + ss1, sq = sq0 + sq1;
    sum += __shfl_xor(sum, 32); sq += __shfl_xor(sq, 32);
    if (lane < 32) *(f32x2*)(lds + SC_PART + (m * 32 + lane) * 8) = (f32x2){sum, sq};
    float mean, inv, m_bf; row_stats(sum, sq, mean, inv, m_bf, scale, center);
    WAIT_V(0); REGS_LANDED();
    u32x4 v = {w2v[0] | (w2v[1] << 16), w2v[2] | (w2v[3] << 16), w2v[4] | (w2v[5] << 16), w2v[6] | (w2v[7] << 16)};
    __builtin_amdgcn_raw_buffer_store_b128(v, rws, w2chunk * 16, (int)WS_W2, AUX_ST);
    if (b == 0 && wid == 0) *(unsigned*)(ws + WS_CNT + lane * 256) = 0u;
  }
  ST(0, 8);
  WAIT_L0(); BAR(); ap = acc;
  tables_h(0, 0);
  UNIT(0, 1, SL1, scale, center); store_h(0);
  ST(0, 9);
  BAR(); ap = acc;
  tables_h(1, 1);
  UNIT(1, 1, SL2, scale, center); store_h(1);
  ST(0, 10);
  BAR(); ap = acc;
  float csa = tables_a(2);
  if (h3) {
    UNIT(0, 2, SL3, csa, 0.f); store_a(0);
    ST(0, 11);
    BAR(); ap = acc;
    tables_h(3, 2);
    UNIT(1, 1, SL4, scale, center); store_h(2);
  } else {
    UNIT(1, 2, SL3, csa, 0.f); store_a(0);
  }
  ST(0, 12);
  BAR(); ap = acc;
  csa = tables_a(h3 ? 4 : 3);
  e_ops<2, 0, 40>(ap, v, t, ow, csa, 0.f);
  store_a(1);
  WAIT_V(0);
  ST(0, 5); ST_FLUSH(0);
}

#define AUX_C 2
__global__ __launch_bounds__(512, 2) void k2(const u16* __restrict__ x, const u16* __restrict__ cre, const u16* __restrict__ cim,
                                             const u16* __restrict__ b2, char* __restrict__ ws, u16* __restrict__ out) {
  extern __shared__ __attribute__((aligned(16))) char lds[];
  const int tid = threadIdx.x, lane = tid & 63, wid = __builtin_amdgcn_readfirstlane(tid >> 6);
  const int b = blockIdx.x, xcd = b & 7, idx = b >> 3;
  const int c = xcd >> 1, rt = (xcd & 1) * 8 + (idx >> 2), j = idx & 3;
  ST_DECL;
  ST(1, 0);
#define SB() __builtin_amdgcn_sched_barrier(0)

  if (wid >= 4) {
    const int lw = wid - 4;
    int aoff[4];
#pragma unroll
    for (int i = 0; i < 4; ++i) { int row = lw * 32 + i * 8 + (lane >> 3); aoff[i] = row * 4096 + (((lane & 7) ^ ((row >> 1) & 7)) * 16) - i * 1024 + 4096; }
    const int boff = lw * 4096 + lane * 16;
    const unsigned ldsw = (unsigned)(size_t)(LAS char*)lds + lw * 4096;
    {
      const char* a_base = (const char*)(c < 2 ? cre : cim) + (size_t)rt * 128 * 4096 + (c & 1) * 2048 - 4096;
      const char* b_base = ws + WS_B + ((size_t)(j * NKK_B + c * 16) << 14);
#define ABL 0
      const auto ra = __builtin_amdgcn_make_buffer_rsrc((void*)a_base, 0, (ABL & 1) ? 0 : 0x7fffffff, 0x00020000);
      const auto rb = __builtin_amdgcn_make_buffer_rsrc((void*)b_base, 0, (ABL & 2) ? 0 : 0x7fffffff, 0x00020000);
      auto stage = [&](int t) {
        const int sa = t * 128, sb = t << 14;
        const unsigned dst = ldsw + (t & 3) * 32768;
#define DA(i) __builtin_amdgcn_raw_ptr_buffer_load_lds(ra, (LAS void*)(size_t)dst, 16, aoff[i], sa, (i) * 1024, AUX_C)
#define DB(i) __builtin_amdgcn_raw_ptr_buffer_load_lds(rb, (LAS void*)(size_t)(dst + 16384), 16, boff, sb, (i) * 1024, 0)
        DA(0); DA(1); DA(2); DA(3); DB(0); DB(1); DB(2); DB(3);
      };
      stage(0); stage(1); stage(2);
      for (int u = 0; u < 13; ++u) { WAIT_V(16); BAR(); stage(u + 3); }
    }
    {
      const char* a_base = ws + WS_H + ((size_t)((rt * 4 + 2 * (lw >> 1) + (c >> 1)) * NSUB) << 10) + (lw & 1) * 4096;
      const char* b_base = ws + WS_W2 + ((size_t)((j * 4 + 2 * (lw >> 1) + (c & 1)) * NSUB) << 10) + (lw & 1) * 4096;
      const int voff = lane * 16;
      for (int u = 13; u < 27; ++u) {
        WAIT_V(16); BAR();
        const int t = u + 3 - 16;
        const bool live = t < 11;
        const auto ra = __builtin_amdgcn_make_buffer_rsrc((void*)a_base, 0, (live && !(ABL & 4)) ? 0x7fffffff : 0, 0x00020000);
        const auto rb = __builtin_amdgcn_make_buffer_rsrc((void*)b_base, 0, (live && !(ABL & 8)) ? 0x7fffffff : 0, 0x00020000);
        const int so = t << 13;
        const unsigned dst = ldsw + ((u + 3) & 3) * 32768;
#define DA2(i) __builtin_amdgcn_raw_ptr_buffer_load_lds(ra, (LAS void*)(size_t)dst, 16, voff, so, (i) * 1024, 0)
#define DB2(i) __builtin_amdgcn_raw_ptr_buffer_load_lds(rb, (LAS void*)(size_t)(dst + 16384), 16, voff, so, (i) * 1024, 0)
        DA2(0); DA2(1); DA2(2); DA2(3); DB2(0); DB2(1); DB2(2); DB2(3);
      }
    }
    WAIT_V(0);
    return;
  }

  const int r31 = lane & 31, hh = lane >> 5;
  const int wm = wid >> 1, wn = wid & 1;
  unsigned ab[4];
#pragma unroll
  for (int s = 0; s < 4; ++s) ab[s] = wm * 8192 + (unsigned)(r31 * 128 + (((2 * s + hh) ^ ((r31 >> 1) & 7)) * 16));
  const unsigned bb = 16384 + wn * 8192 + lane * 16;
  const unsigned ao = wm * 8192 + lane * 16;

  f32x16 acc[2][2] = {};
  f32x16 acco = {};
  bf16x8 F0[16], F1[16];
#define RD(F, s) do { F[4 * (s) + 0] = *(const bf16x8*)(sl + ab[s]); F[4 * (s) + 1] = *(const bf16x8*)(sl + ab[s] + 4096); \
                      F[4 * (s) + 2] = *(const bf16x8*)(sl + bb + (s) * 1024); F[4 * (s) + 3] = *(const bf16x8*)(sl + bb + (s) * 1024 + 4096); } while (0)
#define RDO(F, s) do { F[4 * (s) + 0] = *(const bf16x8*)(sl + ao + (s) * 2048); F[4 * (s) + 1] = *(const bf16x8*)(sl + bb + (s) * 2048); \
                       F[4 * (s) + 2] = *(const bf16x8*)(sl + ao + (s) * 2048 + 1024); F[4 * (s) + 3] = *(const bf16x8*)(sl + bb + (s) * 2048 + 1024); } while (0)
#define MM0(F, s) acc[0][0] = __builtin_amdgcn_mfma_f32_32x32x16_bf16(F[4 * (s) + 2], F[4 * (s) + 0], acc[0][0], 0, 0, 0)
#define MM1(F, s) acc[0][1] = __builtin_amdgcn_mfma_f32_32x32x16_bf16(F[4 * (s) + 3], F[4 * (s) + 0], acc[0][1], 0, 0, 0)
#define MM2(F, s) acc[1][0] = __builtin_amdgcn_mfma_f32_32x32x16_bf16(F[4 * (s) + 2], F[4 * (s) + 1], acc[1][0], 0, 0, 0)
#define MM3(F, s) acc[1][1] = __builtin_amdgcn_mfma_f32_32x32x16_bf16(F[4 * (s) + 3], F[4 * (s) + 1], acc[1][1], 0, 0, 0)
#define MO(F, s) do { acco = __builtin_amdgcn_mfma_f32_32x32x16_bf16(F[4 * (s) + 1], F[4 * (s) + 0], acco, 0, 0, 0); \
                      acco = __builtin_amdgcn_mfma_f32_32x32x16_bf16(F[4 * (s) + 3], F[4 * (s) + 2], acco, 0, 0, 0); } while (0)
#define PERIOD(u, RDX, FR, FM) do { \
    const char* sl = lds + ((u) & 3) * 32768; \
    BAR(); SB(); \
    MM0(FM, 0); SB(); RDX(FR, 0); SB(); MM1(FM, 0); SB(); MM2(FM, 0); SB(); RDX(FR, 1); SB(); MM3(FM, 0); SB(); \
    MM0(FM, 1); SB(); RDX(FR, 2); SB(); MM1(FM, 1); SB(); MM2(FM, 1); SB(); RDX(FR, 3); SB(); MM3(FM, 1); SB(); \
    MM0(FM, 2); MM1(FM, 2); MM2(FM, 2); MM3(FM, 2); \
    MM0(FM, 3); MM1(FM, 3); MM2(FM, 3); SB(); WAIT_L0(); SB(); MM3(FM, 3); SB(); \
  } while (0)
#define PERIODO(u, FR, FM) do { \
    const char* sl = lds + ((u) & 3) * 32768; \
    BAR(); SB(); \
    RDO(FR, 0); SB(); MO(FM, 0); SB(); RDO(FR, 1); SB(); MO(FM, 1); SB(); RDO(FR, 2); SB(); MO(FM, 2); SB(); RDO(FR, 3); SB(); MO(FM, 3); SB(); \
    WAIT_L0(); SB(); \
  } while (0)
  {
    const char* sl = lds;
    BAR(); SB(); ST(1, 1);
    RD(F0, 0); RD(F0, 1); RD(F0, 2); RD(F0, 3); SB();
    WAIT_L0(); SB();
  }
  for (int u = 1; u < 15; u += 2) {
    PERIOD(u, RD, F1, F0);
    PERIOD(u + 1, RD, F0, F1);
  }
  PERIOD(15, RD, F1, F0);
  PERIOD(16, RDO, F0, F1);
  ST(1, 2);
  const int tile = rt * 4 + j;
  const auto rs = __builtin_amdgcn_make_buffer_rsrc(ws + WS_SLAB + ((size_t)tile << 17), 0, (ABL & 16) ? 0 : (1 << 17), 0x00020000);
  const unsigned lo = wid * 2048 + lane * 16;
#pragma unroll
  for (int q = 0; q < 4; ++q) {
    if (q != c) {
      const f32x16 v = acc[q >> 1][q & 1];
#pragma unroll
      for (int hf = 0; hf < 2; ++hf) {
        const u32x4 w = {pk_bf16(v[hf * 8], v[hf * 8 + 1]), pk_bf16(v[hf * 8 + 2], v[hf * 8 + 3]), pk_bf16(v[hf * 8 + 4], v[hf * 8 + 5]), pk_bf16(v[hf * 8 + 6], v[hf * 8 + 7])};
        __builtin_amdgcn_raw_buffer_store_b128(w, rs, (q * 4 + c) * 8192 + lo + hf * 1024, 0, 16);
      }
    }
  }
  const f32x16 own = c == 0 ? acc[0][0] : (c == 1 ? acc[0][1] : (c == 2 ? acc[1][0] : acc[1][1]));
  const int trow = rt * 128 + wm * 64 + (c >> 1) * 32 + r31;
  const int ecol = j * 128 + wn * 64 + (c & 1) * 32 + 8 * hh;
  u32x4 xv[2], bv[2];
#pragma unroll
  for (int half = 0; half < 2; ++half) { xv[half] = *(const u32x4*)(x + (size_t)trow * D_ + ecol + 16 * half); bv[half] = *(const u32x4*)(b2 + ecol + 16 * half); }
  SB();
  unsigned* cnt = (unsigned*)(ws + WS_CNT + tile * 256);
  PERIODO(17, F1, F0);
  PERIODO(18, F0, F1);
  PERIODO(19, F1, F0);
  PERIODO(20, F0, F1);
  WAIT_V(0);
  ST(1, 3);
  PERIODO(21, F1, F0);
  if (tid == 0) __hip_atomic_fetch_add(cnt, 1u, __ATOMIC_RELAXED, __HIP_MEMORY_SCOPE_AGENT);
  PERIODO(22, F0, F1);
  PERIODO(23, F1, F0);
  if (tid == 0) {
    unsigned spins = 0;
    while (__hip_atomic_load(cnt, __ATOMIC_RELAXED, __HIP_MEMORY_SCOPE_AGENT) < 4u) { __builtin_amdgcn_s_sleep(1); if (++spins > (1u << 22)) break; }
  }
  asm volatile("" ::: "memory");
  ST(1, 4);
  PERIODO(24, F0, F1);
  asm volatile("" ::: "memory");
  u32x4 pv[4][2];
#pragma unroll
  for (int s = 0; s < 4; ++s)
#pragma unroll
    for (int hf = 0; hf < 2; ++hf)
      pv[s][hf] = __builtin_amdgcn_raw_buffer_load_b128(rs, (c * 4 + s) * 8192 + lo + hf * 1024, 0, 16);
  SB();
  PERIODO(25, F1, F0);
  PERIODO(26, F0, F1);
  MO(F0, 0); MO(F0, 1); MO(F0, 2); MO(F0, 3);
  ST(1, 6);
#pragma unroll
  for (int half = 0; half < 2; ++half) {
    float o[8];
#pragma unroll
    for (int e = 0; e < 8; ++e) {
      const int rg = half * 8 + e;
      float p[4];
#pragma unroll
      for (int s = 0; s < 4; ++s) { const unsigned w = pv[s][half][e >> 1]; p[s] = c == s ? own[rg] : ((e & 1) ? bfhi(w) : bflo(w)); }
      const float s = (p[0] + p[1]) + (p[2] + p[3]);
      const unsigned xw = xv[half][e >> 1], bw = bv[half][e >> 1];
      const float xx = (e & 1) ? bfhi(xw) : bflo(xw), bb2 = (e & 1) ? bfhi(bw) : bflo(bw);
      const float v = acco[rg] + bb2;
      const float g = v * __builtin_amdgcn_rcpf(1.f + __builtin_amdgcn_exp2f(-1.442695041f * v));
      o[e] = xx + s * g;
    }
    u32x4 ov = {pk_bf16(o[0], o[1]), pk_bf16(o[2], o[3]), pk_bf16(o[4], o[5]), pk_bf16(o[6], o[7])};
    *(u32x4*)(out + (size_t)trow * D_ + ecol + 16 * half) = ov;
  }
  ST(1, 5); ST_FLUSH(1);
}

extern "C" void kernel_launch(void* const* d_in, const int* in_sizes, int n_in, void* d_out, int out_size, void* d_ws, size_t ws_size,
                              hipStream_t stream) {
  const u16* x = (const u16*)d_in[0]; const u16* cre = (const u16*)d_in[1]; const u16* cim = (const u16*)d_in[2];
  const u16* wre = (const u16*)d_in[3]; const u16* wim = (const u16*)d_in[4];
  const u16* w1 = (const u16*)d_in[6]; const u16* b1 = (const u16*)d_in[7]; const u16* w2 = (const u16*)d_in[8]; const u16* b2 = (const u16*)d_in[9];
  (void)hipFuncSetAttribute((const void*)k1, hipFuncAttributeMaxDynamicSharedMemorySize, 163840);
  (void)hipFuncSetAttribute((const void*)k2, hipFuncAttributeMaxDynamicSharedMemorySize, 131072);
  k1<<<256, 512, 163840, stream>>>(x, wre, wim, w1, b1, w2, cre, cim, (char*)d_ws);
  k2<<<256, 512, 131072, stream>>>(x, cre, cim, b2, (char*)d_ws, (u16*)d_out);
}
```

```cpp
#include <hip/hip_runtime.h>
#include <hip/hip_bf16.h>
#include <stdint.h>

#define ABL 0
typedef unsigned short u16;
typedef short v4s __attribute__((ext_vector_type(4)));
typedef short bf16x8 __attribute__((ext_vector_type(8)));
typedef __bf16 v2bf __attribute__((ext_vector_type(2)));
typedef float f32x4 __attribute__((ext_vector_type(4)));
typedef float f32x16 __attribute__((ext_vector_type(16)));
typedef unsigned u32x4 __attribute__((ext_vector_type(4)));
typedef unsigned u32x2 __attribute__((ext_vector_type(2)));
#define LAS __attribute__((address_space(3)))
#define GAS __attribute__((address_space(1)))

constexpr int S_ = 2048, D_ = 512, F_ = 1376;
constexpr int NKK_B = 64;
constexpr int NSUB = 88;
constexpr size_t WS_B    = 0;
constexpr size_t WS_W2   = 4u << 20;
constexpr size_t WS_H    = 8u << 20;
constexpr size_t WS_SLAB = 16u << 20;
constexpr size_t WS_CNT  = 40u << 20;

__device__ __forceinline__ float bf2f(u16 v) { return __uint_as_float(((unsigned)v) << 16); }
__device__ __forceinline__ float bflo(unsigned v) { return __uint_as_float(v << 16); }
__device__ __forceinline__ float bfhi(unsigned v) { return __uint_as_float(v & 0xffff0000u); }
__device__ __forceinline__ unsigned pk_bf16(float lo, float hi) {
  typedef float f32x2_ __attribute__((ext_vector_type(2)));
  v2bf r = __builtin_convertvector((f32x2_){lo, hi}, v2bf);
  return __builtin_bit_cast(unsigned, r);
}
__device__ __forceinline__ float rbf(float f) { return bflo(pk_bf16(f, 0.f)); }
__device__ __forceinline__ int swap23(int r) { return (r & ~12) | ((r & 4) << 1) | ((r & 8) >> 1); }
__device__ __forceinline__ float dot2(unsigned a, unsigned b, float c) {
  return __builtin_amdgcn_fdot2_f32_bf16(__builtin_bit_cast(v2bf, a), __builtin_bit_cast(v2bf, b), c, false);
}
#define GLDS16(g, l) __builtin_amdgcn_global_load_lds((const GAS void*)(g), (LAS void*)(l), 16, 0, 0)
typedef int i32x4 __attribute__((ext_vector_type(4)));
__device__ __forceinline__ i32x4 mk_rsrc(const void* p) {
  const unsigned long long a = (unsigned long long)p;
  return (i32x4){(int)(unsigned)a, (int)((unsigned)(a >> 32) & 0xffffu), 0x7fffffff, 0x00020000};
}
__device__ __forceinline__ void dma16(i32x4 rsrc, unsigned lds_dst, int voff, int soff) {
  unsigned keep;
  asm volatile("s_mov_b32 %0, m0\n\ts_mov_b32 m0, %1\n\ts_nop 0\n\tbuffer_load_dwordx4 %2, %3, %4 offen lds\n\ts_mov_b32 m0, %0"
               : "=&s"(keep) : "s"(lds_dst), "v"(voff), "s"(rsrc), "s"(soff) : "memory");
}
__device__ __forceinline__ void touch4(i32x4 rsrc, unsigned lds_dst, int voff, int soff) {
  unsigned keep;
  asm volatile("s_mov_b32 %0, m0\n\ts_mov_b32 m0, %1\n\ts_nop 0\n\tbuffer_load_dword %2, %3, %4 offen lds\n\ts_mov_b32 m0, %0"
               : "=&s"(keep) : "s"(lds_dst), "v"(voff), "s"(rsrc), "s"(soff) : "memory");
}
template <int OFF> __device__ __forceinline__ void dma16x(i32x4 rsrc, unsigned lds_dst, int voff, int soff) {
  unsigned keep;
  asm volatile("s_mov_b32 %0, m0\n\ts_mov_b32 m0, %1\n\ts_nop 3\n\tbuffer_load_dwordx4 %2, %3, %4 offen offset:%5 lds\n\ts_mov_b32 m0, %0"
               : "=&s"(keep) : "s"(lds_dst), "v"(voff), "s"(rsrc), "s"(soff), "n"(OFF) : "memory");
}
template <int OFF> __device__ __forceinline__ void dma16x_nt(i32x4 rsrc, unsigned lds_dst, int voff, int soff) {
  unsigned keep;
  asm volatile("s_mov_b32 %0, m0\n\ts_mov_b32 m0, %1\n\ts_nop 3\n\tbuffer_load_dwordx4 %2, %3, %4 offen offset:%5 nt lds\n\ts_mov_b32 m0, %0"
               : "=&s"(keep) : "s"(lds_dst), "v"(voff), "s"(rsrc), "s"(soff), "n"(OFF) : "memory");
}
__device__ __forceinline__ void touch4_nt(i32x4 rsrc, unsigned lds_dst, int voff, int soff) {
  unsigned keep;
  asm volatile("s_mov_b32 %0, m0\n\ts_mov_b32 m0, %1\n\ts_nop 0\n\tbuffer_load_dword %2, %3, %4 offen nt lds\n\ts_mov_b32 m0, %0"
               : "=&s"(keep) : "s"(lds_dst), "v"(voff), "s"(rsrc), "s"(soff) : "memory");
}
#define WAIT_V(n) asm volatile("s_waitcnt vmcnt(%0)" ::"n"(n) : "memory")
#define WAIT_L0() asm volatile("s_waitcnt lgkmcnt(0)" ::: "memory")
#define BAR() __builtin_amdgcn_s_barrier()
#define ST_DECL
#define ST(k, i)
#define ST_FLUSH(k)

#define AUX_PF 2
#define PF_ON 0
#define PF_AT 3
#define AUX_ST 16
#define TOUCH 2
typedef float f32x2 __attribute__((ext_vector_type(2)));
constexpr int SL1 = 0, SL2 = 33280, SL3 = 98304, SL4 = 0, SLP = 131072, AROW = 1040;
constexpr int SCR = 66560;
constexpr int SC_PART = SCR;
constexpr int SC_HS   = SCR + 2048;
constexpr int SC_RS   = SCR + 3072;
constexpr int SC_MRS  = SCR + 3584;
constexpr int SC_CS   = SCR + 4096;
constexpr int SC_B1   = SCR + 7168;
constexpr int SC_DUM  = SCR + 8192;
#define PIN(x) asm volatile("" : "+v"(x))
#define SBAR() __builtin_amdgcn_sched_barrier(0)

template <int K> __device__ __forceinline__ void eh_op(const f32x16& ap, float (&v)[16], float (&t)[16], unsigned (&ow)[8], float scale, float center) {
  constexpr int st = K / 16, e = K % 16;
  if constexpr (st == 0) { v[e] = fmaf(-center, t[e], v[e]); PIN(v[e]); }
  else if constexpr (st == 1) { v[e] = fmaf(ap[e], scale, v[e]); PIN(v[e]); }
  else if constexpr (st == 2) { t[e] = v[e] * v[e]; PIN(t[e]); }
  else if constexpr (st == 3) { t[e] = fmaf(t[e], -0.1029432397f, -2.302208198f); PIN(t[e]); }
  else if constexpr (st == 4) { t[e] = t[e] * v[e]; PIN(t[e]); }
  else if constexpr (st == 5) { t[e] = __builtin_amdgcn_exp2f(t[e]); PIN(t[e]); }
  else if constexpr (st == 6) { t[e] = 1.f + t[e]; PIN(t[e]); }
  else if constexpr (st == 7) { t[e] = __builtin_amdgcn_rcpf(t[e]); PIN(t[e]); }
  else if constexpr (st == 8) { v[e] = v[e] * t[e]; PIN(v[e]); }
  else if constexpr (K < 152) { ow[e] = pk_bf16(v[2 * e], v[2 * e + 1]); PIN(ow[e]); }
}
template <int K> __device__ __forceinline__ void ea_op(const f32x16& ap, float (&v)[16], float (&t)[16], unsigned (&ow)[8], float cs) {
  constexpr int st = K / 16, e = K % 16;
  if constexpr (st == 0) { t[e] = ap[e] * t[e]; PIN(t[e]); }
  else if constexpr (st == 1) { t[e] = fmaf(-v[e], cs, t[e]); PIN(t[e]); }
  else if constexpr (K < 40) { ow[e] = pk_bf16(t[2 * e], t[2 * e + 1]); PIN(ow[e]); }
}
template <int ET, int K0, int K1> __device__ __forceinline__ void e_ops(const f32x16& ap, float (&v)[16], float (&t)[16], unsigned (&ow)[8], float s0, float s1) {
  if constexpr (K0 < K1) {
    if constexpr (ET == 1) eh_op<K0>(ap, v, t, ow, s0, s1); else if constexpr (ET == 2) ea_op<K0>(ap, v, t, ow, s0);
    e_ops<ET, K0 + 1, K1>(ap, v, t, ow, s0, s1);
  }
}
template <int MT> __device__ __forceinline__ bf16x8 wread(const char* pan, int ks) {
  if constexpr (MT == 1) { return *(const bf16x8*)(pan + ks * 32); }
  else {
    v4s t0 = __builtin_amdgcn_ds_read_tr16_b64_v4i16((LAS v4s*)(pan + ks * 1024));
    v4s t1 = __builtin_amdgcn_ds_read_tr16_b64_v4i16((LAS v4s*)(pan + ks * 1024 + 256));
    return (bf16x8)__builtin_shufflevector(t0, t1, 0, 1, 2, 3, 4, 5, 6, 7);
  }
}
__device__ __forceinline__ void row_stats(float sum, float sq, float& mean, float& inv, float& m_bf, float& scale, float& center) {
  mean = sum * (1.f / 512.f);
  const float var = fmaxf(sq * (1.f / 512.f) - mean * mean, 0.f);
  m_bf = rbf(mean);
  const float sd = rbf(sqrtf(rbf(rbf(var) + 1e-5f)));
  inv = 1.f / sd;
  const float m2 = rbf((mean - m_bf) * inv);
  const float v2 = rbf(var * inv * inv);
  const float sd2 = rbf(sqrtf(rbf(v2 + 1e-5f)));
  scale = inv / sd2; center = (m_bf + m2 * sd) * scale;
}
constexpr int WR = 6;
template <int MT, int ET, int KS> __device__ __forceinline__ void unit_steps(const char* pan, const bf16x8 (&xf)[32], bf16x8 (&wf)[WR], f32x16& acc,
                                                                          const f32x16& ap, float (&v)[16], float (&t)[16], unsigned (&ow)[8], float s0, float s1) {
  if constexpr (KS < 32) {
    constexpr int NOPS = ET == 1 ? 152 : (ET == 2 ? 40 : 0);
    constexpr int G0 = 2;
    if constexpr (MT == 1) acc = __builtin_amdgcn_mfma_f32_32x32x16_bf16(xf[KS], wf[KS % WR], acc, 0, 0, 0);
    else acc = __builtin_amdgcn_mfma_f32_32x32x16_bf16(wf[KS % WR], xf[KS], acc, 0, 0, 0);
    PIN(acc);
    SBAR();
    if constexpr (KS + WR - 1 < 32) wf[(KS + WR - 1) % WR] = wread<MT>(pan, KS + WR - 1);
    SBAR();
    constexpr int k0 = KS < G0 ? 0 : (KS - G0) * NOPS / (32 - G0), k1 = KS < G0 ? 0 : (KS + 1 - G0) * NOPS / (32 - G0);
    e_ops<ET, k0, k1>(ap, v, t, ow, s0, s1);
    SBAR();
    unit_steps<MT, ET, KS + 1>(pan, xf, wf, acc, ap, v, t, ow, s0, s1);
  }
}

__global__ __launch_bounds__(512, 2) void k1(const u16* __restrict__ x, const u16* __restrict__ wre, const u16* __restrict__ wim,
                                             const u16* __restrict__ w1, const u16* __restrict__ b1, const u16* __restrict__ w2,
                                             const u16* __restrict__ cre, const u16* __restrict__ cim, char* __restrict__ ws) {
  extern __shared__ __attribute__((aligned(16))) char lds[];
  const int tid = threadIdx.x, lane = tid & 63, wid = __builtin_amdgcn_readfirstlane(tid >> 6);
  const int b = blockIdx.x, xcd = b & 7, idx = b >> 3;
  const int rt = (xcd >> 1) * 4 + (idx & 3);
  const int cg = (xcd & 1) * 8 + (idx >> 2);
  const int h0 = cg < 11 ? cg * 3 : 33 + (cg - 11) * 2;
  const bool h3 = cg < 11;
  const int a0 = cg * 2;
  const int m = wid & 3;
  const bool helper = wid >= 4;
  const int r31 = lane & 31, hh = lane >> 5;
  const unsigned lds0 = (unsigned)(size_t)(LAS char*)lds;
  const int prot = idx & 3;
  ST_DECL;
  ST(0, 0);

  const int hoff = (lane >> 2) * (F_ * 2) + (lane & 3) * 16;
  const i32x4 rw1 = mk_rsrc(w1);
  auto piece_h = [&](int u, int q, int slot_off) { dma16(rw1, lds0 + slot_off + q * 1024, hoff, q * 16 * (F_ * 2) + u * 64); };
  const i32x4 rx = mk_rsrc(x + (size_t)(rt * 128 + m * 32) * D_);
  int xo[4];
#pragma unroll
  for (int i = 0; i < 4; ++i) { const int r = i * 4 + (lane >> 4); xo[i] = r * 1024 + (((lane & 15) ^ r) * 16); }
  auto quarter = [&](int q) {
#pragma unroll
    for (int i = 0; i < 8; ++i) dma16(rx, lds0 + q * 32768 + m * 8192 + i * 1024, xo[i & 3], q * 256 + (i >> 2) * 16384);
    piece_h(h0, q * 8 + m * 2, SLP); piece_h(h0, q * 8 + m * 2 + 1, SLP);
  };
  if (helper) { quarter(0); quarter(1); }

  unsigned w2v[8], b1v;
  const int w2chunk = b * 352 + tid;
  {
    i32x4 rw2 = mk_rsrc(w2); rw2.z = F_ * D_ * 2;
    const int c = w2chunk;
    const int ln = c & 63, rest = c >> 6;
    const int ct = rest / NSUB, sub = rest - ct * NSUB;
    const int d = 32 * ct + swap23(ln & 31);
    const int fb = 16 * sub + 4 * (ln >> 5);
    const int off = tid < 352 ? (fb * D_ + d) * 2 : 0x7ff00000;
    const int bo = h0 * 32 + lane * 2;
    const i32x4 rb1 = mk_rsrc(b1);
    asm volatile("buffer_load_ushort %0, %9, %11, 0 offen\n\tbuffer_load_ushort %1, %9, %11, 0 offen offset:1024\n\t"
                 "buffer_load_ushort %2, %9, %11, 0 offen offset:2048\n\tbuffer_load_ushort %3, %9, %11, 0 offen offset:3072\n\t"
                 "buffer_load_ushort %4, %10, %11, 0 offen\n\tbuffer_load_ushort %5, %10, %11, 0 offen offset:1024\n\t"
                 "buffer_load_ushort %6, %10, %11, 0 offen offset:2048\n\tbuffer_load_ushort %7, %10, %11, 0 offen offset:3072\n\t"
                 "buffer_load_dword %8, %12, %13, 0 offen"
                 : "=&v"(w2v[0]), "=&v"(w2v[1]), "=&v"(w2v[2]), "=&v"(w2v[3]), "=&v"(w2v[4]), "=&v"(w2v[5]), "=&v"(w2v[6]), "=&v"(w2v[7]), "=&v"(b1v)
                 : "v"(off), "v"(off + 8192), "s"(rw2), "v"((bo < F_ - 2 ? bo : F_ - 2) * 2), "s"(rb1) : "memory");
  }
#define REGS_LANDED() asm volatile("" : "+v"(w2v[0]), "+v"(w2v[1]), "+v"(w2v[2]), "+v"(w2v[3]), "+v"(w2v[4]), "+v"(w2v[5]), "+v"(w2v[6]), "+v"(w2v[7]), "+v"(b1v))
  const auto rws = __builtin_amdgcn_make_buffer_rsrc((void*)ws, 0, 0x7fffffff, 0x00020000);
  const int xrow = m * 32 + swap23(r31);
  const unsigned xbase = xrow * 256 + ((hh ^ (xrow & 15)) * 16);
#define QWAIT(q) WAIT_V(9 + 10 * (3 - (q)))

  if (helper) {
    const i32x4 rwa = mk_rsrc((a0 < 16 ? wre : wim) + (size_t)(a0 & 15) * 32 * D_);
    auto panel_h = [&](int u, int slot_off) {
#pragma unroll
      for (int k = 0; k < 8; ++k) piece_h(u, m * 8 + ((k + 2 * prot) & 7), slot_off);
    };
    auto panel_a = [&](int w, int slot_off) {
#pragma unroll
      for (int k = 0; k < 8; ++k) { const int q = m * 8 + ((k + 2 * prot) & 7); dma16(rwa, lds0 + slot_off + q * AROW, lane * 16, (w * 32 + q) * 1024); }
    };
    const v4s ones = {0x3f80, 0x3f80, 0x3f80, 0x3f80};
    const char* pzh = lds + ((lane >> 2) & 15) * 64 + (m & 1) * 32 + (lane & 3) * 8;
    auto cs_h_part = [&](f32x4 (&c)[4], int slot_off, int j0, int n) {
#pragma unroll
      for (int j = 0; j < n; ++j)
        c[j & 3] = __builtin_amdgcn_mfma_f32_4x4x4bf16_1k(ones, __builtin_amdgcn_ds_read_tr16_b64_v4i16((LAS v4s*)(pzh + slot_off + (j0 + j) * 1024)), c[j & 3], 0, 0, 0);
    };
    auto cs_h_fin = [&](f32x4 (&c)[4], int i) {
      float r = (c[0][0] + c[1][0]) + (c[2][0] + c[3][0]);
      r += __shfl_xor(r, 16); r += __shfl_xor(r, 32);
      if (lane < 16) *(float*)(lds + SC_CS + i * 512 + (m & 1) * 64 + lane * 4) = r;
    };
    auto cs_h = [&](int i, int slot_off) {
      if ((m >> 1) == (i & 1)) { f32x4 c[4] = {}; cs_h_part(c, slot_off, 0, 32); cs_h_fin(c, i); }
    };
    auto cs_a = [&](int i, int slot_off) {
      const char* pz = lds + slot_off + swap23(r31) * AROW + hh * 16 + m * 256;
      f32x4 c0 = {}, c1 = {};
#pragma unroll
      for (int j = 0; j < 8; ++j) {
        const bf16x8 w = *(const bf16x8*)(pz + j * 32);
        c0 = __builtin_amdgcn_mfma_f32_4x4x4bf16_1k(ones, __builtin_shufflevector(w, w, 0, 1, 2, 3), c0, 0, 0, 0);
        c1 = __builtin_amdgcn_mfma_f32_4x4x4bf16_1k(ones, __builtin_shufflevector(w, w, 4, 5, 6, 7), c1, 0, 0, 0);
      }
      float r = c0[0] + c1[0]; r += __shfl_xor(r, 32);
      if (lane < 32) *(float*)(lds + SC_CS + i * 512 + m * 128 + lane * 4) = r;
    };
    auto prefetch_c = [&]() {
      const int c2 = xcd >> 1;
      const int row = (xcd & 1) * 1024 + idx * 32 + m * 8 + (lane >> 4);
      const i32x4 rc = mk_rsrc(c2 < 2 ? cre : cim);
      const int vo = row * 4096 + (lane & 15) * 128;
      if (AUX_PF == 2) { touch4_nt(rc, lds0 + SC_DUM + m * 256, vo, (c2 & 1) * 2048); touch4_nt(rc, lds0 + SC_DUM + m * 256, vo, (c2 & 1) * 2048 + 4 * 4096); }
      else { touch4(rc, lds0 + SC_DUM + m * 256, vo, (c2 & 1) * 2048); touch4(rc, lds0 + SC_DUM + m * 256, vo, (c2 & 1) * 2048 + 4 * 4096); }
    };
    __builtin_amdgcn_s_setprio(2);
    {
      f32x4 c[4] = {};
      WAIT_V(19); ST(0, 1); BAR();
      quarter(2);
      if (m < 2) cs_h_part(c, SLP, 0, 8);
      WAIT_V(10); BAR();
      quarter(3);
      panel_h(h0 + 1, SL1);
      if (m < 2) cs_h_part(c, SLP, 8, 8);
      WAIT_V(18); BAR();
      if (m < 2) cs_h_part(c, SLP, 16, 8);
      WAIT_V(8); BAR();
      ST(0, 2);
      panel_a(0, SL2);
      if (m < 2) { cs_h_part(c, SLP, 24, 8); cs_h_fin(c, 0); }
    }
    REGS_LANDED();
    if (m == 3 && lane < 48) *(f32x2*)(lds + SC_B1 + lane * 8) = (f32x2){bflo(b1v), bfhi(b1v)};
    if (tid < 352) {
      u32x4 v = {w2v[0] | (w2v[1] << 16), w2v[2] | (w2v[3] << 16), w2v[4] | (w2v[5] << 16), w2v[6] | (w2v[7] << 16)};
      __builtin_amdgcn_raw_buffer_store_b128(v, rws, w2chunk * 16, (int)WS_W2, AUX_ST);
    } else { __builtin_amdgcn_raw_buffer_store_b128((u32x4){0u, 0u, 0u, 0u}, rws, 0x7ffffff0, 0x7ffffff0, AUX_ST); }
    ST(0, 3);
    WAIT_V(9); WAIT_L0(); BAR();
    if (h3) panel_h(h0 + 2, SL3); else panel_a(1, SL3);
    cs_h(1, SL1);
    {
      float mean, inv, m_bf, sc_, ce_;
      const f32x2 pr = *(const f32x2*)(lds + SC_PART + (m * 32 + r31) * 8);
      row_stats(pr.x, pr.y, mean, inv, m_bf, sc_, ce_);
      const float sg = a0 < 16 ? inv : -inv;
      if (lane < 32) { *(float*)(lds + SC_RS + (m * 32 + lane) * 4) = sg; *(float*)(lds + SC_MRS + (m * 32 + lane) * 4) = m_bf * sg; }
    }
    WAIT_V(8); WAIT_L0(); BAR();
    if (h3) panel_a(1, SL4);
    if (PF_ON && PF_AT == 3) prefetch_c();
    cs_a(2, SL2);
    if (PF_ON && PF_AT == 3) { if (h3) WAIT_V(10); else WAIT_V(2); } else { if (h3) WAIT_V(8); else WAIT_V(0); }
    WAIT_L0(); BAR();
    if (PF_ON && PF_AT == 4) prefetch_c();
    if (h3) {
      cs_h(3, SL3);
      WAIT_V(PF_ON ? 2 : 0); WAIT_L0(); BAR();
      cs_a(4, SL4);
    } else cs_a(3, SL3);
    WAIT_L0(); BAR();
    WAIT_V(0);
    ST(0, 5); ST_FLUSH(0);
    return;
  }

  bf16x8 xf[32];
  unsigned long long clk0 = 0; (void)clk0;
  const int wrow = swap23(r31);
  const unsigned wlA = (unsigned)(wrow * AROW + hh * 16);
  const unsigned wlH = (unsigned)((8 * hh + ((lane & 15) >> 2)) * 64 + ((lane >> 4) & 1) * 32 + (lane & 3) * 8);
  f32x16 acc, ap = {};
  bf16x8 wf[WR];
  float v[16], t[16]; unsigned ow[8];
  float scale = 0.f, center = 0.f;
  const int hstore = (swap23(r31) + 32 * hh) * 16;
  const int s0r = rt * 128 + m * 32;

  auto tables_h = [&](int i, int hu) {
#pragma unroll
    for (int g4 = 0; g4 < 4; ++g4) {
      const f32x4 c = *(const f32x4*)(lds + SC_CS + i * 512 + (8 * g4 + 4 * hh) * 4), bq = *(const f32x4*)(lds + SC_B1 + hu * 128 + (8 * g4 + 4 * hh) * 4);
#pragma unroll
      for (int e = 0; e < 4; ++e) { t[g4 * 4 + e] = c[e]; v[g4 * 4 + e] = bq[e]; }
    }
  };
  auto tables_a = [&](int i) -> float {
#pragma unroll
    for (int g4 = 0; g4 < 4; ++g4) {
      const f32x4 c = *(const f32x4*)(lds + SC_RS + (m * 32 + 8 * g4 + 4 * hh) * 4), bq = *(const f32x4*)(lds + SC_MRS + (m * 32 + 8 * g4 + 4 * hh) * 4);
#pragma unroll
      for (int e = 0; e < 4; ++e) { t[g4 * 4 + e] = c[e]; v[g4 * 4 + e] = bq[e]; }
    }
    const char* cq = lds + SC_CS + i * 512 + r31 * 4;
    return (*(const float*)cq + *(const float*)(cq + 128)) + (*(const float*)(cq + 256) + *(const float*)(cq + 384));
  };
  auto store_h = [&](int hu) {
    const int dst = (int)WS_H + (((rt * 4 + m) * NSUB + 2 * (h0 + hu)) << 10);
    __builtin_amdgcn_raw_buffer_store_b128((u32x4){ow[0], ow[1], ow[2], ow[3]}, rws, hstore, dst, AUX_ST);
    __builtin_amdgcn_raw_buffer_store_b128((u32x4){ow[4], ow[5], ow[6], ow[7]}, rws, hstore + 1024, dst, AUX_ST);
    if (h0 + hu == 42) { u32x4 z = {0u, 0u, 0u, 0u}; __builtin_amdgcn_raw_buffer_store_b128(z, rws, lane * 16 + 2048, dst, AUX_ST); __builtin_amdgcn_raw_buffer_store_b128(z, rws, lane * 16 + 3072, dst, AUX_ST); }
  };
  auto store_a = [&](int w) {
    const int u = a0 + w, et = u & 15, j = et >> 2, nt = et & 3;
    const int kk = (u >> 4) * 32 + (s0r >> 6), sub = (s0r & 63) >> 4;
    const int dst = (int)WS_B + (((j * NKK_B + kk) * 16 + nt * 4 + sub) << 10);
    __builtin_amdgcn_raw_buffer_store_b128((u32x4){ow[0], ow[1], ow[2], ow[3]}, rws, lane * 16, dst, AUX_ST);
    __builtin_amdgcn_raw_buffer_store_b128((u32x4){ow[4], ow[5], ow[6], ow[7]}, rws, lane * 16 + 1024, dst, AUX_ST);
  };
#define UNIT(MT, ET, slot, s0, s1) do { const char* pan = lds + (slot) + (MT ? wlA : wlH); acc = (f32x16){}; \
    _Pragma("unroll") for (int e = 0; e < WR - 1; ++e) wf[e] = wread<MT>(pan, e); \
    unit_steps<MT, ET, 0>(pan, xf, wf, acc, ap, v, t, ow, s0, s1); } while (0)
  {
    float ss0 = 0.f, ss1 = 0.f, sq0 = 0.f, sq1 = 0.f;
    acc = (f32x16){};
    const char* pan = lds + SLP + wlH;
#pragma unroll
    for (int q = 0; q < 4; ++q) {
      if (q == 0) ST(0, 1);
      BAR();
      if (q == 3) ST(0, 2);
      bf16x8 w8[8];
#pragma unroll
      for (int k = 0; k < 8; ++k) xf[q * 8 + k] = *(const bf16x8*)(lds + q * 32768 + (xbase ^ (k * 32)));
#pragma unroll
      for (int k = 0; k < 8; ++k) w8[k] = wread<0>(pan, q * 8 + k);
      SBAR();
#pragma unroll
      for (int k = 0; k < 8; ++k) {
        const u32x4 v4 = __builtin_bit_cast(u32x4, xf[q * 8 + k]);
        ss0 = dot2(v4.x, 0x3f803f80u, ss0); sq0 = dot2(v4.x, v4.x, sq0); ss1 = dot2(v4.y, 0x3f803f80u, ss1); sq1 = dot2(v4.y, v4.y, sq1);
        ss0 = dot2(v4.z, 0x3f803f80u, ss0); sq0 = dot2(v4.z, v4.z, sq0); ss1 = dot2(v4.w, 0x3f803f80u, ss1); sq1 = dot2(v4.w, v4.w, sq1);
      }
      asm volatile("" : "+v"(ss0), "+v"(ss1), "+v"(sq0), "+v"(sq1));
      SBAR();
#pragma unroll
      for (int k = 0; k < 8; ++k) { acc = __builtin_amdgcn_mfma_f32_32x32x16_bf16(w8[k], xf[q * 8 + k], acc, 0, 0, 0); PIN(acc); }
      WAIT_L0(); SBAR();
    }
    float sum = ss0 + ss1, sq = sq0 + sq1;
    sum += __shfl_xor(sum, 32); sq += __shfl_xor(sq, 32);
    if (lane < 32) *(f32x2*)(lds + SC_PART + (m * 32 + lane) * 8) = (f32x2){sum, sq};
    float mean, inv, m_bf; row_stats(sum, sq, mean, inv, m_bf, scale, center);
    WAIT_V(0); REGS_LANDED();
    u32x4 v = {w2v[0] | (w2v[1] << 16), w2v[2] | (w2v[3] << 16), w2v[4] | (w2v[5] << 16), w2v[6] | (w2v[7] << 16)};
    __builtin_amdgcn_raw_buffer_store_b128(v, rws, w2chunk * 16, (int)WS_W2, AUX_ST);
    if (b == 0 && wid == 0) *(unsigned*)(ws + WS_CNT + lane * 256) = 0u;
  }
  ST(0, 8);
  WAIT_L0(); BAR(); ap = acc;
  tables_h(0, 0);
  UNIT(0, 1, SL1, scale, center); store_h(0);
  ST(0, 9);
  BAR(); ap = acc;
  tables_h(1, 1);
  UNIT(1, 1, SL2, scale, center); store_h(1);
  ST(0, 10);
  BAR(); ap = acc;
  float csa = tables_a(2);
  if (h3) {
    UNIT(0, 2, SL3, csa, 0.f); store_a(0);
    ST(0, 11);
    BAR(); ap = acc;
    tables_h(3, 2);
    UNIT(1, 1, SL4, scale, center); store_h(2);
  } else {
    UNIT(1, 2, SL3, csa, 0.f); store_a(0);
  }
  ST(0, 12);
  BAR(); ap = acc;
  csa = tables_a(h3 ? 4 : 3);
  e_ops<2, 0, 40>(ap, v, t, ow, csa, 0.f);
  store_a(1);
  WAIT_V(0);
  ST(0, 5); ST_FLUSH(0);
}

#define AUX_C 2
__global__ __launch_bounds__(512, 2) void k2(const u16* __restrict__ x, const u16* __restrict__ cre, const u16* __restrict__ cim,
                                             const u16* __restrict__ b2, char* __restrict__ ws, u16* __restrict__ out) {
  extern __shared__ __attribute__((aligned(16))) char lds[];
  const int tid = threadIdx.x, lane = tid & 63, wid = __builtin_amdgcn_readfirstlane(tid >> 6);
  const int b = blockIdx.x, xcd = b & 7, idx = b >> 3;
#define MAP2 0
  const int c = MAP2 ? (xcd >> 2) * 2 + (idx >> 4) : xcd >> 1, rt = MAP2 ? (xcd & 3) * 4 + ((idx >> 2) & 3) : (xcd & 1) * 8 + (idx >> 2), j = idx & 3;
  ST_DECL;
  ST(1, 0);

#define SB() __builtin_amdgcn_sched_barrier(0)

  if (wid >= 4) {
    const int lw = wid - 4;
    int aoff[4];
#pragma unroll
    for (int i = 0; i < 4; ++i) { int row = lw * 32 + i * 8 + (lane >> 3); aoff[i] = row * 4096 + (((lane & 7) ^ ((row >> 1) & 7)) * 16) - i * 1024 + 4096; }
    const unsigned ldsw = (unsigned)(size_t)(LAS char*)lds + lw * 4096;
    const i32x4 ra_s = mk_rsrc((const char*)(c < 2 ? cre : cim) + (size_t)rt * 128 * 4096 + (c & 1) * 2048 - 4096);
    const i32x4 rb_s = mk_rsrc(ws + WS_B + ((size_t)(j * NKK_B + c * 16) << 14) + lw * 4096);
    const i32x4 ra_o = mk_rsrc(ws + WS_H + ((size_t)((rt * 4 + 2 * (lw >> 1) + (c >> 1)) * NSUB) << 10) + (lw & 1) * 4096 - (16 << 13));
    const i32x4 rb_o = mk_rsrc(ws + WS_W2 + ((size_t)((j * 4 + 2 * (lw >> 1) + (c & 1)) * NSUB) << 10) + (lw & 1) * 4096 - (16 << 13));
    const int voff = lane * 16;
    const int r8 = idx >> 2;
#define LEAD 0
    const int ea = LEAD ? __builtin_amdgcn_readfirstlane(lw == j ? 1 : 0) : 1, eb0 = LEAD ? __builtin_amdgcn_readfirstlane(r8 == 2 * lw ? 1 : 0) : 1,
              eb1 = LEAD ? __builtin_amdgcn_readfirstlane(r8 == 2 * lw + 1 ? 1 : 0) : 1;
    auto issue_a = [&](int T) {
      const unsigned dst = __builtin_amdgcn_readfirstlane(ldsw + (T & 3) * 32768);
      if (T < 16) { const int so = __builtin_amdgcn_readfirstlane(T * 128);
                    dma16x_nt<0>(ra_s, dst, aoff[0], so); dma16x_nt<1024>(ra_s, dst, aoff[1], so); dma16x_nt<2048>(ra_s, dst, aoff[2], so); dma16x_nt<3072>(ra_s, dst, aoff[3], so); }
      else { i32x4 r = ra_o; r.z = __builtin_amdgcn_readfirstlane(T < 27 ? 0x7fffffff : 0); const int so = __builtin_amdgcn_readfirstlane(T << 13);
             dma16x<0>(r, dst, voff, so); dma16x<1024>(r, dst, voff, so); dma16x<2048>(r, dst, voff, so); dma16x<3072>(r, dst, voff, so); }
    };
    auto issue_b = [&](int T, int half) {
      const unsigned dst = __builtin_amdgcn_readfirstlane(ldsw + (T & 3) * 32768 + 16384 + half * 2048);
      i32x4 r = T < 16 ? rb_s : rb_o; r.z = T < 27 ? 0x7fffffff : 0;
      const int so = __builtin_amdgcn_readfirstlane((T < 16 ? T << 14 : T << 13) + half * 2048);
      dma16x<0>(r, dst, voff, so); dma16x<1024>(r, dst, voff, so);
    };
    issue_a(0); issue_b(0, 0); issue_b(0, 1);
    issue_a(1); issue_b(1, 0); issue_b(1, 1);
    if (ea) issue_a(2);
    if (eb0) issue_b(2, 0);
    if (eb1) issue_b(2, 1);
    const int npro = 8 + 4 * ea + 2 * eb0 + 2 * eb1;
    if (npro == 8) WAIT_V(8); else if (npro == 10) WAIT_V(10); else if (npro == 12) WAIT_V(12); else if (npro == 14) WAIT_V(14); else WAIT_V(16);
    for (int u = 0; u < 27; ++u) {
      if (u) WAIT_V(LEAD ? 8 : 16);
      BAR();
      issue_a(u + 2 + ea); issue_b(u + 2 + eb0, 0); issue_b(u + 2 + eb1, 1);
    }
    WAIT_V(0);
    return;
  }

  const int r31 = lane & 31, hh = lane >> 5;
  const int wm = wid >> 1, wn = wid & 1;
  unsigned ab[4];
#pragma unroll
  for (int s = 0; s < 4; ++s) ab[s] = wm * 8192 + (unsigned)(r31 * 128 + (((2 * s + hh) ^ ((r31 >> 1) & 7)) * 16));
  const unsigned bb = 16384 + wn * 8192 + lane * 16;
  const unsigned ao = wm * 8192 + lane * 16;

  f32x16 acc[2][2] = {};
  f32x16 acco = {};
  bf16x8 F0[16], F1[16];
#define RD(F, s) do { F[4 * (s) + 0] = *(const bf16x8*)(sl + ab[s]); F[4 * (s) + 1] = *(const bf16x8*)(sl + ab[s] + 4096); \
                      F[4 * (s) + 2] = *(const bf16x8*)(sl + bb + (s) * 1024); F[4 * (s) + 3] = *(const bf16x8*)(sl + bb + (s) * 1024 + 4096); } while (0)
#define RDO(F, s) do { F[4 * (s) + 0] = *(const bf16x8*)(sl + ao + (s) * 2048); F[4 * (s) + 1] = *(const bf16x8*)(sl + bb + (s) * 2048); \
                       F[4 * (s) + 2] = *(const bf16x8*)(sl + ao + (s) * 2048 + 1024); F[4 * (s) + 3] = *(const bf16x8*)(sl + bb + (s) * 2048 + 1024); } while (0)
#define MM0(F, s) acc[0][0] = __builtin_amdgcn_mfma_f32_32x32x16_bf16(F[4 * (s) + 2], F[4 * (s) + 0], acc[0][0], 0, 0, 0)
#define MM1(F, s) acc[0][1] = __builtin_amdgcn_mfma_f32_32x32x16_bf16(F[4 * (s) + 3], F[4 * (s) + 0], acc[0][1], 0, 0, 0)
#define MM2(F, s) acc[1][0] = __builtin_amdgcn_mfma_f32_32x32x16_bf16(F[4 * (s) + 2], F[4 * (s) + 1], acc[1][0], 0, 0, 0)
#define MM3(F, s) acc[1][1] = __builtin_amdgcn_mfma_f32_32x32x16_bf16(F[4 * (s) + 3], F[4 * (s) + 1], acc[1][1], 0, 0, 0)
#define MO(F, s) do { acco = __builtin_amdgcn_mfma_f32_32x32x16_bf16(F[4 * (s) + 1], F[4 * (s) + 0], acco, 0, 0, 0); \
                      acco = __builtin_amdgcn_mfma_f32_32x32x16_bf16(F[4 * (s) + 3], F[4 * (s) + 2], acco, 0, 0, 0); } while (0)
#define PERIOD(u, RDX, FR, FM) do { \
    const char* sl = lds + ((u) & 3) * 32768; \
    BAR(); SB(); \
    MM0(FM, 0); SB(); RDX(FR, 0); SB(); MM1(FM, 0); SB(); MM2(FM, 0); SB(); RDX(FR, 1); SB(); MM3(FM, 0); SB(); \
    MM0(FM, 1); SB(); RDX(FR, 2); SB(); MM1(FM, 1); SB(); MM2(FM, 1); SB(); RDX(FR, 3); SB(); MM3(FM, 1); SB(); \
    MM0(FM, 2); MM1(FM, 2); MM2(FM, 2); MM3(FM, 2); \
    MM0(FM, 3); MM1(FM, 3); MM2(FM, 3); SB(); WAIT_L0(); SB(); MM3(FM, 3); SB(); \
  } while (0)
#define PERIODO(u, FR, FM) do { \
    const char* sl = lds + ((u) & 3) * 32768; \
    BAR(); SB(); \
    RDO(FR, 0); SB(); MO(FM, 0); SB(); RDO(FR, 1); SB(); MO(FM, 1); SB(); RDO(FR, 2); SB(); MO(FM, 2); SB(); RDO(FR, 3); SB(); MO(FM, 3); SB(); \
    WAIT_L0(); SB(); \
  } while (0)
#define K2PF 0
#define K2PF_POS 0
  const unsigned dum = (unsigned)(size_t)(LAS char*)lds + 131072 + wid * 256;
  auto touches = [&]() {
    if (K2PF & 1) {
      const i32x4 rb = mk_rsrc(ws + WS_B + ((size_t)(j * NKK_B + c * 16) << 14));
      touch4(rb, dum, ((lane >> 4) + 4 * wid) * 16384 + ((idx >> 2) * 16 + (lane & 15)) * 128, 0);
    }
    if (K2PF & 2) {
      const i32x4 rc = mk_rsrc((const char*)(c < 2 ? cre : cim) + (size_t)(rt * 128 + j * 32) * 4096 + (c & 1) * 2048);
      const int vo = r31 * 4096 + (2 * wid + hh) * 128;
      touch4_nt(rc, dum, vo, 0); touch4_nt(rc, dum, vo, 1024);
    }
  };
  if (K2PF_POS == 0) touches();
  {
    const char* sl = lds;
    BAR(); SB(); ST(1, 1);
    if (K2PF_POS == 1) touches();
    RD(F0, 0); RD(F0, 1); RD(F0, 2); RD(F0, 3); SB();
    WAIT_L0(); SB();
  }
  for (int u = 1; u < 15; u += 2) {
    if ((K2PF & 4) && u == 7) {
      const i32x4 rh = mk_rsrc(ws + WS_H + ((size_t)((rt * 4 + 2 * (wid & 1) + (c >> 1)) * NSUB) << 10) + j * 22528);
      const int l2 = (wid >> 1) * 88 + lane;
      touch4(rh, dum, l2 * 128, 0);
      touch4(rh, dum, (lane < 24 ? l2 + 64 : l2) * 128, 0);
      const i32x4 rw = mk_rsrc(ws + WS_W2 + ((size_t)((j * 4 + 2 * (wid & 1) + (c & 1)) * NSUB) << 10) + (idx >> 2) * 11264);
      touch4(rw, dum, ((wid >> 1) * 44 + (lane < 44 ? lane : 0)) * 128, 0);
    }
    PERIOD(u, RD, F1, F0);
    PERIOD(u + 1, RD, F0, F1);
  }
  PERIOD(15, RD, F1, F0);
  PERIOD(16, RDO, F0, F1);
  ST(1, 2);
  const int tile = rt * 4 + j;
  const auto rs = __builtin_amdgcn_make_buffer_rsrc(ws + WS_SLAB + ((size_t)tile << 17), 0, (ABL & 16) ? 0 : (1 << 17), 0x00020000);
  const unsigned lo = wid * 2048 + lane * 16;
#pragma unroll
  for (int q = 0; q < 4; ++q) {
    if (q != c) {
      const f32x16 v = acc[q >> 1][q & 1];
#pragma unroll
      for (int hf = 0; hf < 2; ++hf) {
        const u32x4 w = {pk_bf16(v[hf * 8], v[hf * 8 + 1]), pk_bf16(v[hf * 8 + 2], v[hf * 8 + 3]), pk_bf16(v[hf * 8 + 4], v[hf * 8 + 5]), pk_bf16(v[hf * 8 + 6], v[hf * 8 + 7])};
        __builtin_amdgcn_raw_buffer_store_b128(w, rs, (q * 4 + c) * 8192 + lo + hf * 1024, 0, 16);
      }
    }
  }
  const f32x16 own = c == 0 ? acc[0][0] : (c == 1 ? acc[0][1] : (c == 2 ? acc[1][0] : acc[1][1]));
  const int trow = rt * 128 + wm * 64 + (c >> 1) * 32 + r31;
  const int ecol = j * 128 + wn * 64 + (c & 1) * 32 + 8 * hh;
  u32x4 xv[2], bv[2];
#pragma unroll
  for (int half = 0; half < 2; ++half) { xv[half] = *(const u32x4*)(x + (size_t)trow * D_ + ecol + 16 * half); bv[half] = *(const u32x4*)(b2 + ecol + 16 * half); }
  SB();
  unsigned* cnt = (unsigned*)(ws + WS_CNT + tile * 256);
  PERIODO(17, F1, F0);
  PERIODO(18, F0, F1);
  PERIODO(19, F1, F0);
  WAIT_V(4);
  ST(1, 3);
  PERIODO(20, F0, F1);
  if (tid == 0) __hip_atomic_fetch_add(cnt, 1u, __ATOMIC_RELAXED, __HIP_MEMORY_SCOPE_AGENT);
  PERIODO(21, F1, F0);
  PERIODO(22, F0, F1);
  if (tid == 0) {
    unsigned spins = 0;
    while (__hip_atomic_load(cnt, __ATOMIC_RELAXED, __HIP_MEMORY_SCOPE_AGENT) < 4u) { __builtin_amdgcn_s_sleep(1); if (++spins > (1u << 22)) break; }
  }
  asm volatile("" ::: "memory");
  ST(1, 4);
  PERIODO(23, F1, F0);
  asm volatile("" ::: "memory");
  u32x4 pv[4][2];
#pragma unroll
  for (int s = 0; s < 4; ++s)
#pragma unroll
    for (int hf = 0; hf < 2; ++hf)
      pv[s][hf] = __builtin_amdgcn_raw_buffer_load_b128(rs, (c * 4 + s) * 8192 + lo + hf * 1024, 0, 16);
  SB();
  PERIODO(24, F0, F1);
  PERIODO(25, F1, F0);
  PERIODO(26, F0, F1);
  MO(F0, 0); MO(F0, 1); MO(F0, 2); MO(F0, 3);
  ST(1, 6);
#pragma unroll
  for (int half = 0; half < 2; ++half) {
    float o[8];
#pragma unroll
    for (int e = 0; e < 8; ++e) {
      const int rg = half * 8 + e;
      float p[4];
#pragma unroll
      for (int s = 0; s < 4; ++s) { const unsigned w = pv[s][half][e >> 1]; p[s] = c == s ? own[rg] : ((e & 1) ? bfhi(w) : bflo(w)); }
      const float s = (p[0] + p[1]) + (p[2] + p[3]);
      const unsigned xw = xv[half][e >> 1], bw = bv[half][e >> 1];
      const float xx = (e & 1) ? bfhi(xw) : bflo(xw), bb2 = (e & 1) ? bfhi(bw) : bflo(bw);
      const float v = acco[rg] + bb2;
      const float g = v * __builtin_amdgcn_rcpf(1.f + __builtin_amdgcn_exp2f(-1.442695041f * v));
      o[e] = xx + s * g;
    }
    u32x4 ov = {pk_bf16(o[0], o[1]), pk_bf16(o[2], o[3]), pk_bf16(o[4], o[5]), pk_bf16(o[6], o[7])};
    *(u32x4*)(out + (size_t)trow * D_ + ecol + 16 * half) = ov;
  }
  ST(1, 5); ST_FLUSH(1);
}

extern "C" void kernel_launch(void* const* d_in, const int* in_sizes, int n_in, void* d_out, int out_size, void* d_ws, size_t ws_size,
                              hipStream_t stream) {
  const u16* x = (const u16*)d_in[0]; const u16* cre = (const u16*)d_in[1]; const u16* cim = (const u16*)d_in[2];
  const u16* wre = (const u16*)d_in[3]; const u16* wim = (const u16*)d_in[4];
  const u16* w1 = (const u16*)d_in[6]; const u16* b1 = (const u16*)d_in[7]; const u16* w2 = (const u16*)d_in[8]; const u16* b2 = (const u16*)d_in[9];
  (void)hipFuncSetAttribute((const void*)k1, hipFuncAttributeMaxDynamicSharedMemorySize, 163840);
  (void)hipFuncSetAttribute((const void*)k2, hipFuncAttributeMaxDynamicSharedMemorySize, 132096);
  k1<<<256, 512, 163840, stream>>>(x, wre, wim, w1, b1, w2, cre, cim, (char*)d_ws);
  k2<<<256, 512, 132096, stream>>>(x, cre, cim, b2, (char*)d_ws, (u16*)d_out);
}
```

```cpp
#include <hip/hip_runtime.h>
#include <hip/hip_bf16.h>
#include <stdint.h>

#define ABL 0
typedef unsigned short u16;
typedef short v4s __attribute__((ext_vector_type(4)));
typedef short bf16x8 __attribute__((ext_vector_type(8)));
typedef __bf16 v2bf __attribute__((ext_vector_type(2)));
typedef float f32x4 __attribute__((ext_vector_type(4)));
typedef float f32x16 __attribute__((ext_vector_type(16)));
typedef unsigned u32x4 __attribute__((ext_vector_type(4)));
typedef unsigned u32x2 __attribute__((ext_vector_type(2)));
#define LAS __attribute__((address_space(3)))
#define GAS __attribute__((address_space(1)))

constexpr int S_ = 2048, D_ = 512, F_ = 1376;
constexpr int NKK_B = 64;
constexpr int NSUB = 88;
constexpr size_t WS_B    = 0;
constexpr size_t WS_W2   = 4u << 20;
constexpr size_t WS_H    = 6u << 20;
constexpr size_t WS_SLAB = 12u << 20;
constexpr size_t WS_CNT  = (11u << 20) + (768u << 10);

__device__ __forceinline__ float bf2f(u16 v) { return __uint_as_float(((unsigned)v) << 16); }
__device__ __forceinline__ float bflo(unsigned v) { return __uint_as_float(v << 16); }
__device__ __forceinline__ float bfhi(unsigned v) { return __uint_as_float(v & 0xffff0000u); }
__device__ __forceinline__ unsigned pk_bf16(float lo, float hi) {
  typedef float f32x2_ __attribute__((ext_vector_type(2)));
  v2bf r = __builtin_convertvector((f32x2_){lo, hi}, v2bf);
  return __builtin_bit_cast(unsigned, r);
}
__device__ __forceinline__ float rbf(float f) { return bflo(pk_bf16(f, 0.f)); }
__device__ __forceinline__ int swap23(int r) { return (r & ~12) | ((r & 4) << 1) | ((r & 8) >> 1); }
__device__ __forceinline__ float dot2(unsigned a, unsigned b, float c) {
  return __builtin_amdgcn_fdot2_f32_bf16(__builtin_bit_cast(v2bf, a), __builtin_bit_cast(v2bf, b), c, false);
}
#define GLDS16(g, l) __builtin_amdgcn_global_load_lds((const GAS void*)(g), (LAS void*)(l), 16, 0, 0)
typedef int i32x4 __attribute__((ext_vector_type(4)));
__device__ __forceinline__ i32x4 mk_rsrc(const void* p) {
  const unsigned long long a = (unsigned long long)p;
  return (i32x4){(int)(unsigned)a, (int)((unsigned)(a >> 32) & 0xffffu), 0x7fffffff, 0x00020000};
}
__device__ __forceinline__ void dma16(i32x4 rsrc, unsigned lds_dst, int voff, int soff) {
  unsigned keep;
  asm volatile("s_mov_b32 %0, m0\n\ts_mov_b32 m0, %1\n\ts_nop 0\n\tbuffer_load_dwordx4 %2, %3, %4 offen lds\n\ts_mov_b32 m0, %0"
               : "=&s"(keep) : "s"(lds_dst), "v"(voff), "s"(rsrc), "s"(soff) : "memory");
}
__device__ __forceinline__ void touch4(i32x4 rsrc, unsigned lds_dst, int voff, int soff) {
  unsigned keep;
  asm volatile("s_mov_b32 %0, m0\n\ts_mov_b32 m0, %1\n\ts_nop 0\n\tbuffer_load_dword %2, %3, %4 offen lds\n\ts_mov_b32 m0, %0"
               : "=&s"(keep) : "s"(lds_dst), "v"(voff), "s"(rsrc), "s"(soff) : "memory");
}
template <int OFF> __device__ __forceinline__ void dma16x(i32x4 rsrc, unsigned lds_dst, int voff, int soff) {
  unsigned keep;
  asm volatile("s_mov_b32 %0, m0\n\ts_mov_b32 m0, %1\n\ts_nop 3\n\tbuffer_load_dwordx4 %2, %3, %4 offen offset:%5 lds\n\ts_mov_b32 m0, %0"
               : "=&s"(keep) : "s"(lds_dst), "v"(voff), "s"(rsrc), "s"(soff), "n"(OFF) : "memory");
}
template <int OFF> __device__ __forceinline__ void dma16x_nt(i32x4 rsrc, unsigned lds_dst, int voff, int soff) {
  unsigned keep;
  asm volatile("s_mov_b32 %0, m0\n\ts_mov_b32 m0, %1\n\ts_nop 3\n\tbuffer_load_dwordx4 %2, %3, %4 offen offset:%5 nt lds\n\ts_mov_b32 m0, %0"
               : "=&s"(keep) : "s"(lds_dst), "v"(voff), "s"(rsrc), "s"(soff), "n"(OFF) : "memory");
}
__device__ __forceinline__ void touch4_nt(i32x4 rsrc, unsigned lds_dst, int voff, int soff) {
  unsigned keep;
  asm volatile("s_mov_b32 %0, m0\n\ts_mov_b32 m0, %1\n\ts_nop 0\n\tbuffer_load_dword %2, %3, %4 offen nt lds\n\ts_mov_b32 m0, %0"
               : "=&s"(keep) : "s"(lds_dst), "v"(voff), "s"(rsrc), "s"(soff) : "memory");
}
#define WAIT_V(n) asm volatile("s_waitcnt vmcnt(%0)" ::"n"(n) : "memory")
#define WAIT_L0() asm volatile("s_waitcnt lgkmcnt(0)" ::: "memory")
#define BAR() __builtin_amdgcn_s_barrier()
#define ST_DECL
#define ST(k, i)
#define ST_FLUSH(k)

#define AUX_PF 2
#define PF_ON 0
#define PF_AT 3
#define AUX_ST 16
#define TOUCH 2
typedef float f32x2 __attribute__((ext_vector_type(2)));
constexpr int SL1 = 0, SL2 = 33280, SL3 = 98304, SL4 = 0, SLP = 131072, AROW = 1040;
constexpr int SCR = 66560;
constexpr int SC_PART = SCR;
constexpr int SC_HS   = SCR + 2048;
constexpr int SC_RS   = SCR + 3072;
constexpr int SC_MRS  = SCR + 3584;
constexpr int SC_CS   = SCR + 4096;
constexpr int SC_B1   = SCR + 7168;
constexpr int SC_DUM  = SCR + 8192;
#define PIN(x) asm volatile("" : "+v"(x))
#define SBAR() __builtin_amdgcn_sched_barrier(0)

template <int K> __device__ __forceinline__ void eh_op(const f32x16& ap, float (&v)[16], float (&t)[16], unsigned (&ow)[8], float scale, float center) {
  constexpr int st = K / 16, e = K % 16;
  if constexpr (st == 0) { v[e] = fmaf(-center, t[e], v[e]); PIN(v[e]); }
  else if constexpr (st == 1) { v[e] = fmaf(ap[e], scale, v[e]); PIN(v[e]); }
  else if constexpr (st == 2) { t[e] = v[e] * v[e]; PIN(t[e]); }
  else if constexpr (st == 3) { t[e] = fmaf(t[e], -0.1029432397f, -2.302208198f); PIN(t[e]); }
  else if constexpr (st == 4) { t[e] = t[e] * v[e]; PIN(t[e]); }
  else if constexpr (st == 5) { t[e] = __builtin_amdgcn_exp2f(t[e]); PIN(t[e]); }
  else if constexpr (st == 6) { t[e] = 1.f + t[e]; PIN(t[e]); }
  else if constexpr (st == 7) { t[e] = __builtin_amdgcn_rcpf(t[e]); PIN(t[e]); }
  else if constexpr (st == 8) { v[e] = v[e] * t[e]; PIN(v[e]); }
  else if constexpr (K < 152) { ow[e] = pk_bf16(v[2 * e], v[2 * e + 1]); PIN(ow[e]); }
}
template <int K> __device__ __forceinline__ void ea_op(const f32x16& ap, float (&v)[16], float (&t)[16], unsigned (&ow)[8], float cs) {
  constexpr int st = K / 16, e = K % 16;
  if constexpr (st == 0) { t[e] = ap[e] * t[e]; PIN(t[e]); }
  else if constexpr (st == 1) { t[e] = fmaf(-v[e], cs, t[e]); PIN(t[e]); }
  else if constexpr (K < 40) { ow[e] = pk_bf16(t[2 * e], t[2 * e + 1]); PIN(ow[e]); }
}
template <int ET, int K0, int K1> __device__ __forceinline__ void e_ops(const f32x16& ap, float (&v)[16], float (&t)[16], unsigned (&ow)[8], float s0, float s1) {
  if constexpr (K0 < K1) {
    if constexpr (ET == 1) eh_op<K0>(ap, v, t, ow, s0, s1); else if constexpr (ET == 2) ea_op<K0>(ap, v, t, ow, s0);
    e_ops<ET, K0 + 1, K1>(ap, v, t, ow, s0, s1);
  }
}
template <int MT> __device__ __forceinline__ bf16x8 wread(const char* pan, int ks) {
  if constexpr (MT == 1) { return *(const bf16x8*)(pan + ks * 32); }
  else {
    v4s t0 = __builtin_amdgcn_ds_read_tr16_b64_v4i16((LAS v4s*)(pan + ks * 1024));
    v4s t1 = __builtin_amdgcn_ds_read_tr16_b64_v4i16((LAS v4s*)(pan + ks * 1024 + 256));
    return (bf16x8)__builtin_shufflevector(t0, t1, 0, 1, 2, 3, 4, 5, 6, 7);
  }
}
__device__ __forceinline__ void row_stats(float sum, float sq, float& mean, float& inv, float& m_bf, float& scale, float& center) {
  mean = sum * (1.f / 512.f);
  const float var = fmaxf(sq * (1.f / 512.f) - mean * mean, 0.f);
  m_bf = rbf(mean);
  const float sd = rbf(sqrtf(rbf(rbf(var) + 1e-5f)));
  inv = 1.f / sd;
  const float m2 = rbf((mean - m_bf) * inv);
  const float v2 = rbf(var * inv * inv);
  const float sd2 = rbf(sqrtf(rbf(v2 + 1e-5f)));
  scale = inv / sd2; center = (m_bf + m2 * sd) * scale;
}
__device__ __forceinline__ unsigned page_touch(int lane, const void* x, const void* cre, const void* cim, const void* wre, const void* wim, const void* w1,
                                               const void* b1, const void* w2, const void* b2, const void* ws, const void* out) {
  const char* p; int k, last;
  if (lane < 20) { p = (const char*)ws; k = lane; last = (20 << 20) - 4; }
  else if (lane < 29) { p = (const char*)cre; k = lane - 20; last = (8 << 20) - 4; }
  else if (lane < 38) { p = (const char*)cim; k = lane - 29; last = (8 << 20) - 4; }
  else if (lane < 41) { p = (const char*)x; k = lane - 38; last = (2 << 20) - 4; }
  else if (lane < 44) { p = (const char*)out; k = lane - 41; last = (2 << 20) - 4; }
  else if (lane < 47) { p = (const char*)w1; k = lane - 44; last = D_ * F_ * 2 - 4; }
  else if (lane < 50) { p = (const char*)w2; k = lane - 47; last = D_ * F_ * 2 - 4; }
  else if (lane < 52) { p = (const char*)wre; k = lane - 50; last = D_ * D_ * 2 - 4; }
  else if (lane < 54) { p = (const char*)wim; k = lane - 52; last = D_ * D_ * 2 - 4; }
  else if (lane < 55) { p = (const char*)b1; k = 0; last = 0; }
  else { p = (const char*)b2; k = 0; last = 0; }
  const int off = (k << 20) < last ? (k << 20) : last;
  return *(const volatile unsigned*)(p + off);
}
constexpr int WR = 6;
template <int MT, int ET, int KS> __device__ __forceinline__ void unit_steps(const char* pan, const bf16x8 (&xf)[32], bf16x8 (&wf)[WR], f32x16& acc,
                                                                          const f32x16& ap, float (&v)[16], float (&t)[16], unsigned (&ow)[8], float s0, float s1) {
  if constexpr (KS < 32) {
    constexpr int NOPS = ET == 1 ? 152 : (ET == 2 ? 40 : 0);
    constexpr int G0 = 2;
    if constexpr (MT == 1) acc = __builtin_amdgcn_mfma_f32_32x32x16_bf16(xf[KS], wf[KS % WR], acc, 0, 0, 0);
    else acc = __builtin_amdgcn_mfma_f32_32x32x16_bf16(wf[KS % WR], xf[KS], acc, 0, 0, 0);
    PIN(acc);
    SBAR();
    if constexpr (KS + WR - 1 < 32) wf[(KS + WR - 1) % WR] = wread<MT>(pan, KS + WR - 1);
    SBAR();
    constexpr int k0 = KS < G0 ? 0 : (KS - G0) * NOPS / (32 - G0), k1 = KS < G0 ? 0 : (KS + 1 - G0) * NOPS / (32 - G0);
    e_ops<ET, k0, k1>(ap, v, t, ow, s0, s1);
    SBAR();
    unit_steps<MT, ET, KS + 1>(pan, xf, wf, acc, ap, v, t, ow, s0, s1);
  }
}

__global__ __launch_bounds__(512, 2) void k1(const u16* __restrict__ x, const u16* __restrict__ wre, const u16* __restrict__ wim,
                                             const u16* __restrict__ w1, const u16* __restrict__ b1, const u16* __restrict__ w2,
                                             const u16* __restrict__ cre, const u16* __restrict__ cim, char* __restrict__ ws,
                                             const u16* __restrict__ b2, const u16* __restrict__ outp) {
  extern __shared__ __attribute__((aligned(16))) char lds[];
  const int tid = threadIdx.x, lane = tid & 63, wid = __builtin_amdgcn_readfirstlane(tid >> 6);
  const int b = blockIdx.x, xcd = b & 7, idx = b >> 3;
  const int rt = (xcd >> 1) * 4 + (idx & 3);
  const int cg = (xcd & 1) * 8 + (idx >> 2);
  const int h0 = cg < 11 ? cg * 3 : 33 + (cg - 11) * 2;
  const bool h3 = cg < 11;
  const int a0 = cg * 2;
  const int m = wid & 3;
  const bool helper = wid >= 4;
  const int r31 = lane & 31, hh = lane >> 5;
  const unsigned lds0 = (unsigned)(size_t)(LAS char*)lds;
  const int prot = idx & 3;
  ST_DECL;
  ST(0, 0);

  const int hoff = (lane >> 2) * (F_ * 2) + (lane & 3) * 16;
  const i32x4 rw1 = mk_rsrc(w1);
  auto piece_h = [&](int u, int q, int slot_off) { dma16(rw1, lds0 + slot_off + q * 1024, hoff, q * 16 * (F_ * 2) + u * 64); };
  const i32x4 rx = mk_rsrc(x + (size_t)(rt * 128 + m * 32) * D_);
  int xo[4];
#pragma unroll
  for (int i = 0; i < 4; ++i) { const int r = i * 4 + (lane >> 4); xo[i] = r * 1024 + (((lane & 15) ^ r) * 16); }
  auto quarter = [&](int q) {
#pragma unroll
    for (int i = 0; i < 8; ++i) dma16(rx, lds0 + q * 32768 + m * 8192 + i * 1024, xo[i & 3], q * 256 + (i >> 2) * 16384);
    piece_h(h0, q * 8 + m * 2, SLP); piece_h(h0, q * 8 + m * 2 + 1, SLP);
  };
  if (helper) { quarter(0); quarter(1); }

  unsigned w2v[8], b1v;
  const int w2chunk = b * 352 + tid;
  {
    i32x4 rw2 = mk_rsrc(w2); rw2.z = F_ * D_ * 2;
    const int c = w2chunk;
    const int ln = c & 63, rest = c >> 6;
    const int ct = rest / NSUB, sub = rest - ct * NSUB;
    const int d = 32 * ct + swap23(ln & 31);
    const int fb = 16 * sub + 4 * (ln >> 5);
    const int off = tid < 352 ? (fb * D_ + d) * 2 : 0x7ff00000;
    const int bo = h0 * 32 + lane * 2;
    const i32x4 rb1 = mk_rsrc(b1);
    asm volatile("buffer_load_ushort %0, %9, %11, 0 offen\n\tbuffer_load_ushort %1, %9, %11, 0 offen offset:1024\n\t"
                 "buffer_load_ushort %2, %9, %11, 0 offen offset:2048\n\tbuffer_load_ushort %3, %9, %11, 0 offen offset:3072\n\t"
                 "buffer_load_ushort %4, %10, %11, 0 offen\n\tbuffer_load_ushort %5, %10, %11, 0 offen offset:1024\n\t"
                 "buffer_load_ushort %6, %10, %11, 0 offen offset:2048\n\tbuffer_load_ushort %7, %10, %11, 0 offen offset:3072\n\t"
                 "buffer_load_dword %8, %12, %13, 0 offen"
                 : "=&v"(w2v[0]), "=&v"(w2v[1]), "=&v"(w2v[2]), "=&v"(w2v[3]), "=&v"(w2v[4]), "=&v"(w2v[5]), "=&v"(w2v[6]), "=&v"(w2v[7]), "=&v"(b1v)
                 : "v"(off), "v"(off + 8192), "s"(rw2), "v"((bo < F_ - 2 ? bo : F_ - 2) * 2), "s"(rb1) : "memory");
  }
#define REGS_LANDED() asm volatile("" : "+v"(w2v[0]), "+v"(w2v[1]), "+v"(w2v[2]), "+v"(w2v[3]), "+v"(w2v[4]), "+v"(w2v[5]), "+v"(w2v[6]), "+v"(w2v[7]), "+v"(b1v))
  const auto rws = __builtin_amdgcn_make_buffer_rsrc((void*)ws, 0, 0x7fffffff, 0x00020000);
  const int xrow = m * 32 + swap23(r31);
  const unsigned xbase = xrow * 256 + ((hh ^ (xrow & 15)) * 16);
#define QWAIT(q) WAIT_V(9 + 10 * (3 - (q)))

  if (helper) {
    const i32x4 rwa = mk_rsrc((a0 < 16 ? wre : wim) + (size_t)(a0 & 15) * 32 * D_);
    auto panel_h = [&](int u, int slot_off) {
#pragma unroll
      for (int k = 0; k < 8; ++k) piece_h(u, m * 8 + ((k + 2 * prot) & 7), slot_off);
    };
    auto panel_a = [&](int w, int slot_off) {
#pragma unroll
      for (int k = 0; k < 8; ++k) { const int q = m * 8 + ((k + 2 * prot) & 7); dma16(rwa, lds0 + slot_off + q * AROW, lane * 16, (w * 32 + q) * 1024); }
    };
    const v4s ones = {0x3f80, 0x3f80, 0x3f80, 0x3f80};
    const char* pzh = lds + ((lane >> 2) & 15) * 64 + (m & 1) * 32 + (lane & 3) * 8;
    auto cs_h_part = [&](f32x4 (&c)[4], int slot_off, int j0, int n) {
#pragma unroll
      for (int j = 0; j < n; ++j)
        c[j & 3] = __builtin_amdgcn_mfma_f32_4x4x4bf16_1k(ones, __builtin_amdgcn_ds_read_tr16_b64_v4i16((LAS v4s*)(pzh + slot_off + (j0 + j) * 1024)), c[j & 3], 0, 0, 0);
    };
    auto cs_h_fin = [&](f32x4 (&c)[4], int i) {
      float r = (c[0][0] + c[1][0]) + (c[2][0] + c[3][0]);
      r += __shfl_xor(r, 16); r += __shfl_xor(r, 32);
      if (lane < 16) *(float*)(lds + SC_CS + i * 512 + (m & 1) * 64 + lane * 4) = r;
    };
    auto cs_h = [&](int i, int slot_off) {
      if ((m >> 1) == (i & 1)) { f32x4 c[4] = {}; cs_h_part(c, slot_off, 0, 32); cs_h_fin(c, i); }
    };
    auto cs_a = [&](int i, int slot_off) {
      const char* pz = lds + slot_off + swap23(r31) * AROW + hh * 16 + m * 256;
      f32x4 c0 = {}, c1 = {};
#pragma unroll
      for (int j = 0; j < 8; ++j) {
        const bf16x8 w = *(const bf16x8*)(pz + j * 32);
        c0 = __builtin_amdgcn_mfma_f32_4x4x4bf16_1k(ones, __builtin_shufflevector(w, w, 0, 1, 2, 3), c0, 0, 0, 0);
        c1 = __builtin_amdgcn_mfma_f32_4x4x4bf16_1k(ones, __builtin_shufflevector(w, w, 4, 5, 6, 7), c1, 0, 0, 0);
      }
      float r = c0[0] + c1[0]; r += __shfl_xor(r, 32);
      if (lane < 32) *(float*)(lds + SC_CS + i * 512 + m * 128 + lane * 4) = r;
    };
    auto prefetch_c = [&]() {
      const int c2 = xcd >> 1;
      const int row = (xcd & 1) * 1024 + idx * 32 + m * 8 + (lane >> 4);
      const i32x4 rc = mk_rsrc(c2 < 2 ? cre : cim);
      const int vo = row * 4096 + (lane & 15) * 128;
      if (AUX_PF == 2) { touch4_nt(rc, lds0 + SC_DUM + m * 256, vo, (c2 & 1) * 2048); touch4_nt(rc, lds0 + SC_DUM + m * 256, vo, (c2 & 1) * 2048 + 4 * 4096); }
      else { touch4(rc, lds0 + SC_DUM + m * 256, vo, (c2 & 1) * 2048); touch4(rc, lds0 + SC_DUM + m * 256, vo, (c2 & 1) * 2048 + 4 * 4096); }
    };
    __builtin_amdgcn_s_setprio(2);
    {
      f32x4 c[4] = {};
      WAIT_V(19); ST(0, 1); BAR();
      quarter(2);
      if (m < 2) cs_h_part(c, SLP, 0, 8);
      WAIT_V(10); BAR();
      quarter(3);
      panel_h(h0 + 1, SL1);
      if (m < 2) cs_h_part(c, SLP, 8, 8);
      WAIT_V(18); BAR();
      if (m < 2) cs_h_part(c, SLP, 16, 8);
      WAIT_V(8); BAR();
      ST(0, 2);
      panel_a(0, SL2);
      if (m < 2) { cs_h_part(c, SLP, 24, 8); cs_h_fin(c, 0); }
    }
    REGS_LANDED();
    if (m == 3 && lane < 48) *(f32x2*)(lds + SC_B1 + lane * 8) = (f32x2){bflo(b1v), bfhi(b1v)};
    if (tid < 352) {
      u32x4 v = {w2v[0] | (w2v[1] << 16), w2v[2] | (w2v[3] << 16), w2v[4] | (w2v[5] << 16), w2v[6] | (w2v[7] << 16)};
      __builtin_amdgcn_raw_buffer_store_b128(v, rws, w2chunk * 16, (int)WS_W2, AUX_ST);
    } else { __builtin_amdgcn_raw_buffer_store_b128((u32x4){0u, 0u, 0u, 0u}, rws, 0x7ffffff0, 0x7ffffff0, AUX_ST); }
    ST(0, 3);
    WAIT_V(9); WAIT_L0(); BAR();
    if (h3) panel_h(h0 + 2, SL3); else panel_a(1, SL3);
    cs_h(1, SL1);
    {
      float mean, inv, m_bf, sc_, ce_;
      const f32x2 pr = *(const f32x2*)(lds + SC_PART + (m * 32 + r31) * 8);
      row_stats(pr.x, pr.y, mean, inv, m_bf, sc_, ce_);
      const float sg = a0 < 16 ? inv : -inv;
      if (lane < 32) { *(float*)(lds + SC_RS + (m * 32 + lane) * 4) = sg; *(float*)(lds + SC_MRS + (m * 32 + lane) * 4) = m_bf * sg; }
    }
    WAIT_V(8); WAIT_L0(); BAR();
    if (h3) panel_a(1, SL4);
    if (PF_ON && PF_AT == 3) prefetch_c();
    cs_a(2, SL2);
    if (PF_ON && PF_AT == 3) { if (h3) WAIT_V(10); else WAIT_V(2); } else { if (h3) WAIT_V(8); else WAIT_V(0); }
    WAIT_L0(); BAR();
    if (PF_ON && PF_AT == 4) prefetch_c();
    if (h3) {
      cs_h(3, SL3);
      WAIT_V(PF_ON ? 2 : 0); WAIT_L0(); BAR();
      cs_a(4, SL4);
    } else cs_a(3, SL3);
    WAIT_L0(); BAR();
    WAIT_V(0);
    ST(0, 5); ST_FLUSH(0);
    return;
  }

  bf16x8 xf[32];
  unsigned long long clk0 = 0; (void)clk0;
  const int wrow = swap23(r31);
  const unsigned wlA = (unsigned)(wrow * AROW + hh * 16);
  const unsigned wlH = (unsigned)((8 * hh + ((lane & 15) >> 2)) * 64 + ((lane >> 4) & 1) * 32 + (lane & 3) * 8);
  f32x16 acc, ap = {};
  bf16x8 wf[WR];
  float v[16], t[16]; unsigned ow[8];
  float scale = 0.f, center = 0.f;
  const int hstore = (swap23(r31) + 32 * hh) * 16;
  const int s0r = rt * 128 + m * 32;

  auto tables_h = [&](int i, int hu) {
#pragma unroll
    for (int g4 = 0; g4 < 4; ++g4) {
      const f32x4 c = *(const f32x4*)(lds + SC_CS + i * 512 + (8 * g4 + 4 * hh) * 4), bq = *(const f32x4*)(lds + SC_B1 + hu * 128 + (8 * g4 + 4 * hh) * 4);
#pragma unroll
      for (int e = 0; e < 4; ++e) { t[g4 * 4 + e] = c[e]; v[g4 * 4 + e] = bq[e]; }
    }
  };
  auto tables_a = [&](int i) -> float {
#pragma unroll
    for (int g4 = 0; g4 < 4; ++g4) {
      const f32x4 c = *(const f32x4*)(lds + SC_RS + (m * 32 + 8 * g4 + 4 * hh) * 4), bq = *(const f32x4*)(lds + SC_MRS + (m * 32 + 8 * g4 + 4 * hh) * 4);
#pragma unroll
      for (int e = 0; e < 4; ++e) { t[g4 * 4 + e] = c[e]; v[g4 * 4 + e] = bq[e]; }
    }
    const char* cq = lds + SC_CS + i * 512 + r31 * 4;
    return (*(const float*)cq + *(const float*)(cq + 128)) + (*(const float*)(cq + 256) + *(const float*)(cq + 384));
  };
  auto store_h = [&](int hu) {
    const int dst = (int)WS_H + (((rt * 4 + m) * NSUB + 2 * (h0 + hu)) << 10);
    __builtin_amdgcn_raw_buffer_store_b128((u32x4){ow[0], ow[1], ow[2], ow[3]}, rws, hstore, dst, AUX_ST);
    __builtin_amdgcn_raw_buffer_store_b128((u32x4){ow[4], ow[5], ow[6], ow[7]}, rws, hstore + 1024, dst, AUX_ST);
    if (h0 + hu == 42) { u32x4 z = {0u, 0u, 0u, 0u}; __builtin_amdgcn_raw_buffer_store_b128(z, rws, lane * 16 + 2048, dst, AUX_ST); __builtin_amdgcn_raw_buffer_store_b128(z, rws, lane * 16 + 3072, dst, AUX_ST); }
  };
  auto store_a = [&](int w) {
    const int u = a0 + w, et = u & 15, j = et >> 2, nt = et & 3;
    const int kk = (u >> 4) * 32 + (s0r >> 6), sub = (s0r & 63) >> 4;
    const int dst = (int)WS_B + (((j * NKK_B + kk) * 16 + nt * 4 + sub) << 10);
    __builtin_amdgcn_raw_buffer_store_b128((u32x4){ow[0], ow[1], ow[2], ow[3]}, rws, lane * 16, dst, AUX_ST);
    __builtin_amdgcn_raw_buffer_store_b128((u32x4){ow[4], ow[5], ow[6], ow[7]}, rws, lane * 16 + 1024, dst, AUX_ST);
  };
#define UNIT(MT, ET, slot, s0, s1) do { const char* pan = lds + (slot) + (MT ? wlA : wlH); acc = (f32x16){}; \
    _Pragma("unroll") for (int e = 0; e < WR - 1; ++e) wf[e] = wread<MT>(pan, e); \
    unit_steps<MT, ET, 0>(pan, xf, wf, acc, ap, v, t, ow, s0, s1); } while (0)
#define TLB 3
  unsigned ptv = 0;
  if ((TLB & 1) && wid == 0) ptv = page_touch(lane, x, cre, cim, wre, wim, w1, b1, w2, b2, ws, outp);
  {
    float ss0 = 0.f, ss1 = 0.f, sq0 = 0.f, sq1 = 0.f;
    acc = (f32x16){};
    const char* pan = lds + SLP + wlH;
#pragma unroll
    for (int q = 0; q < 4; ++q) {
      if (q == 0) ST(0, 1);
      BAR();
      if (q == 3) ST(0, 2);
      bf16x8 w8[8];
#pragma unroll
      for (int k = 0; k < 8; ++k) xf[q * 8 + k] = *(const bf16x8*)(lds + q * 32768 + (xbase ^ (k * 32)));
#pragma unroll
      for (int k = 0; k < 8; ++k) w8[k] = wread<0>(pan, q * 8 + k);
      SBAR();
#pragma unroll
      for (int k = 0; k < 8; ++k) {
        const u32x4 v4 = __builtin_bit_cast(u32x4, xf[q * 8 + k]);
        ss0 = dot2(v4.x, 0x3f803f80u, ss0); sq0 = dot2(v4.x, v4.x, sq0); ss1 = dot2(v4.y, 0x3f803f80u, ss1); sq1 = dot2(v4.y, v4.y, sq1);
        ss0 = dot2(v4.z, 0x3f803f80u, ss0); sq0 = dot2(v4.z, v4.z, sq0); ss1 = dot2(v4.w, 0x3f803f80u, ss1); sq1 = dot2(v4.w, v4.w, sq1);
      }
      asm volatile("" : "+v"(ss0), "+v"(ss1), "+v"(sq0), "+v"(sq1));
      SBAR();
#pragma unroll
      for (int k = 0; k < 8; ++k) { acc = __builtin_amdgcn_mfma_f32_32x32x16_bf16(w8[k], xf[q * 8 + k], acc, 0, 0, 0); PIN(acc); }
      WAIT_L0(); SBAR();
    }
    float sum = ss0 + ss1, sq = sq0 + sq1;
    sum += __shfl_xor(sum, 32); sq += __shfl_xor(sq, 32);
    if (lane < 32) *(f32x2*)(lds + SC_PART + (m * 32 + lane) * 8) = (f32x2){sum, sq};
    float mean, inv, m_bf; row_stats(sum, sq, mean, inv, m_bf, scale, center);
    WAIT_V(0); REGS_LANDED(); PIN(ptv);
    u32x4 v = {w2v[0] | (w2v[1] << 16), w2v[2] | (w2v[3] << 16), w2v[4] | (w2v[5] << 16), w2v[6] | (w2v[7] << 16)};
    __builtin_amdgcn_raw_buffer_store_b128(v, rws, w2chunk * 16, (int)WS_W2, AUX_ST);
    if (b == 0 && wid == 0) *(unsigned*)(ws + WS_CNT + lane * 256) = 0u;
  }
  ST(0, 8);
  WAIT_L0(); BAR(); ap = acc;
  tables_h(0, 0);
  UNIT(0, 1, SL1, scale, center); store_h(0);
  ST(0, 9);
  BAR(); ap = acc;
  tables_h(1, 1);
  UNIT(1, 1, SL2, scale, center); store_h(1);
  ST(0, 10);
  BAR(); ap = acc;
  float csa = tables_a(2);
  if (h3) {
    UNIT(0, 2, SL3, csa, 0.f); store_a(0);
    ST(0, 11);
    BAR(); ap = acc;
    tables_h(3, 2);
    UNIT(1, 1, SL4, scale, center); store_h(2);
  } else {
    UNIT(1, 2, SL3, csa, 0.f); store_a(0);
  }
  ST(0, 12);
  BAR(); ap = acc;
  csa = tables_a(h3 ? 4 : 3);
  e_ops<2, 0, 40>(ap, v, t, ow, csa, 0.f);
  store_a(1);
  WAIT_V(0);
  ST(0, 5); ST_FLUSH(0);
}

#define AUX_C 2
__global__ __launch_bounds__(512, 2) void k2(const u16* __restrict__ x, const u16* __restrict__ cre, const u16* __restrict__ cim,
                                             const u16* __restrict__ b2, char* __restrict__ ws, u16* __restrict__ out) {
  extern __shared__ __attribute__((aligned(16))) char lds[];
  const int tid = threadIdx.x, lane = tid & 63, wid = __builtin_amdgcn_readfirstlane(tid >> 6);
  const int b = blockIdx.x, xcd = b & 7, idx = b >> 3;
#define MAP2 0
  const int c = MAP2 ? (xcd >> 2) * 2 + (idx >> 4) : xcd >> 1, rt = MAP2 ? (xcd & 3) * 4 + ((idx >> 2) & 3) : (xcd & 1) * 8 + (idx >> 2), j = idx & 3;
  ST_DECL;
  ST(1, 0);

#define SB() __builtin_amdgcn_sched_barrier(0)

  if (wid >= 4) {
    const int lw = wid - 4;
    int aoff[4];
#pragma unroll
    for (int i = 0; i < 4; ++i) { int row = lw * 32 + i * 8 + (lane >> 3); aoff[i] = row * 4096 + (((lane & 7) ^ ((row >> 1) & 7)) * 16) - i * 1024 + 4096; }
    const unsigned ldsw = (unsigned)(size_t)(LAS char*)lds + lw * 4096;
    const i32x4 ra_s = mk_rsrc((const char*)(c < 2 ? cre : cim) + (size_t)rt * 128 * 4096 + (c & 1) * 2048 - 4096);
    const i32x4 rb_s = mk_rsrc(ws + WS_B + ((size_t)(j * NKK_B + c * 16) << 14) + lw * 4096);
    const i32x4 ra_o = mk_rsrc(ws + WS_H + ((size_t)((rt * 4 + 2 * (lw >> 1) + (c >> 1)) * NSUB) << 10) + (lw & 1) * 4096 - (16 << 13));
    const i32x4 rb_o = mk_rsrc(ws + WS_W2 + ((size_t)((j * 4 + 2 * (lw >> 1) + (c & 1)) * NSUB) << 10) + (lw & 1) * 4096 - (16 << 13));
    const int voff = lane * 16;
    const int r8 = idx >> 2;
#define LEAD 0
    const int ea = LEAD ? __builtin_amdgcn_readfirstlane(lw == j ? 1 : 0) : 1, eb0 = LEAD ? __builtin_amdgcn_readfirstlane(r8 == 2 * lw ? 1 : 0) : 1,
              eb1 = LEAD ? __builtin_amdgcn_readfirstlane(r8 == 2 * lw + 1 ? 1 : 0) : 1;
    auto issue_a = [&](int T) {
      const unsigned dst = __builtin_amdgcn_readfirstlane(ldsw + (T & 3) * 32768);
      if (T < 16) { const int so = __builtin_amdgcn_readfirstlane(T * 128);
                    dma16x_nt<0>(ra_s, dst, aoff[0], so); dma16x_nt<1024>(ra_s, dst, aoff[1], so); dma16x_nt<2048>(ra_s, dst, aoff[2], so); dma16x_nt<3072>(ra_s, dst, aoff[3], so); }
      else { i32x4 r = ra_o; r.z = __builtin_amdgcn_readfirstlane(T < 27 ? 0x7fffffff : 0); const int so = __builtin_amdgcn_readfirstlane(T << 13);
             dma16x<0>(r, dst, voff, so); dma16x<1024>(r, dst, voff, so); dma16x<2048>(r, dst, voff, so); dma16x<3072>(r, dst, voff, so); }
    };
    auto issue_b = [&](int T, int half) {
      const unsigned dst = __builtin_amdgcn_readfirstlane(ldsw + (T & 3) * 32768 + 16384 + half * 2048);
      i32x4 r = T < 16 ? rb_s : rb_o; r.z = T < 27 ? 0x7fffffff : 0;
      const int so = __builtin_amdgcn_readfirstlane((T < 16 ? T << 14 : T << 13) + half * 2048);
      dma16x<0>(r, dst, voff, so); dma16x<1024>(r, dst, voff, so);
    };
    issue_a(0); issue_b(0, 0); issue_b(0, 1);
    issue_a(1); issue_b(1, 0); issue_b(1, 1);
    if (ea) issue_a(2);
    if (eb0) issue_b(2, 0);
    if (eb1) issue_b(2, 1);
    const int npro = 8 + 4 * ea + 2 * eb0 + 2 * eb1;
    if (npro == 8) WAIT_V(8); else if (npro == 10) WAIT_V(10); else if (npro == 12) WAIT_V(12); else if (npro == 14) WAIT_V(14); else WAIT_V(16);
    for (int u = 0; u < 27; ++u) {
      if (u) WAIT_V(LEAD ? 8 : 16);
      BAR();
      issue_a(u + 2 + ea); issue_b(u + 2 + eb0, 0); issue_b(u + 2 + eb1, 1);
    }
    WAIT_V(0);
    return;
  }

  const int r31 = lane & 31, hh = lane >> 5;
  const int wm = wid >> 1, wn = wid & 1;
  unsigned ab[4];
#pragma unroll
  for (int s = 0; s < 4; ++s) ab[s] = wm * 8192 + (unsigned)(r31 * 128 + (((2 * s + hh) ^ ((r31 >> 1) & 7)) * 16));
  const unsigned bb = 16384 + wn * 8192 + lane * 16;
  const unsigned ao = wm * 8192 + lane * 16;

  f32x16 acc[2][2] = {};
  f32x16 acco = {};
  bf16x8 F0[16], F1[16];
#define RD(F, s) do { F[4 * (s) + 0] = *(const bf16x8*)(sl + ab[s]); F[4 * (s) + 1] = *(const bf16x8*)(sl + ab[s] + 4096); \
                      F[4 * (s) + 2] = *(const bf16x8*)(sl + bb + (s) * 1024); F[4 * (s) + 3] = *(const bf16x8*)(sl + bb + (s) * 1024 + 4096); } while (0)
#define RDO(F, s) do { F[4 * (s) + 0] = *(const bf16x8*)(sl + ao + (s) * 2048); F[4 * (s) + 1] = *(const bf16x8*)(sl + bb + (s) * 2048); \
                       F[4 * (s) + 2] = *(const bf16x8*)(sl + ao + (s) * 2048 + 1024); F[4 * (s) + 3] = *(const bf16x8*)(sl + bb + (s) * 2048 + 1024); } while (0)
#define MM0(F, s) acc[0][0] = __builtin_amdgcn_mfma_f32_32x32x16_bf16(F[4 * (s) + 2], F[4 * (s) + 0], acc[0][0], 0, 0, 0)
#define MM1(F, s) acc[0][1] = __builtin_amdgcn_mfma_f32_32x32x16_bf16(F[4 * (s) + 3], F[4 * (s) + 0], acc[0][1], 0, 0, 0)
#define MM2(F, s) acc[1][0] = __builtin_amdgcn_mfma_f32_32x32x16_bf16(F[4 * (s) + 2], F[4 * (s) + 1], acc[1][0], 0, 0, 0)
#define MM3(F, s) acc[1][1] = __builtin_amdgcn_mfma_f32_32x32x16_bf16(F[4 * (s) + 3], F[4 * (s) + 1], acc[1][1], 0, 0, 0)
#define MO(F, s) do { acco = __builtin_amdgcn_mfma_f32_32x32x16_bf16(F[4 * (s) + 1], F[4 * (s) + 0], acco, 0, 0, 0); \
                      acco = __builtin_amdgcn_mfma_f32_32x32x16_bf16(F[4 * (s) + 3], F[4 * (s) + 2], acco, 0, 0, 0); } while (0)
#define PERIOD(u, RDX, FR, FM) do { \
    const char* sl = lds + ((u) & 3) * 32768; \
    BAR(); SB(); \
    MM0(FM, 0); SB(); RDX(FR, 0); SB(); MM1(FM, 0); SB(); MM2(FM, 0); SB(); RDX(FR, 1); SB(); MM3(FM, 0); SB(); \
    MM0(FM, 1); SB(); RDX(FR, 2); SB(); MM1(FM, 1); SB(); MM2(FM, 1); SB(); RDX(FR, 3); SB(); MM3(FM, 1); SB(); \
    MM0(FM, 2); MM1(FM, 2); MM2(FM, 2); MM3(FM, 2); \
    MM0(FM, 3); MM1(FM, 3); MM2(FM, 3); SB(); WAIT_L0(); SB(); MM3(FM, 3); SB(); \
  } while (0)
#define PERIODO(u, FR, FM) do { \
    const char* sl = lds + ((u) & 3) * 32768; \
    BAR(); SB(); \
    RDO(FR, 0); SB(); MO(FM, 0); SB(); RDO(FR, 1); SB(); MO(FM, 1); SB(); RDO(FR, 2); SB(); MO(FM, 2); SB(); RDO(FR, 3); SB(); MO(FM, 3); SB(); \
    WAIT_L0(); SB(); \
  } while (0)
#define K2PF 0
#define K2PF_POS 0
  const unsigned dum = (unsigned)(size_t)(LAS char*)lds + 131072 + wid * 256;
  auto touches = [&]() {
    if (K2PF & 1) {
      const i32x4 rb = mk_rsrc(ws + WS_B + ((size_t)(j * NKK_B + c * 16) << 14));
      touch4(rb, dum, ((lane >> 4) + 4 * wid) * 16384 + ((idx >> 2) * 16 + (lane & 15)) * 128, 0);
    }
    if (K2PF & 2) {
      const i32x4 rc = mk_rsrc((const char*)(c < 2 ? cre : cim) + (size_t)(rt * 128 + j * 32) * 4096 + (c & 1) * 2048);
      const int vo = r31 * 4096 + (2 * wid + hh) * 128;
      touch4_nt(rc, dum, vo, 0); touch4_nt(rc, dum, vo, 1024);
    }
  };
  unsigned ptv = 0;
  if ((TLB & 2) && wid == 0) ptv = page_touch(lane, x, cre, cim, x, x, x, b2, x, b2, ws, out);
  if (K2PF_POS == 0) touches();
  {
    const char* sl = lds;
    BAR(); SB(); ST(1, 1);
    if (K2PF_POS == 1) touches();
    RD(F0, 0); RD(F0, 1); RD(F0, 2); RD(F0, 3); SB();
    WAIT_L0(); SB();
  }
  for (int u = 1; u < 15; u += 2) {
    if ((K2PF & 4) && u == 7) {
      const i32x4 rh = mk_rsrc(ws + WS_H + ((size_t)((rt * 4 + 2 * (wid & 1) + (c >> 1)) * NSUB) << 10) + j * 22528);
      const int l2 = (wid >> 1) * 88 + lane;
      touch4(rh, dum, l2 * 128, 0);
      touch4(rh, dum, (lane < 24 ? l2 + 64 : l2) * 128, 0);
      const i32x4 rw = mk_rsrc(ws + WS_W2 + ((size_t)((j * 4 + 2 * (wid & 1) + (c & 1)) * NSUB) << 10) + (idx >> 2) * 11264);
      touch4(rw, dum, ((wid >> 1) * 44 + (lane < 44 ? lane : 0)) * 128, 0);
    }
    PERIOD(u, RD, F1, F0);
    PERIOD(u + 1, RD, F0, F1);
  }
  PERIOD(15, RD, F1, F0);
  PERIOD(16, RDO, F0, F1);
  ST(1, 2);
  const int tile = rt * 4 + j;
  const auto rs = __builtin_amdgcn_make_buffer_rsrc(ws + WS_SLAB + ((size_t)tile << 17), 0, (ABL & 16) ? 0 : (1 << 17), 0x00020000);
  const unsigned lo = wid * 2048 + lane * 16;
#pragma unroll
  for (int q = 0; q < 4; ++q) {
    if (q != c) {
      const f32x16 v = acc[q >> 1][q & 1];
#pragma unroll
      for (int hf = 0; hf < 2; ++hf) {
        const u32x4 w = {pk_bf16(v[hf * 8], v[hf * 8 + 1]), pk_bf16(v[hf * 8 + 2], v[hf * 8 + 3]), pk_bf16(v[hf * 8 + 4], v[hf * 8 + 5]), pk_bf16(v[hf * 8 + 6], v[hf * 8 + 7])};
        __builtin_amdgcn_raw_buffer_store_b128(w, rs, (q * 4 + c) * 8192 + lo + hf * 1024, 0, 16);
      }
    }
  }
  asm volatile("" :: "v"(ptv));
  const f32x16 own = c == 0 ? acc[0][0] : (c == 1 ? acc[0][1] : (c == 2 ? acc[1][0] : acc[1][1]));
  const int trow = rt * 128 + wm * 64 + (c >> 1) * 32 + r31;
  const int ecol = j * 128 + wn * 64 + (c & 1) * 32 + 8 * hh;
  u32x4 xv[2], bv[2];
#pragma unroll
  for (int half = 0; half < 2; ++half) { xv[half] = *(const u32x4*)(x + (size_t)trow * D_ + ecol + 16 * half); bv[half] = *(const u32x4*)(b2 + ecol + 16 * half); }
  SB();
  unsigned* cnt = (unsigned*)(ws + WS_CNT + tile * 256);
  PERIODO(17, F1, F0);
  PERIODO(18, F0, F1);
  PERIODO(19, F1, F0);
  WAIT_V(4);
  ST(1, 3);
  PERIODO(20, F0, F1);
  if (tid == 0) __hip_atomic_fetch_add(cnt, 1u, __ATOMIC_RELAXED, __HIP_MEMORY_SCOPE_AGENT);
  PERIODO(21, F1, F0);
  PERIODO(22, F0, F1);
  if (tid == 0) {
    unsigned spins = 0;
    while (__hip_atomic_load(cnt, __ATOMIC_RELAXED, __HIP_MEMORY_SCOPE_AGENT) < 4u) { __builtin_amdgcn_s_sleep(1); if (++spins > (1u << 22)) break; }
  }
  asm volatile("" ::: "memory");
  ST(1, 4);
  PERIODO(23, F1, F0);
  asm volatile("" ::: "memory");
  u32x4 pv[4][2];
#pragma unroll
  for (int s = 0; s < 4; ++s)
#pragma unroll
    for (int hf = 0; hf < 2; ++hf)
      pv[s][hf] = __builtin_amdgcn_raw_buffer_load_b128(rs, (c * 4 + s) * 8192 + lo + hf * 1024, 0, 16);
  SB();
  PERIODO(24, F0, F1);
  PERIODO(25, F1, F0);
  PERIODO(26, F0, F1);
  MO(F0, 0); MO(F0, 1); MO(F0, 2); MO(F0, 3);
  ST(1, 6);
#pragma unroll
  for (int half = 0; half < 2; ++half) {
    float o[8];
#pragma unroll
    for (int e = 0; e < 8; ++e) {
      const int rg = half * 8 + e;
      float p[4];
#pragma unroll
      for (int s = 0; s < 4; ++s) { const unsigned w = pv[s][half][e >> 1]; p[s] = c == s ? own[rg] : ((e & 1) ? bfhi(w) : bflo(w)); }
      const float s = (p[0] + p[1]) + (p[2] + p[3]);
      const unsigned xw = xv[half][e >> 1], bw = bv[half][e >> 1];
      const float xx = (e & 1) ? bfhi(xw) : bflo(xw), bb2 = (e & 1) ? bfhi(bw) : bflo(bw);
      const float v = acco[rg] + bb2;
      const float g = v * __builtin_amdgcn_rcpf(1.f + __builtin_amdgcn_exp2f(-1.442695041f * v));
      o[e] = xx + s * g;
    }
    u32x4 ov = {pk_bf16(o[0], o[1]), pk_bf16(o[2], o[3]), pk_bf16(o[4], o[5]), pk_bf16(o[6], o[7])};
    *(u32x4*)(out + (size_t)trow * D_ + ecol + 16 * half) = ov;
  }
  ST(1, 5); ST_FLUSH(1);
}

extern "C" void kernel_launch(void* const* d_in, const int* in_sizes, int n_in, void* d_out, int out_size, void* d_ws, size_t ws_size,
                              hipStream_t stream) {
  const u16* x = (const u16*)d_in[0]; const u16* cre = (const u16*)d_in[1]; const u16* cim = (const u16*)d_in[2];
  const u16* wre = (const u16*)d_in[3]; const u16* wim = (const u16*)d_in[4];
  const u16* w1 = (const u16*)d_in[6]; const u16* b1 = (const u16*)d_in[7]; const u16* w2 = (const u16*)d_in[8]; const u16* b2 = (const u16*)d_in[9];
  (void)hipFuncSetAttribute((const void*)k1, hipFuncAttributeMaxDynamicSharedMemorySize, 163840);
  (void)hipFuncSetAttribute((const void*)k2, hipFuncAttributeMaxDynamicSharedMemorySize, 132096);
  k1<<<256, 512, 163840, stream>>>(x, wre, wim, w1, b1, w2, cre, cim, (char*)d_ws, b2, (const u16*)d_out);
  k2<<<256, 512, 132096, stream>>>(x, cre, cim, b2, (char*)d_ws, (u16*)d_out);
}
```

```cpp
#include <hip/hip_runtime.h>
#include <hip/hip_bf16.h>
#include <stdint.h>
#include <type_traits>

#define ABL 0
typedef unsigned short u16;
typedef short v4s __attribute__((ext_vector_type(4)));
typedef short bf16x8 __attribute__((ext_vector_type(8)));
typedef __bf16 v2bf __attribute__((ext_vector_type(2)));
typedef float f32x4 __attribute__((ext_vector_type(4)));
typedef float f32x16 __attribute__((ext_vector_type(16)));
typedef unsigned u32x4 __attribute__((ext_vector_type(4)));
typedef unsigned u32x2 __attribute__((ext_vector_type(2)));
#define LAS __attribute__((address_space(3)))
#define GAS __attribute__((address_space(1)))

constexpr int S_ = 2048, D_ = 512, F_ = 1376;
constexpr int NKK_B = 64;
constexpr int NSUB = 88;
constexpr size_t WS_B    = 0;
constexpr size_t WS_W2   = 4u << 20;
constexpr size_t WS_H    = 8u << 20;
constexpr size_t WS_SLAB = 16u << 20;
constexpr size_t WS_CNT  = 40u << 20;

__device__ __forceinline__ float bf2f(u16 v) { return __uint_as_float(((unsigned)v) << 16); }
__device__ __forceinline__ float bflo(unsigned v) { return __uint_as_float(v << 16); }
__device__ __forceinline__ float bfhi(unsigned v) { return __uint_as_float(v & 0xffff0000u); }
__device__ __forceinline__ unsigned pk_bf16(float lo, float hi) {
  typedef float f32x2_ __attribute__((ext_vector_type(2)));
  v2bf r = __builtin_convertvector((f32x2_){lo, hi}, v2bf);
  return __builtin_bit_cast(unsigned, r);
}
__device__ __forceinline__ float rbf(float f) { return bflo(pk_bf16(f, 0.f)); }
__device__ __forceinline__ int swap23(int r) { return (r & ~12) | ((r & 4) << 1) | ((r & 8) >> 1); }
__device__ __forceinline__ float dot2(unsigned a, unsigned b, float c) {
  return __builtin_amdgcn_fdot2_f32_bf16(__builtin_bit_cast(v2bf, a), __builtin_bit_cast(v2bf, b), c, false);
}
#define GLDS16(g, l) __builtin_amdgcn_global_load_lds((const GAS void*)(g), (LAS void*)(l), 16, 0, 0)
typedef int i32x4 __attribute__((ext_vector_type(4)));
__device__ __forceinline__ i32x4 mk_rsrc(const void* p) {
  const unsigned long long a = (unsigned long long)p;
  return (i32x4){(int)(unsigned)a, (int)((unsigned)(a >> 32) & 0xffffu), 0x7fffffff, 0x00020000};
}
__device__ __forceinline__ void dma16(i32x4 rsrc, unsigned lds_dst, int voff, int soff) {
  unsigned keep;
  asm volatile("s_mov_b32 %0, m0\n\ts_mov_b32 m0, %1\n\ts_nop 0\n\tbuffer_load_dwordx4 %2, %3, %4 offen lds\n\ts_mov_b32 m0, %0"
               : "=&s"(keep) : "s"(lds_dst), "v"(voff), "s"(rsrc), "s"(soff) : "memory");
}
__device__ __forceinline__ void touch4(i32x4 rsrc, unsigned lds_dst, int voff, int soff) {
  unsigned keep;
  asm volatile("s_mov_b32 %0, m0\n\ts_mov_b32 m0, %1\n\ts_nop 0\n\tbuffer_load_dword %2, %3, %4 offen lds\n\ts_mov_b32 m0, %0"
               : "=&s"(keep) : "s"(lds_dst), "v"(voff), "s"(rsrc), "s"(soff) : "memory");
}
template <int OFF> __device__ __forceinline__ void dma16x(i32x4 rsrc, unsigned lds_dst, int voff, int soff) {
  unsigned keep;
  asm volatile("s_mov_b32 %0, m0\n\ts_mov_b32 m0, %1\n\ts_nop 3\n\tbuffer_load_dwordx4 %2, %3, %4 offen offset:%5 lds\n\ts_mov_b32 m0, %0"
               : "=&s"(keep) : "s"(lds_dst), "v"(voff), "s"(rsrc), "s"(soff), "n"(OFF) : "memory");
}
template <int OFF> __device__ __forceinline__ void dma16x_nt(i32x4 rsrc, unsigned lds_dst, int voff, int soff) {
  unsigned keep;
  asm volatile("s_mov_b32 %0, m0\n\ts_mov_b32 m0, %1\n\ts_nop 3\n\tbuffer_load_dwordx4 %2, %3, %4 offen offset:%5 nt lds\n\ts_mov_b32 m0, %0"
               : "=&s"(keep) : "s"(lds_dst), "v"(voff), "s"(rsrc), "s"(soff), "n"(OFF) : "memory");
}
__device__ __forceinline__ void touch4_nt(i32x4 rsrc, unsigned lds_dst, int voff, int soff) {
  unsigned keep;
  asm volatile("s_mov_b32 %0, m0\n\ts_mov_b32 m0, %1\n\ts_nop 0\n\tbuffer_load_dword %2, %3, %4 offen nt lds\n\ts_mov_b32 m0, %0"
               : "=&s"(keep) : "s"(lds_dst), "v"(voff), "s"(rsrc), "s"(soff) : "memory");
}
#define WAIT_V(n) asm volatile("s_waitcnt vmcnt(%0)" ::"n"(n) : "memory")
#define WAIT_L0() asm volatile("s_waitcnt lgkmcnt(0)" ::: "memory")
#define BAR() __builtin_amdgcn_s_barrier()
#define ST_DECL
#define ST(k, i)
#define ST_FLUSH(k)

#define AUX_PF 2
#define PF_ON 0
#define PF_AT 3
#define AUX_ST 16
#define TOUCH 2
typedef float f32x2 __attribute__((ext_vector_type(2)));
constexpr int SL1 = 0, SL2 = 33280, SL3 = 98304, SL4 = 0, SLP = 131072, AROW = 1040;
constexpr int SCR = 66560;
constexpr int SC_PART = SCR;
constexpr int SC_HS   = SCR + 2048;
constexpr int SC_RS   = SCR + 3072;
constexpr int SC_MRS  = SCR + 3584;
constexpr int SC_CS   = SCR + 4096;
constexpr int SC_B1   = SCR + 7168;
constexpr int SC_DUM  = SCR + 8192;
#define PIN(x) asm volatile("" : "+v"(x))
#define SBAR() __builtin_amdgcn_sched_barrier(0)

template <int K> __device__ __forceinline__ void eh_op(const f32x16& ap, float (&v)[16], float (&t)[16], unsigned (&ow)[8], float scale, float center) {
  constexpr int st = K / 16, e = K % 16;
  if constexpr (st == 0) { v[e] = fmaf(-center, t[e], v[e]); PIN(v[e]); }
  else if constexpr (st == 1) { v[e] = fmaf(ap[e], scale, v[e]); PIN(v[e]); }
  else if constexpr (st == 2) { t[e] = v[e] * v[e]; PIN(t[e]); }
  else if constexpr (st == 3) { t[e] = fmaf(t[e], -0.1029432397f, -2.302208198f); PIN(t[e]); }
  else if constexpr (st == 4) { t[e] = t[e] * v[e]; PIN(t[e]); }
  else if constexpr (st == 5) { t[e] = __builtin_amdgcn_exp2f(t[e]); PIN(t[e]); }
  else if constexpr (st == 6) { t[e] = 1.f + t[e]; PIN(t[e]); }
  else if constexpr (st == 7) { t[e] = __builtin_amdgcn_rcpf(t[e]); PIN(t[e]); }
  else if constexpr (st == 8) { v[e] = v[e] * t[e]; PIN(v[e]); }
  else if constexpr (K < 152) { ow[e] = pk_bf16(v[2 * e], v[2 * e + 1]); PIN(ow[e]); }
}
template <int K> __device__ __forceinline__ void ea_op(const f32x16& ap, float (&v)[16], float (&t)[16], unsigned (&ow)[8], float cs) {
  constexpr int st = K / 16, e = K % 16;
  if constexpr (st == 0) { t[e] = ap[e] * t[e]; PIN(t[e]); }
  else if constexpr (st == 1) { t[e] = fmaf(-v[e], cs, t[e]); PIN(t[e]); }
  else if constexpr (K < 40) { ow[e] = pk_bf16(t[2 * e], t[2 * e + 1]); PIN(ow[e]); }
}
template <int ET, int K0, int K1> __device__ __forceinline__ void e_ops(const f32x16& ap, float (&v)[16], float (&t)[16], unsigned (&ow)[8], float s0, float s1) {
  if constexpr (K0 < K1) {
    if constexpr (ET == 1) eh_op<K0>(ap, v, t, ow, s0, s1); else if constexpr (ET == 2) ea_op<K0>(ap, v, t, ow, s0);
    e_ops<ET, K0 + 1, K1>(ap, v, t, ow, s0, s1);
  }
}
template <int MT> __device__ __forceinline__ bf16x8 wread(const char* pan, int ks) {
  if constexpr (MT == 1) { return *(const bf16x8*)(pan + ks * 32); }
  else {
    v4s t0 = __builtin_amdgcn_ds_read_tr16_b64_v4i16((LAS v4s*)(pan + ks * 1024));
    v4s t1 = __builtin_amdgcn_ds_read_tr16_b64_v4i16((LAS v4s*)(pan + ks * 1024 + 256));
    return (bf16x8)__builtin_shufflevector(t0, t1, 0, 1, 2, 3, 4, 5, 6, 7);
  }
}
__device__ __forceinline__ void row_stats(float sum, float sq, float& mean, float& inv, float& m_bf, float& scale, float& center) {
  mean = sum * (1.f / 512.f);
  const float var = fmaxf(sq * (1.f / 512.f) - mean * mean, 0.f);
  m_bf = rbf(mean);
  const float sd = rbf(sqrtf(rbf(rbf(var) + 1e-5f)));
  inv = 1.f / sd;
  const float m2 = rbf((mean - m_bf) * inv);
  const float v2 = rbf(var * inv * inv);
  const float sd2 = rbf(sqrtf(rbf(v2 + 1e-5f)));
  scale = inv / sd2; center = (m_bf + m2 * sd) * scale;
}
constexpr int WR = 8, WD = 3;
struct RowStat { float mean, var, m_bf, sd, inv, m2, v2, sd2, scale, center; };
template <int S> __device__ __forceinline__ void rs_stage(RowStat& r) {
  if constexpr (S == 0) { r.mean *= (1.f / 512.f); r.var = fmaxf(r.var * (1.f / 512.f) - r.mean * r.mean, 0.f); r.m_bf = rbf(r.mean); PIN(r.var); PIN(r.m_bf); }
  else if constexpr (S == 1) { r.sd = rbf(rbf(r.var) + 1e-5f); PIN(r.sd); }
  else if constexpr (S == 2) { r.sd = rbf(sqrtf(r.sd)); PIN(r.sd); }
  else if constexpr (S == 3) { r.inv = 1.f / r.sd; PIN(r.inv); }
  else if constexpr (S == 4) { r.m2 = rbf((r.mean - r.m_bf) * r.inv); r.v2 = rbf(rbf(r.var * r.inv * r.inv) + 1e-5f); PIN(r.m2); PIN(r.v2); }
  else if constexpr (S == 5) { r.sd2 = rbf(sqrtf(r.v2)); PIN(r.sd2); }
  else if constexpr (S == 6) { r.scale = r.inv / r.sd2; PIN(r.scale); }
  else if constexpr (S == 7) { r.center = (r.m_bf + r.m2 * r.sd) * r.scale; PIN(r.center); }
}
template <int MT, int ET, int NT, int G0, int KS, class Hook>
__device__ __forceinline__ void unit_steps(const char* pan, const char* pnx, const bf16x8 (&xf)[32], bf16x8 (&wf)[WR], f32x16& aw, const f32x16& ar,
                                           float (&v)[16], float (&t)[16], unsigned (&ow)[8], const float& s0, const float& s1, Hook&& hook) {
  if constexpr (KS < 32) {
    constexpr int NOPS = ET == 1 ? 152 : 40;
    if constexpr (KS == 0) {
      if constexpr (MT == 1) aw = __builtin_amdgcn_mfma_f32_32x32x16_bf16(xf[KS], wf[KS % WR], (f32x16){}, 0, 0, 0);
      else aw = __builtin_amdgcn_mfma_f32_32x32x16_bf16(wf[KS % WR], xf[KS], (f32x16){}, 0, 0, 0);
    } else {
      if constexpr (MT == 1) aw = __builtin_amdgcn_mfma_f32_32x32x16_bf16(xf[KS], wf[KS % WR], aw, 0, 0, 0);
      else aw = __builtin_amdgcn_mfma_f32_32x32x16_bf16(wf[KS % WR], xf[KS], aw, 0, 0, 0);
    }
    PIN(aw);
    SBAR();
    if constexpr (KS + WD < 32) wf[(KS + WD) % WR] = wread<MT>(pan, KS + WD);
    else if constexpr (NT >= 0) wf[(KS + WD) % WR] = wread<NT>(pnx, KS + WD - 32);
    asm volatile("" :: "v"(wf[KS % WR]), "v"(wf[(KS + WR - 1) % WR]));
    SBAR();
    hook(std::integral_constant<int, KS>{});
    constexpr int k0 = KS < G0 ? 0 : (KS - G0) * NOPS / (32 - G0), k1 = KS < G0 ? 0 : (KS + 1 - G0) * NOPS / (32 - G0);
    e_ops<ET, k0, k1>(ar, v, t, ow, s0, s1);
    SBAR();
    unit_steps<MT, ET, NT, G0, KS + 1>(pan, pnx, xf, wf, aw, ar, v, t, ow, s0, s1, hook);
  }
}

__global__ __launch_bounds__(512, 2) void k1(const u16* __restrict__ x, const u16* __restrict__ wre, const u16* __restrict__ wim,
                                             const u16* __restrict__ w1, const u16* __restrict__ b1, const u16* __restrict__ w2,
                                             const u16* __restrict__ cre, const u16* __restrict__ cim, char* __restrict__ ws) {
  extern __shared__ __attribute__((aligned(16))) char lds[];
  const int tid = threadIdx.x, lane = tid & 63, wid = __builtin_amdgcn_readfirstlane(tid >> 6);
  const int b = blockIdx.x, xcd = b & 7, idx = b >> 3;
  const int rt = (xcd >> 1) * 4 + (idx & 3);
  const int cg = (xcd & 1) * 8 + (idx >> 2);
  const int h0 = cg < 11 ? cg * 3 : 33 + (cg - 11) * 2;
  const bool h3 = cg < 11;
  const int a0 = cg * 2;
  const int m = wid & 3;
  const bool helper = wid >= 4;
  const int r31 = lane & 31, hh = lane >> 5;
  const unsigned lds0 = (unsigned)(size_t)(LAS char*)lds;
  const int prot = idx & 3;
  ST_DECL;
  ST(0, 0);

  const int hoff = (lane >> 2) * (F_ * 2) + (lane & 3) * 16;
  const i32x4 rw1 = mk_rsrc(w1);
  auto piece_h = [&](int u, int q, int slot_off) { dma16(rw1, lds0 + slot_off + q * 1024, hoff, q * 16 * (F_ * 2) + u * 64); };
  const i32x4 rx = mk_rsrc(x + (size_t)(rt * 128 + m * 32) * D_);
  int xo[4];
#pragma unroll
  for (int i = 0; i < 4; ++i) { const int r = i * 4 + (lane >> 4); xo[i] = r * 1024 + (((lane & 15) ^ r) * 16); }
  auto quarter = [&](int q) {
#pragma unroll
    for (int i = 0; i < 8; ++i) dma16(rx, lds0 + q * 32768 + m * 8192 + i * 1024, xo[i & 3], q * 256 + (i >> 2) * 16384);
    piece_h(h0, q * 8 + m * 2, SLP); piece_h(h0, q * 8 + m * 2 + 1, SLP);
  };
  if (helper) { quarter(0); quarter(1); }

  const auto rws = __builtin_amdgcn_make_buffer_rsrc((void*)ws, 0, 0x7fffffff, 0x00020000);
  const int xrow = m * 32 + swap23(r31);
  const unsigned xbase = xrow * 256 + ((hh ^ (xrow & 15)) * 16);
#define QWAIT(q) WAIT_V(9 + 10 * (3 - (q)))

  if (helper) {
    const auto rw2t = __builtin_amdgcn_make_buffer_rsrc((void*)(ws + WS_W2), 0, 16 * NSUB * 1024, 0x00020000);
    const i32x4 rwa = mk_rsrc((a0 < 16 ? wre : wim) + (size_t)(a0 & 15) * 32 * D_);
    auto panel_h = [&](int u, int slot_off) {
#pragma unroll
      for (int k = 0; k < 8; ++k) piece_h(u, m * 8 + ((k + 2 * prot) & 7), slot_off);
    };
    auto panel_a = [&](int w, int slot_off) {
#pragma unroll
      for (int k = 0; k < 8; ++k) { const int q = m * 8 + ((k + 2 * prot) & 7); dma16(rwa, lds0 + slot_off + q * AROW, lane * 16, (w * 32 + q) * 1024); }
    };
    const v4s ones = {0x3f80, 0x3f80, 0x3f80, 0x3f80};
    const char* pzh = lds + ((lane >> 2) & 15) * 64 + (m & 1) * 32 + (lane & 3) * 8;
    auto cs_h_part = [&](f32x4 (&c)[4], int slot_off, int j0, int n) {
#pragma unroll
      for (int j = 0; j < n; ++j)
        c[j & 3] = __builtin_amdgcn_mfma_f32_4x4x4bf16_1k(ones, __builtin_amdgcn_ds_read_tr16_b64_v4i16((LAS v4s*)(pzh + slot_off + (j0 + j) * 1024)), c[j & 3], 0, 0, 0);
    };
    auto cs_h_fin = [&](f32x4 (&c)[4], int i) {
      float r = (c[0][0] + c[1][0]) + (c[2][0] + c[3][0]);
      r += __shfl_xor(r, 16); r += __shfl_xor(r, 32);
      if (lane < 16) *(float*)(lds + SC_CS + i * 512 + (m & 1) * 64 + lane * 4) = r;
    };
    auto cs_h = [&](int i, int slot_off) {
      if ((m >> 1) == (i & 1)) { f32x4 c[4] = {}; cs_h_part(c, slot_off, 0, 32); cs_h_fin(c, i); }
    };
    auto cs_a = [&](int i, int slot_off) {
      const char* pz = lds + slot_off + swap23(r31) * AROW + hh * 16 + m * 256;
      f32x4 c0 = {}, c1 = {};
#pragma unroll
      for (int j = 0; j < 8; ++j) {
        const bf16x8 w = *(const bf16x8*)(pz + j * 32);
        c0 = __builtin_amdgcn_mfma_f32_4x4x4bf16_1k(ones, __builtin_shufflevector(w, w, 0, 1, 2, 3), c0, 0, 0, 0);
        c1 = __builtin_amdgcn_mfma_f32_4x4x4bf16_1k(ones, __builtin_shufflevector(w, w, 4, 5, 6, 7), c1, 0, 0, 0);
      }
      float r = c0[0] + c1[0]; r += __shfl_xor(r, 32);
      if (lane < 32) *(float*)(lds + SC_CS + i * 512 + m * 128 + lane * 4) = r;
    };
    auto prefetch_c = [&]() {
      const int c2 = xcd >> 1;
      const int row = (xcd & 1) * 1024 + idx * 32 + m * 8 + (lane >> 4);
      const i32x4 rc = mk_rsrc(c2 < 2 ? cre : cim);
      const int vo = row * 4096 + (lane & 15) * 128;
      if (AUX_PF == 2) { touch4_nt(rc, lds0 + SC_DUM + m * 256, vo, (c2 & 1) * 2048); touch4_nt(rc, lds0 + SC_DUM + m * 256, vo, (c2 & 1) * 2048 + 4 * 4096); }
      else { touch4(rc, lds0 + SC_DUM + m * 256, vo, (c2 & 1) * 2048); touch4(rc, lds0 + SC_DUM + m * 256, vo, (c2 & 1) * 2048 + 4 * 4096); }
    };
    __builtin_amdgcn_s_setprio(2);
    {
      f32x4 c[4] = {};
      WAIT_V(10); ST(0, 1); BAR();
      quarter(2);
      if (m < 2) cs_h_part(c, SLP, 0, 8);
      WAIT_V(10); BAR();
      quarter(3);
      panel_h(h0 + 1, SL1);
      if (m < 2) cs_h_part(c, SLP, 8, 8);
      WAIT_V(18); BAR();
      unsigned w2v[2][8], b1v;
      int w2dst[2];
      {
        i32x4 rw2 = mk_rsrc(w2); rw2.z = F_ * D_ * 2;
#pragma unroll
        for (int r = 0; r < 2; ++r) {
          const int hl = r * 256 + (tid - 256), cc = b * 352 + hl;
          const int ln = cc & 63, rest = cc >> 6, ct = rest / NSUB, sub = rest - ct * NSUB;
          const int off = hl < 352 ? ((16 * sub + 4 * (ln >> 5)) * D_ + 32 * ct + swap23(ln & 31)) * 2 : 0x7ff00000;
          w2dst[r] = hl < 352 ? cc * 16 : 0x7ffffff0;
          asm volatile("buffer_load_ushort %0, %8, %10, 0 offen\n\tbuffer_load_ushort %1, %8, %10, 0 offen offset:1024\n\t"
                       "buffer_load_ushort %2, %8, %10, 0 offen offset:2048\n\tbuffer_load_ushort %3, %8, %10, 0 offen offset:3072\n\t"
                       "buffer_load_ushort %4, %9, %10, 0 offen\n\tbuffer_load_ushort %5, %9, %10, 0 offen offset:1024\n\t"
                       "buffer_load_ushort %6, %9, %10, 0 offen offset:2048\n\tbuffer_load_ushort %7, %9, %10, 0 offen offset:3072"
                       : "=&v"(w2v[r][0]), "=&v"(w2v[r][1]), "=&v"(w2v[r][2]), "=&v"(w2v[r][3]), "=&v"(w2v[r][4]), "=&v"(w2v[r][5]), "=&v"(w2v[r][6]), "=&v"(w2v[r][7])
                       : "v"(off), "v"(off + 8192), "s"(rw2) : "memory");
        }
        const int bo = h0 * 32 + lane * 2;
        asm volatile("buffer_load_dword %0, %1, %2, 0 offen" : "=&v"(b1v) : "v"((bo < F_ - 2 ? bo : F_ - 2) * 2), "s"(mk_rsrc(b1)) : "memory");
      }
      if (m < 2) cs_h_part(c, SLP, 16, 8);
      WAIT_V(25); BAR();
      ST(0, 2);
      panel_a(0, SL2);
      if (m < 2) { cs_h_part(c, SLP, 24, 8); cs_h_fin(c, 0); }
      WAIT_V(8);
      asm volatile("" : "+v"(w2v[0][0]), "+v"(w2v[0][1]), "+v"(w2v[0][2]), "+v"(w2v[0][3]), "+v"(w2v[0][4]), "+v"(w2v[0][5]), "+v"(w2v[0][6]), "+v"(w2v[0][7]), "+v"(b1v));
      asm volatile("" : "+v"(w2v[1][0]), "+v"(w2v[1][1]), "+v"(w2v[1][2]), "+v"(w2v[1][3]), "+v"(w2v[1][4]), "+v"(w2v[1][5]), "+v"(w2v[1][6]), "+v"(w2v[1][7]));
      if (m == 3 && lane < 48) *(f32x2*)(lds + SC_B1 + lane * 8) = (f32x2){bflo(b1v), bfhi(b1v)};
#pragma unroll
      for (int r = 0; r < 2; ++r) {
        const u32x4 w = {w2v[r][0] | (w2v[r][1] << 16), w2v[r][2] | (w2v[r][3] << 16), w2v[r][4] | (w2v[r][5] << 16), w2v[r][6] | (w2v[r][7] << 16)};
        __builtin_amdgcn_raw_buffer_store_b128(w, rw2t, w2dst[r], 0, AUX_ST);
      }
      __builtin_amdgcn_raw_buffer_store_b32(0u, __builtin_amdgcn_make_buffer_rsrc((void*)(ws + WS_CNT), 0, 16384, 0x00020000), (b == 0 && m == 0) ? lane * 256 : 0x7ffffff0, 0, AUX_ST);
    }
    ST(0, 3);
    WAIT_V(3); WAIT_L0(); BAR();
    panel_h(h3 ? h0 + 2 : h0, SL3);
    cs_h(1, SL1);
    {
      float mean, inv, m_bf, sc_, ce_;
      const f32x2 pr = *(const f32x2*)(lds + SC_PART + (m * 32 + r31) * 8);
      row_stats(pr.x, pr.y, mean, inv, m_bf, sc_, ce_);
      const float sg = a0 < 16 ? inv : -inv;
      if (lane < 32) { *(float*)(lds + SC_RS + (m * 32 + lane) * 4) = sg; *(float*)(lds + SC_MRS + (m * 32 + lane) * 4) = m_bf * sg; }
    }
    WAIT_V(0); WAIT_L0(); BAR();
    panel_a(1, SL4);
    if (PF_ON && PF_AT == 3) prefetch_c();
    cs_a(2, SL2);
    WAIT_V(0);
    WAIT_L0(); BAR();
    if (PF_ON && PF_AT == 4) prefetch_c();
    cs_h(3, SL3);
    WAIT_L0(); BAR();
    cs_a(4, SL4);
    WAIT_L0(); BAR();
    WAIT_V(0);
    ST(0, 5); ST_FLUSH(0);
    return;
  }

  bf16x8 xf[32];
  unsigned long long clk0 = 0; (void)clk0;
  const int wrow = swap23(r31);
  const unsigned wlA = (unsigned)(wrow * AROW + hh * 16);
  const unsigned wlH = (unsigned)((8 * hh + ((lane & 15) >> 2)) * 64 + ((lane >> 4) & 1) * 32 + (lane & 3) * 8);
  f32x16 acc, ac2;
  bf16x8 wf[WR];
  float v[16], t[16]; unsigned ow[8];
  auto nohook = [](auto) {};
  RowStat rs;
  const int hstore = (swap23(r31) + 32 * hh) * 16;
  const int s0r = rt * 128 + m * 32;

  auto tables_h = [&](int i, int hu) {
#pragma unroll
    for (int g4 = 0; g4 < 4; ++g4) {
      const f32x4 c = *(const f32x4*)(lds + SC_CS + i * 512 + (8 * g4 + 4 * hh) * 4), bq = *(const f32x4*)(lds + SC_B1 + hu * 128 + (8 * g4 + 4 * hh) * 4);
#pragma unroll
      for (int e = 0; e < 4; ++e) { t[g4 * 4 + e] = c[e]; v[g4 * 4 + e] = bq[e]; }
    }
  };
  auto tables_a = [&](int i) -> float {
#pragma unroll
    for (int g4 = 0; g4 < 4; ++g4) {
      const f32x4 c = *(const f32x4*)(lds + SC_RS + (m * 32 + 8 * g4 + 4 * hh) * 4), bq = *(const f32x4*)(lds + SC_MRS + (m * 32 + 8 * g4 + 4 * hh) * 4);
#pragma unroll
      for (int e = 0; e < 4; ++e) { t[g4 * 4 + e] = c[e]; v[g4 * 4 + e] = bq[e]; }
    }
    const char* cq = lds + SC_CS + i * 512 + r31 * 4;
    return (*(const float*)cq + *(const float*)(cq + 128)) + (*(const float*)(cq + 256) + *(const float*)(cq + 384));
  };
  auto tables_a4 = [&](int i, float (&cp)[4]) {
#pragma unroll
    for (int g4 = 0; g4 < 4; ++g4) {
      const f32x4 c = *(const f32x4*)(lds + SC_RS + (m * 32 + 8 * g4 + 4 * hh) * 4), bq = *(const f32x4*)(lds + SC_MRS + (m * 32 + 8 * g4 + 4 * hh) * 4);
#pragma unroll
      for (int e = 0; e < 4; ++e) { t[g4 * 4 + e] = c[e]; v[g4 * 4 + e] = bq[e]; }
      cp[g4] = *(const float*)(lds + SC_CS + i * 512 + g4 * 128 + r31 * 4);
    }
  };
  auto store_h = [&](int hu, const unsigned (&ow)[8]) {
    const int dst = (int)WS_H + (((rt * 4 + m) * NSUB + 2 * (h0 + hu)) << 10);
    if (hu < 2 || h3) {
      __builtin_amdgcn_raw_buffer_store_b128((u32x4){ow[0], ow[1], ow[2], ow[3]}, rws, hstore, dst, AUX_ST);
      __builtin_amdgcn_raw_buffer_store_b128((u32x4){ow[4], ow[5], ow[6], ow[7]}, rws, hstore + 1024, dst, AUX_ST);
    }
    if (h0 + hu == 42 && (hu < 2 || h3)) { u32x4 z = {0u, 0u, 0u, 0u}; __builtin_amdgcn_raw_buffer_store_b128(z, rws, lane * 16 + 2048, dst, AUX_ST); __builtin_amdgcn_raw_buffer_store_b128(z, rws, lane * 16 + 3072, dst, AUX_ST); }
  };
  auto store_a = [&](int w, const unsigned (&ow)[8]) {
    const int u = a0 + w, et = u & 15, j = et >> 2, nt = et & 3;
    const int kk = (u >> 4) * 32 + (s0r >> 6), sub = (s0r & 63) >> 4;
    const int dst = (int)WS_B + (((j * NKK_B + kk) * 16 + nt * 4 + sub) << 10);
    __builtin_amdgcn_raw_buffer_store_b128((u32x4){ow[0], ow[1], ow[2], ow[3]}, rws, lane * 16, dst, AUX_ST);
    __builtin_amdgcn_raw_buffer_store_b128((u32x4){ow[4], ow[5], ow[6], ow[7]}, rws, lane * 16 + 1024, dst, AUX_ST);
  };
  {
    f32x4 ss0 = {}, ss1 = {}, sq0 = {}, sq1 = {};
    const v4s ones4 = {0x3f80, 0x3f80, 0x3f80, 0x3f80};
    acc = (f32x16){};
    const char* pan = lds + SLP + wlH;
#pragma unroll
    for (int q = 0; q < 4; ++q) {
      if (q == 0) ST(0, 1);
      BAR();
      if (q == 3) ST(0, 2);
      bf16x8 w8[8];
#pragma unroll
      for (int k = 0; k < 8; ++k) xf[q * 8 + k] = *(const bf16x8*)(lds + q * 32768 + (xbase ^ (k * 32)));
#pragma unroll
      for (int k = 0; k < 8; ++k) w8[k] = wread<0>(pan, q * 8 + k);
      SBAR();
#pragma unroll
      for (int k = 0; k < 8; ++k) {
        const v4s lo4 = __builtin_shufflevector(xf[q * 8 + k], xf[q * 8 + k], 0, 1, 2, 3), hi4 = __builtin_shufflevector(xf[q * 8 + k], xf[q * 8 + k], 4, 5, 6, 7);
        ss0 = __builtin_amdgcn_mfma_f32_4x4x4bf16_1k(ones4, lo4, ss0, 0, 0, 0); sq0 = __builtin_amdgcn_mfma_f32_4x4x4bf16_1k(lo4, lo4, sq0, 0, 0, 0);
        ss1 = __builtin_amdgcn_mfma_f32_4x4x4bf16_1k(ones4, hi4, ss1, 0, 0, 0); sq1 = __builtin_amdgcn_mfma_f32_4x4x4bf16_1k(hi4, hi4, sq1, 0, 0, 0);
      }
      asm volatile("" : "+v"(ss0), "+v"(ss1), "+v"(sq0), "+v"(sq1));
      SBAR();
#pragma unroll
      for (int k = 0; k < 8; ++k) { acc = __builtin_amdgcn_mfma_f32_32x32x16_bf16(w8[k], xf[q * 8 + k], acc, 0, 0, 0); PIN(acc); }
      WAIT_L0(); SBAR();
    }
    const f32x4 sqv = sq0 + sq1;
    float sum = ss0[0] + ss1[0], sq = (lane & 2) ? ((lane & 1) ? sqv[3] : sqv[2]) : ((lane & 1) ? sqv[1] : sqv[0]);
    sum += __shfl_xor(sum, 32); sq += __shfl_xor(sq, 32);
    if (lane < 32) *(f32x2*)(lds + SC_PART + (m * 32 + lane) * 8) = (f32x2){sum, sq};
    rs.mean = sum; rs.var = sq;
  }
  ST(0, 8);
  WAIT_L0(); BAR();
  ST(0, 4);
  const char* pH1 = lds + SL1 + wlH; const char* pA0 = lds + SL2 + wlA;
  const char* p3H = lds + SL3 + wlH; const char* pA1 = lds + SL4 + wlA;
#pragma unroll
  for (int e = 0; e < WD; ++e) wf[e] = wread<0>(pH1, e);
  wf[WR - 1] = wf[0];
  tables_h(0, 0);
  float csa = 0.f;
  unit_steps<0, 1, 1, 8, 0>(pH1, pA0, xf, wf, ac2, acc, v, t, ow, rs.scale, rs.center, [&](auto g) {
    constexpr int G = decltype(g)::value;
    if constexpr (G < 8) rs_stage<G>(rs);
  });
  store_h(0, ow);
  ST(0, 9);
  BAR();
  ST(0, 6);
  {
    tables_h(1, 1);
    unit_steps<1, 1, 0, 3, 0>(pA0, p3H, xf, wf, acc, ac2, v, t, ow, rs.scale, rs.center, nohook); store_h(1, ow);
    ST(0, 10);
    BAR();
    ST(0, 7);
    float cp[4]; tables_a4(2, cp);
    unit_steps<0, 2, 1, 3, 0>(p3H, pA1, xf, wf, ac2, acc, v, t, ow, csa, csa, [&](auto g) { if constexpr (decltype(g)::value == 2) { csa = (cp[0] + cp[1]); PIN(csa); float c2 = cp[2] + cp[3]; PIN(c2); csa += c2; PIN(csa); } }); store_a(0, ow);
    ST(0, 11);
    BAR();
    ST(0, 13);
    tables_h(3, 2);
    unit_steps<1, 1, -1, 3, 0>(pA1, pA1, xf, wf, acc, ac2, v, t, ow, rs.scale, rs.center, nohook); store_h(2, ow);
    ST(0, 12);
    BAR();
    csa = tables_a(4);
    e_ops<2, 0, 40>(acc, v, t, ow, csa, csa);
    store_a(1, ow);
  }
  WAIT_V(0);
  ST(0, 5); ST_FLUSH(0);
}

#define AUX_C 2
__global__ __launch_bounds__(512, 2) void k2(const u16* __restrict__ x, const u16* __restrict__ cre, const u16* __restrict__ cim,
                                             const u16* __restrict__ b2, char* __restrict__ ws, u16* __restrict__ out) {
  extern __shared__ __attribute__((aligned(16))) char lds[];
  const int tid = threadIdx.x, lane = tid & 63, wid = __builtin_amdgcn_readfirstlane(tid >> 6);
  const int b = blockIdx.x, xcd = b & 7, idx = b >> 3;
#define MAP2 0
  const int c = MAP2 ? (xcd >> 2) * 2 + (idx >> 4) : xcd >> 1, rt = MAP2 ? (xcd & 3) * 4 + ((idx >> 2) & 3) : (xcd & 1) * 8 + (idx >> 2), j = idx & 3;
  ST_DECL;
  ST(1, 0);

#define SB() __builtin_amdgcn_sched_barrier(0)

  if (wid >= 4) {
    const int lw = wid - 4;
    int aoff[4];
#pragma unroll
    for (int i = 0; i < 4; ++i) { int row = lw * 32 + i * 8 + (lane >> 3); aoff[i] = row * 4096 + (((lane & 7) ^ ((row >> 1) & 7)) * 16) - i * 1024 + 4096; }
    const unsigned ldsw = (unsigned)(size_t)(LAS char*)lds + lw * 4096;
    const i32x4 ra_s = mk_rsrc((const char*)(c < 2 ? cre : cim) + (size_t)rt * 128 * 4096 + (c & 1) * 2048 - 4096);
    const i32x4 rb_s = mk_rsrc(ws + WS_B + ((size_t)(j * NKK_B + c * 16) << 14) + lw * 4096);
    const i32x4 ra_o = mk_rsrc(ws + WS_H + ((size_t)((rt * 4 + 2 * (lw >> 1) + (c >> 1)) * NSUB) << 10) + (lw & 1) * 4096 - (16 << 13));
    const i32x4 rb_o = mk_rsrc(ws + WS_W2 + ((size_t)((j * 4 + 2 * (lw >> 1) + (c & 1)) * NSUB) << 10) + (lw & 1) * 4096 - (16 << 13));
    const int voff = lane * 16;
    const int r8 = idx >> 2;
#define LEAD 0
    const int ea = LEAD ? __builtin_amdgcn_readfirstlane(lw == j ? 1 : 0) : 1, eb0 = LEAD ? __builtin_amdgcn_readfirstlane(r8 == 2 * lw ? 1 : 0) : 1,
              eb1 = LEAD ? __builtin_amdgcn_readfirstlane(r8 == 2 * lw + 1 ? 1 : 0) : 1;
    auto issue_a = [&](int T) {
      const unsigned dst = __builtin_amdgcn_readfirstlane(ldsw + (T & 3) * 32768);
      if (T < 16) { const int so = __builtin_amdgcn_readfirstlane(T * 128);
                    dma16x<0>(ra_s, dst, aoff[0], so); dma16x<1024>(ra_s, dst, aoff[1], so); dma16x<2048>(ra_s, dst, aoff[2], so); dma16x<3072>(ra_s, dst, aoff[3], so); }
      else { i32x4 r = ra_o; r.z = __builtin_amdgcn_readfirstlane(T < 27 ? 0x7fffffff : 0); const int so = __builtin_amdgcn_readfirstlane(T << 13);
             dma16x<0>(r, dst, voff, so); dma16x<1024>(r, dst, voff, so); dma16x<2048>(r, dst, voff, so); dma16x<3072>(r, dst, voff, so); }
    };
    auto issue_b = [&](int T, int half) {
      const unsigned dst = __builtin_amdgcn_readfirstlane(ldsw + (T & 3) * 32768 + 16384 + half * 2048);
      i32x4 r = T < 16 ? rb_s : rb_o; r.z = T < 27 ? 0x7fffffff : 0;
      const int so = __builtin_amdgcn_readfirstlane((T < 16 ? T << 14 : T << 13) + half * 2048);
      dma16x<0>(r, dst, voff, so); dma16x<1024>(r, dst, voff, so);
    };
    issue_a(0); issue_b(0, 0); issue_b(0, 1);
    issue_a(1); issue_b(1, 0); issue_b(1, 1);
    if (ea) issue_a(2);
    if (eb0) issue_b(2, 0);
    if (eb1) issue_b(2, 1);
    const int npro = 8 + 4 * ea + 2 * eb0 + 2 * eb1;
    if (npro == 8) WAIT_V(8); else if (npro == 10) WAIT_V(10); else if (npro == 12) WAIT_V(12); else if (npro == 14) WAIT_V(14); else WAIT_V(16);
    for (int u = 0; u < 27; ++u) {
      if (u) WAIT_V(LEAD ? 8 : 16);
      BAR();
      issue_a(u + 2 + ea); issue_b(u + 2 + eb0, 0); issue_b(u + 2 + eb1, 1);
    }
    WAIT_V(0);
    return;
  }

  const int r31 = lane & 31, hh = lane >> 5;
  const int wm = wid >> 1, wn = wid & 1;
  unsigned ab[4];
#pragma unroll
  for (int s = 0; s < 4; ++s) ab[s] = wm * 8192 + (unsigned)(r31 * 128 + (((2 * s + hh) ^ ((r31 >> 1) & 7)) * 16));
  const unsigned bb = 16384 + wn * 8192 + lane * 16;
  const unsigned ao = wm * 8192 + lane * 16;

  f32x16 acc[2][2] = {};
  f32x16 acco = {};
  bf16x8 F0[16], F1[16];
#define RD(F, s) do { F[4 * (s) + 0] = *(const bf16x8*)(sl + ab[s]); F[4 * (s) + 1] = *(const bf16x8*)(sl + ab[s] + 4096); \
                      F[4 * (s) + 2] = *(const bf16x8*)(sl + bb + (s) * 1024); F[4 * (s) + 3] = *(const bf16x8*)(sl + bb + (s) * 1024 + 4096); } while (0)
#define RDO(F, s) do { F[4 * (s) + 0] = *(const bf16x8*)(sl + ao + (s) * 2048); F[4 * (s) + 1] = *(const bf16x8*)(sl + bb + (s) * 2048); \
                       F[4 * (s) + 2] = *(const bf16x8*)(sl + ao + (s) * 2048 + 1024); F[4 * (s) + 3] = *(const bf16x8*)(sl + bb + (s) * 2048 + 1024); } while (0)
#define MM0(F, s) acc[0][0] = __builtin_amdgcn_mfma_f32_32x32x16_bf16(F[4 * (s) + 2], F[4 * (s) + 0], acc[0][0], 0, 0, 0)
#define MM1(F, s) acc[0][1] = __builtin_amdgcn_mfma_f32_32x32x16_bf16(F[4 * (s) + 3], F[4 * (s) + 0], acc[0][1], 0, 0, 0)
#define MM2(F, s) acc[1][0] = __builtin_amdgcn_mfma_f32_32x32x16_bf16(F[4 * (s) + 2], F[4 * (s) + 1], acc[1][0], 0, 0, 0)
#define MM3(F, s) acc[1][1] = __builtin_amdgcn_mfma_f32_32x32x16_bf16(F[4 * (s) + 3], F[4 * (s) + 1], acc[1][1], 0, 0, 0)
#define MO(F, s) do { acco = __builtin_amdgcn_mfma_f32_32x32x16_bf16(F[4 * (s) + 1], F[4 * (s) + 0], acco, 0, 0, 0); \
                      acco = __builtin_amdgcn_mfma_f32_32x32x16_bf16(F[4 * (s) + 3], F[4 * (s) + 2], acco, 0, 0, 0); } while (0)
#define PERIOD(u, RDX, FR, FM) do { \
    const char* sl = lds + ((u) & 3) * 32768; \
    BAR(); SB(); \
    MM0(FM, 0); SB(); RDX(FR, 0); SB(); MM1(FM, 0); SB(); MM2(FM, 0); SB(); RDX(FR, 1); SB(); MM3(FM, 0); SB(); \
    MM0(FM, 1); SB(); RDX(FR, 2); SB(); MM1(FM, 1); SB(); MM2(FM, 1); SB(); RDX(FR, 3); SB(); MM3(FM, 1); SB(); \
    MM0(FM, 2); MM1(FM, 2); MM2(FM, 2); MM3(FM, 2); \
    MM0(FM, 3); MM1(FM, 3); MM2(FM, 3); SB(); WAIT_L0(); SB(); MM3(FM, 3); SB(); \
  } while (0)
#define PERIODO(u, FR, FM) do { \
    const char* sl = lds + ((u) & 3) * 32768; \
    BAR(); SB(); \
    RDO(FR, 0); SB(); MO(FM, 0); SB(); RDO(FR, 1); SB(); MO(FM, 1); SB(); RDO(FR, 2); SB(); MO(FM, 2); SB(); RDO(FR, 3); SB(); MO(FM, 3); SB(); \
    WAIT_L0(); SB(); \
  } while (0)
#define K2PF 0
#define K2PF_POS 0
  const unsigned dum = (unsigned)(size_t)(LAS char*)lds + 131072 + wid * 256;
  auto touches = [&]() {
    if (K2PF & 1) {
      const i32x4 rb = mk_rsrc(ws + WS_B + ((size_t)(j * NKK_B + c * 16) << 14));
      touch4(rb, dum, ((lane >> 4) + 4 * wid) * 16384 + ((idx >> 2) * 16 + (lane & 15)) * 128, 0);
    }
    if (K2PF & 2) {
      const i32x4 rc = mk_rsrc((const char*)(c < 2 ? cre : cim) + (size_t)(rt * 128 + j * 32) * 4096 + (c & 1) * 2048);
      const int vo = r31 * 4096 + (2 * wid + hh) * 128;
      touch4_nt(rc, dum, vo, 0); touch4_nt(rc, dum, vo, 1024);
    }
  };
  if (K2PF_POS == 0) touches();
  {
    const char* sl = lds;
    BAR(); SB(); ST(1, 1);
    if (K2PF_POS == 1) touches();
    RD(F0, 0); RD(F0, 1); RD(F0, 2); RD(F0, 3); SB();
    WAIT_L0(); SB();
  }
  for (int u = 1; u < 15; u += 2) {
    if ((K2PF & 4) && u == 7) {
      const i32x4 rh = mk_rsrc(ws + WS_H + ((size_t)((rt * 4 + 2 * (wid & 1) + (c >> 1)) * NSUB) << 10) + j * 22528);
      const int l2 = (wid >> 1) * 88 + lane;
      touch4(rh, dum, l2 * 128, 0);
      touch4(rh, dum, (lane < 24 ? l2 + 64 : l2) * 128, 0);
      const i32x4 rw = mk_rsrc(ws + WS_W2 + ((size_t)((j * 4 + 2 * (wid & 1) + (c & 1)) * NSUB) << 10) + (idx >> 2) * 11264);
      touch4(rw, dum, ((wid >> 1) * 44 + (lane < 44 ? lane : 0)) * 128, 0);
    }
    PERIOD(u, RD, F1, F0);
    PERIOD(u + 1, RD, F0, F1);
  }
  PERIOD(15, RD, F1, F0);
  PERIOD(16, RDO, F0, F1);
  ST(1, 2);
  const int tile = rt * 4 + j;
  const auto rs = __builtin_amdgcn_make_buffer_rsrc(ws + WS_SLAB + ((size_t)tile << 17), 0, (ABL & 16) ? 0 : (1 << 17), 0x00020000);
  const unsigned lo = wid * 2048 + lane * 16;
#pragma unroll
  for (int q = 0; q < 4; ++q) {
    if (q != c) {
      const f32x16 v = acc[q >> 1][q & 1];
#pragma unroll
      for (int hf = 0; hf < 2; ++hf) {
        const u32x4 w = {pk_bf16(v[hf * 8], v[hf * 8 + 1]), pk_bf16(v[hf * 8 + 2], v[hf * 8 + 3]), pk_bf16(v[hf * 8 + 4], v[hf * 8 + 5]), pk_bf16(v[hf * 8 + 6], v[hf * 8 + 7])};
        __builtin_amdgcn_raw_buffer_store_b128(w, rs, (q * 4 + c) * 8192 + lo + hf * 1024, 0, 16);
      }
    }
  }
  const f32x16 own = c == 0 ? acc[0][0] : (c == 1 ? acc[0][1] : (c == 2 ? acc[1][0] : acc[1][1]));
  const int trow = rt * 128 + wm * 64 + (c >> 1) * 32 + r31;
  const int ecol = j * 128 + wn * 64 + (c & 1) * 32 + 8 * hh;
  u32x4 xv[2], bv[2];
#pragma unroll
  for (int half = 0; half < 2; ++half) { xv[half] = *(const u32x4*)(x + (size_t)trow * D_ + ecol + 16 * half); bv[half] = *(const u32x4*)(b2 + ecol + 16 * half); }
  SB();
  unsigned* cnt = (unsigned*)(ws + WS_CNT + tile * 256);
  u32x4 pv[4][2];
#pragma unroll
  for (int s = 0; s < 4; ++s) { pv[s][0] = (u32x4){}; pv[s][1] = (u32x4){}; }
#pragma unroll 1
  for (int u = 17; u < 27; u += 2) {
    PERIODO(u, F1, F0);
    if (u == 19) { WAIT_V(4); ST(1, 3); }
    if (u == 23) {
      asm volatile("" ::: "memory");
#pragma unroll
      for (int s = 0; s < 4; ++s)
#pragma unroll
        for (int hf = 0; hf < 2; ++hf)
          pv[s][hf] = __builtin_amdgcn_raw_buffer_load_b128(rs, (c * 4 + s) * 8192 + lo + hf * 1024, 0, 16);
      SB();
    }
    PERIODO(u + 1, F0, F1);
    if (u == 19 && tid == 0) __hip_atomic_fetch_add(cnt, 1u, __ATOMIC_RELAXED, __HIP_MEMORY_SCOPE_AGENT);
    if (u == 21) {
      if (tid == 0) {
        unsigned spins = 0;
        while (__hip_atomic_load(cnt, __ATOMIC_RELAXED, __HIP_MEMORY_SCOPE_AGENT) < 4u) { __builtin_amdgcn_s_sleep(1); if (++spins > (1u << 22)) break; }
      }
      asm volatile("" ::: "memory");
      ST(1, 4);
    }
  }
  MO(F0, 0); MO(F0, 1); MO(F0, 2); MO(F0, 3);
  ST(1, 6);
#pragma unroll
  for (int half = 0; half < 2; ++half) {
    float o[8];
#pragma unroll
    for (int e = 0; e < 8; ++e) {
      const int rg = half * 8 + e;
      float p[4];
#pragma unroll
      for (int s = 0; s < 4; ++s) { const unsigned w = pv[s][half][e >> 1]; p[s] = c == s ? own[rg] : ((e & 1) ? bfhi(w) : bflo(w)); }
      const float s = (p[0] + p[1]) + (p[2] + p[3]);
      const unsigned xw = xv[half][e >> 1], bw = bv[half][e >> 1];
      const float xx = (e & 1) ? bfhi(xw) : bflo(xw), bb2 = (e & 1) ? bfhi(bw) : bflo(bw);
      const float v = acco[rg] + bb2;
      const float g = v * __builtin_amdgcn_rcpf(1.f + __builtin_amdgcn_exp2f(-1.442695041f * v));
      o[e] = xx + s * g;
    }
    u32x4 ov = {pk_bf16(o[0], o[1]), pk_bf16(o[2], o[3]), pk_bf16(o[4], o[5]), pk_bf16(o[6], o[7])};
    *(u32x4*)(out + (size_t)trow * D_ + ecol + 16 * half) = ov;
  }
  ST(1, 5); ST_FLUSH(1);
}

extern "C" void kernel_launch(void* const* d_in, const int* in_sizes, int n_in, void* d_out, int out_size, void* d_ws, size_t ws_size,
                              hipStream_t stream) {
  const u16* x = (const u16*)d_in[0]; const u16* cre = (const u16*)d_in[1]; const u16* cim = (const u16*)d_in[2];
  const u16* wre = (const u16*)d_in[3]; const u16* wim = (const u16*)d_in[4];
  const u16* w1 = (const u16*)d_in[6]; const u16* b1 = (const u16*)d_in[7]; const u16* w2 = (const u16*)d_in[8]; const u16* b2 = (const u16*)d_in[9];
  (void)hipFuncSetAttribute((const void*)k1, hipFuncAttributeMaxDynamicSharedMemorySize, 163840);
  (void)hipFuncSetAttribute((const void*)k2, hipFuncAttributeMaxDynamicSharedMemorySize, 132096);
  k1<<<256, 512, 163840, stream>>>(x, wre, wim, w1, b1, w2, cre, cim, (char*)d_ws);
  k2<<<256, 512, 132096, stream>>>(x, cre, cim, b2, (char*)d_ws, (u16*)d_out);
}
```

```cpp
#include <hip/hip_runtime.h>
#include <hip/hip_bf16.h>
#include <stdint.h>
#include <type_traits>

#define ABL 0
typedef unsigned short u16;
typedef short v4s __attribute__((ext_vector_type(4)));
typedef short bf16x8 __attribute__((ext_vector_type(8)));
typedef __bf16 v2bf __attribute__((ext_vector_type(2)));
typedef float f32x4 __attribute__((ext_vector_type(4)));
typedef float f32x16 __attribute__((ext_vector_type(16)));
typedef unsigned u32x4 __attribute__((ext_vector_type(4)));
typedef unsigned u32x2 __attribute__((ext_vector_type(2)));
#define LAS __attribute__((address_space(3)))
#define GAS __attribute__((address_space(1)))

constexpr int S_ = 2048, D_ = 512, F_ = 1376;
constexpr int NKK_B = 64;
constexpr int NSUB = 88;
constexpr size_t WS_B    = 0;
constexpr size_t WS_W2   = 4u << 20;
constexpr size_t WS_H    = 6u << 20;
constexpr size_t WS_SLAB = 12u << 20;
constexpr size_t WS_CNT  = (11u << 20) + (768u << 10);

__device__ __forceinline__ float bf2f(u16 v) { return __uint_as_float(((unsigned)v) << 16); }
__device__ __forceinline__ float bflo(unsigned v) { return __uint_as_float(v << 16); }
__device__ __forceinline__ float bfhi(unsigned v) { return __uint_as_float(v & 0xffff0000u); }
__device__ __forceinline__ unsigned pk_bf16(float lo, float hi) {
  typedef float f32x2_ __attribute__((ext_vector_type(2)));
  v2bf r = __builtin_convertvector((f32x2_){lo, hi}, v2bf);
  return __builtin_bit_cast(unsigned, r);
}
__device__ __forceinline__ float rbf(float f) { return bflo(pk_bf16(f, 0.f)); }
__device__ __forceinline__ int swap23(int r) { return (r & ~12) | ((r & 4) << 1) | ((r & 8) >> 1); }
__device__ __forceinline__ float dot2(unsigned a, unsigned b, float c) {
  return __builtin_amdgcn_fdot2_f32_bf16(__builtin_bit_cast(v2bf, a), __builtin_bit_cast(v2bf, b), c, false);
}
#define GLDS16(g, l) __builtin_amdgcn_global_load_lds((const GAS void*)(g), (LAS void*)(l), 16, 0, 0)
typedef int i32x4 __attribute__((ext_vector_type(4)));
__device__ __forceinline__ i32x4 mk_rsrc(const void* p) {
  const unsigned long long a = (unsigned long long)p;
  return (i32x4){(int)(unsigned)a, (int)((unsigned)(a >> 32) & 0xffffu), 0x7fffffff, 0x00020000};
}
__device__ __forceinline__ void dma16(i32x4 rsrc, unsigned lds_dst, int voff, int soff) {
  unsigned keep;
  asm volatile("s_mov_b32 %0, m0\n\ts_mov_b32 m0, %1\n\ts_nop 0\n\tbuffer_load_dwordx4 %2, %3, %4 offen lds\n\ts_mov_b32 m0, %0"
               : "=&s"(keep) : "s"(lds_dst), "v"(voff), "s"(rsrc), "s"(soff) : "memory");
}
__device__ __forceinline__ void touch4(i32x4 rsrc, unsigned lds_dst, int voff, int soff) {
  unsigned keep;
  asm volatile("s_mov_b32 %0, m0\n\ts_mov_b32 m0, %1\n\ts_nop 0\n\tbuffer_load_dword %2, %3, %4 offen lds\n\ts_mov_b32 m0, %0"
               : "=&s"(keep) : "s"(lds_dst), "v"(voff), "s"(rsrc), "s"(soff) : "memory");
}
template <int OFF> __device__ __forceinline__ void dma16x(i32x4 rsrc, unsigned lds_dst, int voff, int soff) {
  unsigned keep;
  asm volatile("s_mov_b32 %0, m0\n\ts_mov_b32 m0, %1\n\ts_nop 3\n\tbuffer_load_dwordx4 %2, %3, %4 offen offset:%5 lds\n\ts_mov_b32 m0, %0"
               : "=&s"(keep) : "s"(lds_dst), "v"(voff), "s"(rsrc), "s"(soff), "n"(OFF) : "memory");
}
template <int OFF> __device__ __forceinline__ void dma16x_sc1(i32x4 rsrc, unsigned lds_dst, int voff, int soff) {
  unsigned keep;
  asm volatile("s_mov_b32 %0, m0\n\ts_mov_b32 m0, %1\n\ts_nop 3\n\tbuffer_load_dwordx4 %2, %3, %4 offen offset:%5 sc1 lds\n\ts_mov_b32 m0, %0"
               : "=&s"(keep) : "s"(lds_dst), "v"(voff), "s"(rsrc), "s"(soff), "n"(OFF) : "memory");
}
template <int OFF> __device__ __forceinline__ void dma16x_nt(i32x4 rsrc, unsigned lds_dst, int voff, int soff) {
  unsigned keep;
  asm volatile("s_mov_b32 %0, m0\n\ts_mov_b32 m0, %1\n\ts_nop 3\n\tbuffer_load_dwordx4 %2, %3, %4 offen offset:%5 nt lds\n\ts_mov_b32 m0, %0"
               : "=&s"(keep) : "s"(lds_dst), "v"(voff), "s"(rsrc), "s"(soff), "n"(OFF) : "memory");
}
__device__ __forceinline__ void touch4_nt(i32x4 rsrc, unsigned lds_dst, int voff, int soff) {
  unsigned keep;
  asm volatile("s_mov_b32 %0, m0\n\ts_mov_b32 m0, %1\n\ts_nop 0\n\tbuffer_load_dword %2, %3, %4 offen nt lds\n\ts_mov_b32 m0, %0"
               : "=&s"(keep) : "s"(lds_dst), "v"(voff), "s"(rsrc), "s"(soff) : "memory");
}
#define WAIT_V(n) asm volatile("s_waitcnt vmcnt(%0)" ::"n"(n) : "memory")
#define WAIT_L0() asm volatile("s_waitcnt lgkmcnt(0)" ::: "memory")
#define BAR() __builtin_amdgcn_s_barrier()
#define ST_DECL
#define ST(k, i)
#define ST_FLUSH(k)

#define AUX_PF 2
#define PF_ON 0
#define PF_AT 3
#define AUX_ST 16
#define TOUCH 2
typedef float f32x2 __attribute__((ext_vector_type(2)));
constexpr int SL1 = 0, SL2 = 33280, SL3 = 98304, SL4 = 0, SLP = 131072, AROW = 1040;
constexpr int SCR = 66560;
constexpr int SC_PART = SCR;
constexpr int SC_HS   = SCR + 2048;
constexpr int SC_RS   = SCR + 3072;
constexpr int SC_MRS  = SCR + 3584;
constexpr int SC_CS   = SCR + 4096;
constexpr int SC_B1   = SCR + 7168;
constexpr int SC_DUM  = SCR + 8192;
#define PIN(x) asm volatile("" : "+v"(x))
#define SBAR() __builtin_amdgcn_sched_barrier(0)

template <int K> __device__ __forceinline__ void eh_op(const f32x16& ap, float (&v)[16], float (&t)[16], unsigned (&ow)[8], float scale, float center) {
  constexpr int st = K / 16, e = K % 16;
  if constexpr (st == 0) { v[e] = fmaf(-center, t[e], v[e]); PIN(v[e]); }
  else if constexpr (st == 1) { v[e] = fmaf(ap[e], scale, v[e]); PIN(v[e]); }
  else if constexpr (st == 2) { t[e] = v[e] * v[e]; PIN(t[e]); }
  else if constexpr (st == 3) { t[e] = fmaf(t[e], -0.1029432397f, -2.302208198f); PIN(t[e]); }
  else if constexpr (st == 4) { t[e] = t[e] * v[e]; PIN(t[e]); }
  else if constexpr (st == 5) { t[e] = __builtin_amdgcn_exp2f(t[e]); PIN(t[e]); }
  else if constexpr (st == 6) { t[e] = 1.f + t[e]; PIN(t[e]); }
  else if constexpr (st == 7) { t[e] = __builtin_amdgcn_rcpf(t[e]); PIN(t[e]); }
  else if constexpr (st == 8) { v[e] = v[e] * t[e]; PIN(v[e]); }
  else if constexpr (K < 152) { ow[e] = pk_bf16(v[2 * e], v[2 * e + 1]); PIN(ow[e]); }
}
template <int K> __device__ __forceinline__ void ea_op(const f32x16& ap, float (&v)[16], float (&t)[16], unsigned (&ow)[8], float cs) {
  constexpr int st = K / 16, e = K % 16;
  if constexpr (st == 0) { t[e] = ap[e] * t[e]; PIN(t[e]); }
  else if constexpr (st == 1) { t[e] = fmaf(-v[e], cs, t[e]); PIN(t[e]); }
  else if constexpr (K < 40) { ow[e] = pk_bf16(t[2 * e], t[2 * e + 1]); PIN(ow[e]); }
}
template <int ET, int K0, int K1> __device__ __forceinline__ void e_ops(const f32x16& ap, float (&v)[16], float (&t)[16], unsigned (&ow)[8], float s0, float s1) {
  if constexpr (K0 < K1) {
    if constexpr (ET == 1) eh_op<K0>(ap, v, t, ow, s0, s1); else if constexpr (ET == 2) ea_op<K0>(ap, v, t, ow, s0);
    e_ops<ET, K0 + 1, K1>(ap, v, t, ow, s0, s1);
  }
}
template <int MT> __device__ __forceinline__ bf16x8 wread(const char* pan, int ks) {
  if constexpr (MT == 1) { return *(const bf16x8*)(pan + ks * 32); }
  else {
    v4s t0 = __builtin_amdgcn_ds_read_tr16_b64_v4i16((LAS v4s*)(pan + ks * 1024));
    v4s t1 = __builtin_amdgcn_ds_read_tr16_b64_v4i16((LAS v4s*)(pan + ks * 1024 + 256));
    return (bf16x8)__builtin_shufflevector(t0, t1, 0, 1, 2, 3, 4, 5, 6, 7);
  }
}
__device__ __forceinline__ void row_stats(float sum, float sq, float& mean, float& inv, float& m_bf, float& scale, float& center) {
  mean = sum * (1.f / 512.f);
  const float var = fmaxf(sq * (1.f / 512.f) - mean * mean, 0.f);
  m_bf = rbf(mean);
  const float sd = rbf(sqrtf(rbf(rbf(var) + 1e-5f)));
  inv = 1.f / sd;
  const float m2 = rbf((mean - m_bf) * inv);
  const float v2 = rbf(var * inv * inv);
  const float sd2 = rbf(sqrtf(rbf(v2 + 1e-5f)));
  scale = inv / sd2; center = (m_bf + m2 * sd) * scale;
}
constexpr int WR = 8, WD = 3;
struct RowStat { float mean, var, m_bf, sd, inv, m2, v2, sd2, scale, center; };
template <int S> __device__ __forceinline__ void rs_stage(RowStat& r) {
  if constexpr (S == 0) { r.mean *= (1.f / 512.f); r.var = fmaxf(r.var * (1.f / 512.f) - r.mean * r.mean, 0.f); r.m_bf = rbf(r.mean); PIN(r.var); PIN(r.m_bf); }
  else if constexpr (S == 1) { r.sd = rbf(rbf(r.var) + 1e-5f); PIN(r.sd); }
  else if constexpr (S == 2) { r.sd = rbf(sqrtf(r.sd)); PIN(r.sd); }
  else if constexpr (S == 3) { r.inv = 1.f / r.sd; PIN(r.inv); }
  else if constexpr (S == 4) { r.m2 = rbf((r.mean - r.m_bf) * r.inv); r.v2 = rbf(rbf(r.var * r.inv * r.inv) + 1e-5f); PIN(r.m2); PIN(r.v2); }
  else if constexpr (S == 5) { r.sd2 = rbf(sqrtf(r.v2)); PIN(r.sd2); }
  else if constexpr (S == 6) { r.scale = r.inv / r.sd2; PIN(r.scale); }
  else if constexpr (S == 7) { r.center = (r.m_bf + r.m2 * r.sd) * r.scale; PIN(r.center); }
}
template <int MT, int ET, int NT, int G0, int KS, class Hook>
__device__ __forceinline__ void unit_steps(const char* pan, const char* pnx, const bf16x8 (&xf)[32], bf16x8 (&wf)[WR], f32x16& aw, const f32x16& ar,
                                           float (&v)[16], float (&t)[16], unsigned (&ow)[8], const float& s0, const float& s1, Hook&& hook) {
  if constexpr (KS < 32) {
    constexpr int NOPS = ET == 1 ? 152 : 40;
    if constexpr (KS == 0) {
      if constexpr (MT == 1) aw = __builtin_amdgcn_mfma_f32_32x32x16_bf16(xf[KS], wf[KS % WR], (f32x16){}, 0, 0, 0);
      else aw = __builtin_amdgcn_mfma_f32_32x32x16_bf16(wf[KS % WR], xf[KS], (f32x16){}, 0, 0, 0);
    } else {
      if constexpr (MT == 1) aw = __builtin_amdgcn_mfma_f32_32x32x16_bf16(xf[KS], wf[KS % WR], aw, 0, 0, 0);
      else aw = __builtin_amdgcn_mfma_f32_32x32x16_bf16(wf[KS % WR], xf[KS], aw, 0, 0, 0);
    }
    PIN(aw);
    SBAR();
    if constexpr (KS + WD < 32) wf[(KS + WD) % WR] = wread<MT>(pan, KS + WD);
    else if constexpr (NT >= 0) wf[(KS + WD) % WR] = wread<NT>(pnx, KS + WD - 32);
    asm volatile("" :: "v"(wf[KS % WR]), "v"(wf[(KS + WR - 1) % WR]));
    SBAR();
    hook(std::integral_constant<int, KS>{});
    constexpr int k0 = KS < G0 ? 0 : (KS - G0) * NOPS / (32 - G0), k1 = KS < G0 ? 0 : (KS + 1 - G0) * NOPS / (32 - G0);
    e_ops<ET, k0, k1>(ar, v, t, ow, s0, s1);
    SBAR();
    unit_steps<MT, ET, NT, G0, KS + 1>(pan, pnx, xf, wf, aw, ar, v, t, ow, s0, s1, hook);
  }
}

__device__ __forceinline__ void phase1(const u16* __restrict__ x, const u16* __restrict__ wre, const u16* __restrict__ wim,
                                       const u16* __restrict__ w1, const u16* __restrict__ b1, const u16* __restrict__ w2,
                                       const u16* __restrict__ cre, const u16* __restrict__ cim, char* __restrict__ ws, char* lds, const u16* outp) {
  const int tid = threadIdx.x, lane = tid & 63, wid = __builtin_amdgcn_readfirstlane(tid >> 6);
  const int b = blockIdx.x, xcd = b & 7, idx = b >> 3;
  const int rt = (xcd >> 1) * 4 + (idx & 3);
  const int cg = (xcd & 1) * 8 + (idx >> 2);
  const int h0 = cg < 11 ? cg * 3 : 33 + (cg - 11) * 2;
  const bool h3 = cg < 11;
  const int a0 = cg * 2;
  const int m = wid & 3;
  const bool helper = wid >= 4;
  const int r31 = lane & 31, hh = lane >> 5;
  const unsigned lds0 = (unsigned)(size_t)(LAS char*)lds;
  const int prot = idx & 3;
  ST_DECL;
  ST(0, 0);

  const int hoff = (lane >> 2) * (F_ * 2) + (lane & 3) * 16;
  const i32x4 rw1 = mk_rsrc(w1);
  auto piece_h = [&](int u, int q, int slot_off) { dma16(rw1, lds0 + slot_off + q * 1024, hoff, q * 16 * (F_ * 2) + u * 64); };
  const i32x4 rx = mk_rsrc(x + (size_t)(rt * 128 + m * 32) * D_);
  int xo[4];
#pragma unroll
  for (int i = 0; i < 4; ++i) { const int r = i * 4 + (lane >> 4); xo[i] = r * 1024 + (((lane & 15) ^ r) * 16); }
  auto quarter = [&](int q) {
#pragma unroll
    for (int i = 0; i < 8; ++i) dma16(rx, lds0 + q * 32768 + m * 8192 + i * 1024, xo[i & 3], q * 256 + (i >> 2) * 16384);
    piece_h(h0, q * 8 + m * 2, SLP); piece_h(h0, q * 8 + m * 2 + 1, SLP);
  };
  if (helper) { quarter(0); quarter(1); }

  const auto rws = __builtin_amdgcn_make_buffer_rsrc((void*)ws, 0, 0x7fffffff, 0x00020000);
  const int xrow = m * 32 + swap23(r31);
  const unsigned xbase = xrow * 256 + ((hh ^ (xrow & 15)) * 16);
#define QWAIT(q) WAIT_V(9 + 10 * (3 - (q)))

  if (helper) {
    const auto rw2t = __builtin_amdgcn_make_buffer_rsrc((void*)(ws + WS_W2), 0, 16 * NSUB * 1024, 0x00020000);
    const i32x4 rwa = mk_rsrc((a0 < 16 ? wre : wim) + (size_t)(a0 & 15) * 32 * D_);
    auto panel_h = [&](int u, int slot_off) {
#pragma unroll
      for (int k = 0; k < 8; ++k) piece_h(u, m * 8 + ((k + 2 * prot) & 7), slot_off);
    };
    auto panel_a = [&](int w, int slot_off) {
#pragma unroll
      for (int k = 0; k < 8; ++k) { const int q = m * 8 + ((k + 2 * prot) & 7); dma16(rwa, lds0 + slot_off + q * AROW, lane * 16, (w * 32 + q) * 1024); }
    };
    const v4s ones = {0x3f80, 0x3f80, 0x3f80, 0x3f80};
    const char* pzh = lds + ((lane >> 2) & 15) * 64 + (m & 1) * 32 + (lane & 3) * 8;
    auto cs_h_part = [&](f32x4 (&c)[4], int slot_off, int j0, int n) {
#pragma unroll
      for (int j = 0; j < n; ++j)
        c[j & 3] = __builtin_amdgcn_mfma_f32_4x4x4bf16_1k(ones, __builtin_amdgcn_ds_read_tr16_b64_v4i16((LAS v4s*)(pzh + slot_off + (j0 + j) * 1024)), c[j & 3], 0, 0, 0);
    };
    auto cs_h_fin = [&](f32x4 (&c)[4], int i) {
      float r = (c[0][0] + c[1][0]) + (c[2][0] + c[3][0]);
      r += __shfl_xor(r, 16); r += __shfl_xor(r, 32);
      if (lane < 16) *(float*)(lds + SC_CS + i * 512 + (m & 1) * 64 + lane * 4) = r;
    };
    auto cs_h = [&](int i, int slot_off) {
      if ((m >> 1) == (i & 1)) { f32x4 c[4] = {}; cs_h_part(c, slot_off, 0, 32); cs_h_fin(c, i); }
    };
    auto cs_a = [&](int i, int slot_off) {
      const char* pz = lds + slot_off + swap23(r31) * AROW + hh * 16 + m * 256;
      f32x4 c0 = {}, c1 = {};
#pragma unroll
      for (int j = 0; j < 8; ++j) {
        const bf16x8 w = *(const bf16x8*)(pz + j * 32);
        c0 = __builtin_amdgcn_mfma_f32_4x4x4bf16_1k(ones, __builtin_shufflevector(w, w, 0, 1, 2, 3), c0, 0, 0, 0);
        c1 = __builtin_amdgcn_mfma_f32_4x4x4bf16_1k(ones, __builtin_shufflevector(w, w, 4, 5, 6, 7), c1, 0, 0, 0);
      }
      float r = c0[0] + c1[0]; r += __shfl_xor(r, 32);
      if (lane < 32) *(float*)(lds + SC_CS + i * 512 + m * 128 + lane * 4) = r;
    };
    auto prefetch_c = [&]() {
      const int c2 = xcd >> 1;
      const int row = (xcd & 1) * 1024 + idx * 32 + m * 8 + (lane >> 4);
      const i32x4 rc = mk_rsrc(c2 < 2 ? cre : cim);
      const int vo = row * 4096 + (lane & 15) * 128;
      if (AUX_PF == 2) { touch4_nt(rc, lds0 + SC_DUM + m * 256, vo, (c2 & 1) * 2048); touch4_nt(rc, lds0 + SC_DUM + m * 256, vo, (c2 & 1) * 2048 + 4 * 4096); }
      else { touch4(rc, lds0 + SC_DUM + m * 256, vo, (c2 & 1) * 2048); touch4(rc, lds0 + SC_DUM + m * 256, vo, (c2 & 1) * 2048 + 4 * 4096); }
    };
    __builtin_amdgcn_s_setprio(2);
    {
      f32x4 c[4] = {};
      WAIT_V(10); ST(0, 1); BAR();
      quarter(2);
      if (m < 2) cs_h_part(c, SLP, 0, 8);
      WAIT_V(10); BAR();
      quarter(3);
      panel_h(h0 + 1, SL1);
      if (m < 2) cs_h_part(c, SLP, 8, 8);
      WAIT_V(18); BAR();
      unsigned w2v[2][8], b1v;
      int w2dst[2];
      {
        i32x4 rw2 = mk_rsrc(w2); rw2.z = F_ * D_ * 2;
#pragma unroll
        for (int r = 0; r < 2; ++r) {
          const int hl = r * 256 + (tid - 256), cc = b * 352 + hl;
          const int ln = cc & 63, rest = cc >> 6, ct = rest / NSUB, sub = rest - ct * NSUB;
          const int off = hl < 352 ? ((16 * sub + 4 * (ln >> 5)) * D_ + 32 * ct + swap23(ln & 31)) * 2 : 0x7ff00000;
          w2dst[r] = hl < 352 ? cc * 16 : 0x7ffffff0;
          asm volatile("buffer_load_ushort %0, %8, %10, 0 offen\n\tbuffer_load_ushort %1, %8, %10, 0 offen offset:1024\n\t"
                       "buffer_load_ushort %2, %8, %10, 0 offen offset:2048\n\tbuffer_load_ushort %3, %8, %10, 0 offen offset:3072\n\t"
                       "buffer_load_ushort %4, %9, %10, 0 offen\n\tbuffer_load_ushort %5, %9, %10, 0 offen offset:1024\n\t"
                       "buffer_load_ushort %6, %9, %10, 0 offen offset:2048\n\tbuffer_load_ushort %7, %9, %10, 0 offen offset:3072"
                       : "=&v"(w2v[r][0]), "=&v"(w2v[r][1]), "=&v"(w2v[r][2]), "=&v"(w2v[r][3]), "=&v"(w2v[r][4]), "=&v"(w2v[r][5]), "=&v"(w2v[r][6]), "=&v"(w2v[r][7])
                       : "v"(off), "v"(off + 8192), "s"(rw2) : "memory");
        }
        const int bo = h0 * 32 + lane * 2;
        asm volatile("buffer_load_dword %0, %1, %2, 0 offen" : "=&v"(b1v) : "v"((bo < F_ - 2 ? bo : F_ - 2) * 2), "s"(mk_rsrc(b1)) : "memory");
      }
      if (m < 2) cs_h_part(c, SLP, 16, 8);
      WAIT_V(25); BAR();
      ST(0, 2);
      panel_a(0, SL2);
      if (m < 2) { cs_h_part(c, SLP, 24, 8); cs_h_fin(c, 0); }
      WAIT_V(8);
      asm volatile("" : "+v"(w2v[0][0]), "+v"(w2v[0][1]), "+v"(w2v[0][2]), "+v"(w2v[0][3]), "+v"(w2v[0][4]), "+v"(w2v[0][5]), "+v"(w2v[0][6]), "+v"(w2v[0][7]), "+v"(b1v));
      asm volatile("" : "+v"(w2v[1][0]), "+v"(w2v[1][1]), "+v"(w2v[1][2]), "+v"(w2v[1][3]), "+v"(w2v[1][4]), "+v"(w2v[1][5]), "+v"(w2v[1][6]), "+v"(w2v[1][7]));
      if (m == 3 && lane < 48) *(f32x2*)(lds + SC_B1 + lane * 8) = (f32x2){bflo(b1v), bfhi(b1v)};
#pragma unroll
      for (int r = 0; r < 2; ++r) {
        const u32x4 w = {w2v[r][0] | (w2v[r][1] << 16), w2v[r][2] | (w2v[r][3] << 16), w2v[r][4] | (w2v[r][5] << 16), w2v[r][6] | (w2v[r][7] << 16)};
        __builtin_amdgcn_raw_buffer_store_b128(w, rw2t, w2dst[r], 0, AUX_ST);
      }
    }
    ST(0, 3);
    WAIT_V(2); WAIT_L0(); BAR();
    panel_h(h3 ? h0 + 2 : h0, SL3);
    cs_h(1, SL1);
    {
      float mean, inv, m_bf, sc_, ce_;
      const f32x2 pr = *(const f32x2*)(lds + SC_PART + (m * 32 + r31) * 8);
      row_stats(pr.x, pr.y, mean, inv, m_bf, sc_, ce_);
      const float sg = a0 < 16 ? inv : -inv;
      if (lane < 32) { *(float*)(lds + SC_RS + (m * 32 + lane) * 4) = sg; *(float*)(lds + SC_MRS + (m * 32 + lane) * 4) = m_bf * sg; }
    }
    WAIT_V(0); WAIT_L0(); BAR();
    panel_a(1, SL4);
    if (PF_ON && PF_AT == 3) prefetch_c();
    cs_a(2, SL2);
    WAIT_V(0);
    WAIT_L0(); BAR();
    if (PF_ON && PF_AT == 4) prefetch_c();
    cs_h(3, SL3);
    WAIT_L0(); BAR();
    cs_a(4, SL4);
    WAIT_L0(); BAR();
    __builtin_amdgcn_s_setprio(0);
    WAIT_V(0);
    ST(0, 5); ST_FLUSH(0);
    return;
  }

  bf16x8 xf[32];
  unsigned long long clk0 = 0; (void)clk0;
  const int wrow = swap23(r31);
  const unsigned wlA = (unsigned)(wrow * AROW + hh * 16);
  const unsigned wlH = (unsigned)((8 * hh + ((lane & 15) >> 2)) * 64 + ((lane >> 4) & 1) * 32 + (lane & 3) * 8);
  f32x16 acc, ac2;
  bf16x8 wf[WR];
  float v[16], t[16]; unsigned ow[8];
  auto nohook = [](auto) {};
  RowStat rs;
  const int hstore = (swap23(r31) + 32 * hh) * 16;
  const int s0r = rt * 128 + m * 32;

  auto tables_h = [&](int i, int hu) {
#pragma unroll
    for (int g4 = 0; g4 < 4; ++g4) {
      const f32x4 c = *(const f32x4*)(lds + SC_CS + i * 512 + (8 * g4 + 4 * hh) * 4), bq = *(const f32x4*)(lds + SC_B1 + hu * 128 + (8 * g4 + 4 * hh) * 4);
#pragma unroll
      for (int e = 0; e < 4; ++e) { t[g4 * 4 + e] = c[e]; v[g4 * 4 + e] = bq[e]; }
    }
  };
  auto tables_a = [&](int i) -> float {
#pragma unroll
    for (int g4 = 0; g4 < 4; ++g4) {
      const f32x4 c = *(const f32x4*)(lds + SC_RS + (m * 32 + 8 * g4 + 4 * hh) * 4), bq = *(const f32x4*)(lds + SC_MRS + (m * 32 + 8 * g4 + 4 * hh) * 4);
#pragma unroll
      for (int e = 0; e < 4; ++e) { t[g4 * 4 + e] = c[e]; v[g4 * 4 + e] = bq[e]; }
    }
    const char* cq = lds + SC_CS + i * 512 + r31 * 4;
    return (*(const float*)cq + *(const float*)(cq + 128)) + (*(const float*)(cq + 256) + *(const float*)(cq + 384));
  };
  auto tables_a4 = [&](int i, float (&cp)[4]) {
#pragma unroll
    for (int g4 = 0; g4 < 4; ++g4) {
      const f32x4 c = *(const f32x4*)(lds + SC_RS + (m * 32 + 8 * g4 + 4 * hh) * 4), bq = *(const f32x4*)(lds + SC_MRS + (m * 32 + 8 * g4 + 4 * hh) * 4);
#pragma unroll
      for (int e = 0; e < 4; ++e) { t[g4 * 4 + e] = c[e]; v[g4 * 4 + e] = bq[e]; }
      cp[g4] = *(const float*)(lds + SC_CS + i * 512 + g4 * 128 + r31 * 4);
    }
  };
  auto store_h = [&](int hu, const unsigned (&ow)[8]) {
    const int dst = (int)WS_H + (((rt * 4 + m) * NSUB + 2 * (h0 + hu)) << 10);
    if (hu < 2 || h3) {
      __builtin_amdgcn_raw_buffer_store_b128((u32x4){ow[0], ow[1], ow[2], ow[3]}, rws, hstore, dst, AUX_ST);
      __builtin_amdgcn_raw_buffer_store_b128((u32x4){ow[4], ow[5], ow[6], ow[7]}, rws, hstore + 1024, dst, AUX_ST);
    }
    if (h0 + hu == 42 && (hu < 2 || h3)) { u32x4 z = {0u, 0u, 0u, 0u}; __builtin_amdgcn_raw_buffer_store_b128(z, rws, lane * 16 + 2048, dst, AUX_ST); __builtin_amdgcn_raw_buffer_store_b128(z, rws, lane * 16 + 3072, dst, AUX_ST); }
  };
  auto store_a = [&](int w, const unsigned (&ow)[8]) {
    const int u = a0 + w, et = u & 15, j = et >> 2, nt = et & 3;
    const int kk = (u >> 4) * 32 + (s0r >> 6), sub = (s0r & 63) >> 4;
    const int dst = (int)WS_B + (((j * NKK_B + kk) * 16 + nt * 4 + sub) << 10);
    __builtin_amdgcn_raw_buffer_store_b128((u32x4){ow[0], ow[1], ow[2], ow[3]}, rws, lane * 16, dst, AUX_ST);
    __builtin_amdgcn_raw_buffer_store_b128((u32x4){ow[4], ow[5], ow[6], ow[7]}, rws, lane * 16 + 1024, dst, AUX_ST);
  };
#define PGS 15
  unsigned ptv = 0;
  if (wid == 0) {
    const int P = idx + 32 * lane;
    const int u = 1 << (20 - PGS);
    const char* p; int k;
    if (P < 20 * u) { p = ws; k = P; }
    else if (P < 28 * u) { p = (const char*)cre; k = P - 20 * u; }
    else if (P < 36 * u) { p = (const char*)cim; k = P - 28 * u; }
    else if (P < 38 * u) { p = (const char*)x; k = P - 36 * u; }
    else if (P < 40 * u) { p = (const char*)outp; k = P - 38 * u; }
    else if (P < 41 * u) { p = (const char*)w1; k = P - 40 * u; }
    else if (P < 42 * u) { p = (const char*)w2; k = P - 41 * u; }
    else if (P < 42 * u + u / 2) { p = (const char*)wre; k = P - 42 * u; }
    else if (P < 43 * u) { p = (const char*)wim; k = P - 42 * u - u / 2; }
    else { p = (const char*)x; k = 0; }
    p += (size_t)k << PGS;
    asm volatile("global_load_dword %0, %1, off" : "=v"(ptv) : "v"(p) : "memory");
  }
  {
    f32x4 ss0 = {}, ss1 = {}, sq0 = {}, sq1 = {};
    const v4s ones4 = {0x3f80, 0x3f80, 0x3f80, 0x3f80};
    acc = (f32x16){};
    const char* pan = lds + SLP + wlH;
#pragma unroll
    for (int q = 0; q < 4; ++q) {
      if (q == 0) ST(0, 1);
      BAR();
      if (q == 3) ST(0, 2);
      bf16x8 w8[8];
#pragma unroll
      for (int k = 0; k < 8; ++k) xf[q * 8 + k] = *(const bf16x8*)(lds + q * 32768 + (xbase ^ (k * 32)));
#pragma unroll
      for (int k = 0; k < 8; ++k) w8[k] = wread<0>(pan, q * 8 + k);
      SBAR();
#pragma unroll
      for (int k = 0; k < 8; ++k) {
        const v4s lo4 = __builtin_shufflevector(xf[q * 8 + k], xf[q * 8 + k], 0, 1, 2, 3), hi4 = __builtin_shufflevector(xf[q * 8 + k], xf[q * 8 + k], 4, 5, 6, 7);
        ss0 = __builtin_amdgcn_mfma_f32_4x4x4bf16_1k(ones4, lo4, ss0, 0, 0, 0); sq0 = __builtin_amdgcn_mfma_f32_4x4x4bf16_1k(lo4, lo4, sq0, 0, 0, 0);
        ss1 = __builtin_amdgcn_mfma_f32_4x4x4bf16_1k(ones4, hi4, ss1, 0, 0, 0); sq1 = __builtin_amdgcn_mfma_f32_4x4x4bf16_1k(hi4, hi4, sq1, 0, 0, 0);
      }
      asm volatile("" : "+v"(ss0), "+v"(ss1), "+v"(sq0), "+v"(sq1));
      SBAR();
#pragma unroll
      for (int k = 0; k < 8; ++k) { acc = __builtin_amdgcn_mfma_f32_32x32x16_bf16(w8[k], xf[q * 8 + k], acc, 0, 0, 0); PIN(acc); }
      WAIT_L0(); SBAR();
    }
    const f32x4 sqv = sq0 + sq1;
    float sum = ss0[0] + ss1[0], sq = (lane & 2) ? ((lane & 1) ? sqv[3] : sqv[2]) : ((lane & 1) ? sqv[1] : sqv[0]);
    sum += __shfl_xor(sum, 32); sq += __shfl_xor(sq, 32);
    if (lane < 32) *(f32x2*)(lds + SC_PART + (m * 32 + lane) * 8) = (f32x2){sum, sq};
    rs.mean = sum; rs.var = sq;
  }
  ST(0, 8);
  WAIT_V(0); PIN(ptv);
  WAIT_L0(); BAR();
  ST(0, 4);
  const char* pH1 = lds + SL1 + wlH; const char* pA0 = lds + SL2 + wlA;
  const char* p3H = lds + SL3 + wlH; const char* pA1 = lds + SL4 + wlA;
#pragma unroll
  for (int e = 0; e < WD; ++e) wf[e] = wread<0>(pH1, e);
  wf[WR - 1] = wf[0];
  tables_h(0, 0);
  float csa = 0.f;
  unit_steps<0, 1, 1, 8, 0>(pH1, pA0, xf, wf, ac2, acc, v, t, ow, rs.scale, rs.center, [&](auto g) {
    constexpr int G = decltype(g)::value;
    if constexpr (G < 8) rs_stage<G>(rs);
  });
  store_h(0, ow);
  ST(0, 9);
  BAR();
  ST(0, 6);
  {
    tables_h(1, 1);
    unit_steps<1, 1, 0, 3, 0>(pA0, p3H, xf, wf, acc, ac2, v, t, ow, rs.scale, rs.center, nohook); store_h(1, ow);
    ST(0, 10);
    BAR();
    ST(0, 7);
    float cp[4]; tables_a4(2, cp);
    unit_steps<0, 2, 1, 3, 0>(p3H, pA1, xf, wf, ac2, acc, v, t, ow, csa, csa, [&](auto g) { if constexpr (decltype(g)::value == 2) { csa = (cp[0] + cp[1]); PIN(csa); float c2 = cp[2] + cp[3]; PIN(c2); csa += c2; PIN(csa); } }); store_a(0, ow);
    ST(0, 11);
    BAR();
    ST(0, 13);
    tables_h(3, 2);
    unit_steps<1, 1, -1, 3, 0>(pA1, pA1, xf, wf, acc, ac2, v, t, ow, rs.scale, rs.center, nohook); store_h(2, ow);
    ST(0, 12);
    BAR();
    csa = tables_a(4);
    e_ops<2, 0, 40>(acc, v, t, ow, csa, csa);
    store_a(1, ow);
  }
  WAIT_V(0);
  ST(0, 5); ST_FLUSH(0);
}

#define AUX_C 2
typedef __attribute__((address_space(1))) unsigned long long gu64;
__device__ __forceinline__ void phase2(const u16* __restrict__ x, const u16* __restrict__ cre, const u16* __restrict__ cim,
                                       const u16* __restrict__ b2, char* __restrict__ ws, u16* __restrict__ out, char* lds, const unsigned long long tag) {
  const int tid = threadIdx.x, lane = tid & 63, wid = __builtin_amdgcn_readfirstlane(tid >> 6);
  const int b = blockIdx.x, xcd = b & 7, idx = b >> 3;
#define MAP2 0
  const int c = MAP2 ? (xcd >> 2) * 2 + (idx >> 4) : xcd >> 1, rt = MAP2 ? (xcd & 3) * 4 + ((idx >> 2) & 3) : (xcd & 1) * 8 + (idx >> 2), j = idx & 3;
  ST_DECL;
  ST(1, 0);

#define SB() __builtin_amdgcn_sched_barrier(0)

  if (wid >= 4) {
    const int lw = wid - 4;
    int aoff[4];
#pragma unroll
    for (int i = 0; i < 4; ++i) { int row = lw * 32 + i * 8 + (lane >> 3); aoff[i] = row * 4096 + (((lane & 7) ^ ((row >> 1) & 7)) * 16) - i * 1024 + 4096; }
    const unsigned ldsw = (unsigned)(size_t)(LAS char*)lds + lw * 4096;
    const i32x4 ra_s = mk_rsrc((const char*)(c < 2 ? cre : cim) + (size_t)rt * 128 * 4096 + (c & 1) * 2048 - 4096);
    const i32x4 rb_s = mk_rsrc(ws + WS_B + ((size_t)(j * NKK_B + c * 16) << 14) + lw * 4096);
    const i32x4 ra_o = mk_rsrc(ws + WS_H + ((size_t)((rt * 4 + 2 * (lw >> 1) + (c >> 1)) * NSUB) << 10) + (lw & 1) * 4096 - (16 << 13));
    const i32x4 rb_o = mk_rsrc(ws + WS_W2 + ((size_t)((j * 4 + 2 * (lw >> 1) + (c & 1)) * NSUB) << 10) + (lw & 1) * 4096 - (16 << 13));
    const int voff = lane * 16;
    const int r8 = idx >> 2;
#define LEAD 0
    const int ea = LEAD ? __builtin_amdgcn_readfirstlane(lw == j ? 1 : 0) : 1, eb0 = LEAD ? __builtin_amdgcn_readfirstlane(r8 == 2 * lw ? 1 : 0) : 1,
              eb1 = LEAD ? __builtin_amdgcn_readfirstlane(r8 == 2 * lw + 1 ? 1 : 0) : 1;
    auto issue_a = [&](int T) {
      const unsigned dst = __builtin_amdgcn_readfirstlane(ldsw + (T & 3) * 32768);
      if (T < 16) { const int so = __builtin_amdgcn_readfirstlane(T * 128);
                    dma16x_nt<0>(ra_s, dst, aoff[0], so); dma16x_nt<1024>(ra_s, dst, aoff[1], so); dma16x_nt<2048>(ra_s, dst, aoff[2], so); dma16x_nt<3072>(ra_s, dst, aoff[3], so); }
      else { i32x4 r = ra_o; r.z = __builtin_amdgcn_readfirstlane(T < 27 ? 0x7fffffff : 0); const int so = __builtin_amdgcn_readfirstlane(T << 13);
             dma16x_sc1<0>(r, dst, voff, so); dma16x_sc1<1024>(r, dst, voff, so); dma16x_sc1<2048>(r, dst, voff, so); dma16x_sc1<3072>(r, dst, voff, so); }
    };
    auto issue_b = [&](int T, int half) {
      const unsigned dst = __builtin_amdgcn_readfirstlane(ldsw + (T & 3) * 32768 + 16384 + half * 2048);
      i32x4 r = T < 16 ? rb_s : rb_o; r.z = T < 27 ? 0x7fffffff : 0;
      const int so = __builtin_amdgcn_readfirstlane((T < 16 ? T << 14 : T << 13) + half * 2048);
      dma16x_sc1<0>(r, dst, voff, so); dma16x_sc1<1024>(r, dst, voff, so);
    };
    issue_a(0); issue_a(1); issue_a(2);
    BAR();
    issue_b(0, 0); issue_b(0, 1); issue_b(1, 0); issue_b(1, 1); issue_b(2, 0); issue_b(2, 1);
    for (int u = 0; u < 27; ++u) {
      if (u == 0) WAIT_V(8); else if (u == 1) WAIT_V(12); else WAIT_V(16);
      BAR();
      issue_a(u + 3); issue_b(u + 3, 0); issue_b(u + 3, 1);
    }
    WAIT_V(0);
    return;
  }

  const int r31 = lane & 31, hh = lane >> 5;
  const int wm = wid >> 1, wn = wid & 1;
  unsigned ab[4];
#pragma unroll
  for (int s = 0; s < 4; ++s) ab[s] = wm * 8192 + (unsigned)(r31 * 128 + (((2 * s + hh) ^ ((r31 >> 1) & 7)) * 16));
  const unsigned bb = 16384 + wn * 8192 + lane * 16;
  const unsigned ao = wm * 8192 + lane * 16;

  f32x16 acc[2][2] = {};
  f32x16 acco = {};
  bf16x8 F0[16], F1[16];
#define RD(F, s) do { F[4 * (s) + 0] = *(const bf16x8*)(sl + ab[s]); F[4 * (s) + 1] = *(const bf16x8*)(sl + ab[s] + 4096); \
                      F[4 * (s) + 2] = *(const bf16x8*)(sl + bb + (s) * 1024); F[4 * (s) + 3] = *(const bf16x8*)(sl + bb + (s) * 1024 + 4096); } while (0)
#define RDO(F, s) do { F[4 * (s) + 0] = *(const bf16x8*)(sl + ao + (s) * 2048); F[4 * (s) + 1] = *(const bf16x8*)(sl + bb + (s) * 2048); \
                       F[4 * (s) + 2] = *(const bf16x8*)(sl + ao + (s) * 2048 + 1024); F[4 * (s) + 3] = *(const bf16x8*)(sl + bb + (s) * 2048 + 1024); } while (0)
#define MM0(F, s) acc[0][0] = __builtin_amdgcn_mfma_f32_32x32x16_bf16(F[4 * (s) + 2], F[4 * (s) + 0], acc[0][0], 0, 0, 0)
#define MM1(F, s) acc[0][1] = __builtin_amdgcn_mfma_f32_32x32x16_bf16(F[4 * (s) + 3], F[4 * (s) + 0], acc[0][1], 0, 0, 0)
#define MM2(F, s) acc[1][0] = __builtin_amdgcn_mfma_f32_32x32x16_bf16(F[4 * (s) + 2], F[4 * (s) + 1], acc[1][0], 0, 0, 0)
#define MM3(F, s) acc[1][1] = __builtin_amdgcn_mfma_f32_32x32x16_bf16(F[4 * (s) + 3], F[4 * (s) + 1], acc[1][1], 0, 0, 0)
#define MO(F, s) do { acco = __builtin_amdgcn_mfma_f32_32x32x16_bf16(F[4 * (s) + 1], F[4 * (s) + 0], acco, 0, 0, 0); \
                      acco = __builtin_amdgcn_mfma_f32_32x32x16_bf16(F[4 * (s) + 3], F[4 * (s) + 2], acco, 0, 0, 0); } while (0)
#define PERIOD(u, RDX, FR, FM) do { \
    const char* sl = lds + ((u) & 3) * 32768; \
    BAR(); SB(); \
    MM0(FM, 0); SB(); RDX(FR, 0); SB(); MM1(FM, 0); SB(); MM2(FM, 0); SB(); RDX(FR, 1); SB(); MM3(FM, 0); SB(); \
    MM0(FM, 1); SB(); RDX(FR, 2); SB(); MM1(FM, 1); SB(); MM2(FM, 1); SB(); RDX(FR, 3); SB(); MM3(FM, 1); SB(); \
    MM0(FM, 2); MM1(FM, 2); MM2(FM, 2); MM3(FM, 2); \
    MM0(FM, 3); MM1(FM, 3); MM2(FM, 3); SB(); WAIT_L0(); SB(); MM3(FM, 3); SB(); \
  } while (0)
#define PERIODO(u, FR, FM) do { \
    const char* sl = lds + ((u) & 3) * 32768; \
    BAR(); SB(); \
    RDO(FR, 0); SB(); MO(FM, 0); SB(); RDO(FR, 1); SB(); MO(FM, 1); SB(); RDO(FR, 2); SB(); MO(FM, 2); SB(); RDO(FR, 3); SB(); MO(FM, 3); SB(); \
    WAIT_L0(); SB(); \
  } while (0)
#define K2PF 0
#define K2PF_POS 0
  const unsigned dum = (unsigned)(size_t)(LAS char*)lds + 131072 + wid * 256;
  auto touches = [&]() {
    if (K2PF & 1) {
      const i32x4 rb = mk_rsrc(ws + WS_B + ((size_t)(j * NKK_B + c * 16) << 14));
      touch4(rb, dum, ((lane >> 4) + 4 * wid) * 16384 + ((idx >> 2) * 16 + (lane & 15)) * 128, 0);
    }
    if (K2PF & 2) {
      const i32x4 rc = mk_rsrc((const char*)(c < 2 ? cre : cim) + (size_t)(rt * 128 + j * 32) * 4096 + (c & 1) * 2048);
      const int vo = r31 * 4096 + (2 * wid + hh) * 128;
      touch4_nt(rc, dum, vo, 0); touch4_nt(rc, dum, vo, 1024);
    }
  };
  gu64* fl = (gu64*)(ws + WS_CNT);
  if (wid == 0) {
    gu64* f16 = fl + ((4 * (c >> 1) + j) * 2 + (c & 1)) * 16 + (lane & 15);
    for (unsigned spins = 0;; ++spins) {
      const unsigned long long f = __hip_atomic_load(f16, __ATOMIC_RELAXED, __HIP_MEMORY_SCOPE_AGENT);
      if (__all(f == tag) || spins > (1u << 20)) break;
      __builtin_amdgcn_s_sleep(1);
    }
    asm volatile("" ::: "memory");
    ST(1, 7);
  }
  BAR();
  {
    const char* sl = lds;
    BAR(); SB(); ST(1, 1);
    if (K2PF_POS == 1) touches();
    RD(F0, 0); RD(F0, 1); RD(F0, 2); RD(F0, 3); SB();
    WAIT_L0(); SB();
  }
  for (int u = 1; u < 15; u += 2) {
    if ((K2PF & 4) && u == 7) {
      const i32x4 rh = mk_rsrc(ws + WS_H + ((size_t)((rt * 4 + 2 * (wid & 1) + (c >> 1)) * NSUB) << 10) + j * 22528);
      const int l2 = (wid >> 1) * 88 + lane;
      touch4(rh, dum, l2 * 128, 0);
      touch4(rh, dum, (lane < 24 ? l2 + 64 : l2) * 128, 0);
      const i32x4 rw = mk_rsrc(ws + WS_W2 + ((size_t)((j * 4 + 2 * (wid & 1) + (c & 1)) * NSUB) << 10) + (idx >> 2) * 11264);
      touch4(rw, dum, ((wid >> 1) * 44 + (lane < 44 ? lane : 0)) * 128, 0);
    }
    PERIOD(u, RD, F1, F0);
    PERIOD(u + 1, RD, F0, F1);
    if (u == 11 && wid == 0) {
      for (unsigned spins = 0;; ++spins) {
        const unsigned long long f0 = __hip_atomic_load(fl + lane * 4, __ATOMIC_RELAXED, __HIP_MEMORY_SCOPE_AGENT), f1 = __hip_atomic_load(fl + lane * 4 + 1, __ATOMIC_RELAXED, __HIP_MEMORY_SCOPE_AGENT),
                                 f2 = __hip_atomic_load(fl + lane * 4 + 2, __ATOMIC_RELAXED, __HIP_MEMORY_SCOPE_AGENT), f3 = __hip_atomic_load(fl + lane * 4 + 3, __ATOMIC_RELAXED, __HIP_MEMORY_SCOPE_AGENT);
        if (__all(f0 == tag && f1 == tag && f2 == tag && f3 == tag) || spins > (1u << 20)) break;
        __builtin_amdgcn_s_sleep(1);
      }
      asm volatile("" ::: "memory");
    }
  }
  PERIOD(15, RD, F1, F0);
  PERIOD(16, RDO, F0, F1);
  ST(1, 2);
  const int tile = rt * 4 + j;
  const auto rs = __builtin_amdgcn_make_buffer_rsrc(ws + WS_SLAB + ((size_t)tile << 17), 0, (ABL & 16) ? 0 : (1 << 17), 0x00020000);
  const unsigned lo = wid * 2048 + lane * 16;
#pragma unroll
  for (int q = 0; q < 4; ++q) {
    if (q != c) {
      const f32x16 v = acc[q >> 1][q & 1];
#pragma unroll
      for (int hf = 0; hf < 2; ++hf) {
        const u32x4 w = {pk_bf16(v[hf * 8], v[hf * 8 + 1]), pk_bf16(v[hf * 8 + 2], v[hf * 8 + 3]), pk_bf16(v[hf * 8 + 4], v[hf * 8 + 5]), pk_bf16(v[hf * 8 + 6], v[hf * 8 + 7])};
        __builtin_amdgcn_raw_buffer_store_b128(w, rs, (q * 4 + c) * 8192 + lo + hf * 1024, 0, 16);
      }
    }
  }
  const f32x16 own = c == 0 ? acc[0][0] : (c == 1 ? acc[0][1] : (c == 2 ? acc[1][0] : acc[1][1]));
  const int trow = rt * 128 + wm * 64 + (c >> 1) * 32 + r31;
  const int ecol = j * 128 + wn * 64 + (c & 1) * 32 + 8 * hh;
  u32x4 xv[2], bv[2];
#pragma unroll
  for (int half = 0; half < 2; ++half) { xv[half] = *(const u32x4*)(x + (size_t)trow * D_ + ecol + 16 * half); bv[half] = *(const u32x4*)(b2 + ecol + 16 * half); }
  SB();
  gu64* xfl = (gu64*)(ws + WS_CNT + 4096 + tile * 128);
  u32x4 pv[4][2];
#pragma unroll
  for (int s = 0; s < 4; ++s) { pv[s][0] = (u32x4){}; pv[s][1] = (u32x4){}; }
#pragma unroll 1
  for (int u = 17; u < 27; u += 2) {
    PERIODO(u, F1, F0);
    if (u == 19) { WAIT_V(4); ST(1, 3); }
    if (u == 23) {
      asm volatile("" ::: "memory");
#pragma unroll
      for (int s = 0; s < 4; ++s)
#pragma unroll
        for (int hf = 0; hf < 2; ++hf)
          pv[s][hf] = __builtin_amdgcn_raw_buffer_load_b128(rs, (c * 4 + s) * 8192 + lo + hf * 1024, 0, 16);
      SB();
    }
    PERIODO(u + 1, F0, F1);
    if (u == 19 && tid == 0) __hip_atomic_store(xfl + c, tag, __ATOMIC_RELAXED, __HIP_MEMORY_SCOPE_AGENT);
    if (u == 21) {
      if (wid == 0) {
        for (unsigned spins = 0;; ++spins) {
          const unsigned long long f = __hip_atomic_load(xfl + (lane & 3), __ATOMIC_RELAXED, __HIP_MEMORY_SCOPE_AGENT);
          if (__all(f == tag) || spins > (1u << 20)) break;
          __builtin_amdgcn_s_sleep(1);
        }
      }
      asm volatile("" ::: "memory");
      ST(1, 4);
    }
  }
  MO(F0, 0); MO(F0, 1); MO(F0, 2); MO(F0, 3);
  ST(1, 6);
#pragma unroll
  for (int half = 0; half < 2; ++half) {
    float o[8];
#pragma unroll
    for (int e = 0; e < 8; ++e) {
      const int rg = half * 8 + e;
      float p[4];
#pragma unroll
      for (int s = 0; s < 4; ++s) { const unsigned w = pv[s][half][e >> 1]; p[s] = c == s ? own[rg] : ((e & 1) ? bfhi(w) : bflo(w)); }
      const float s = (p[0] + p[1]) + (p[2] + p[3]);
      const unsigned xw = xv[half][e >> 1], bw = bv[half][e >> 1];
      const float xx = (e & 1) ? bfhi(xw) : bflo(xw), bb2 = (e & 1) ? bfhi(bw) : bflo(bw);
      const float v = acco[rg] + bb2;
      const float g = v * __builtin_amdgcn_rcpf(1.f + __builtin_amdgcn_exp2f(-1.442695041f * v));
      o[e] = xx + s * g;
    }
    u32x4 ov = {pk_bf16(o[0], o[1]), pk_bf16(o[2], o[3]), pk_bf16(o[4], o[5]), pk_bf16(o[6], o[7])};
    *(u32x4*)(out + (size_t)trow * D_ + ecol + 16 * half) = ov;
  }
  ST(1, 5); ST_FLUSH(1);
}

extern "C" __device__ unsigned long long amd_dispatch_id(void) __asm("llvm.amdgcn.dispatch.id");

__global__ __launch_bounds__(512, 2) void fused(const u16* __restrict__ x, const u16* __restrict__ cre, const u16* __restrict__ cim,
                                                const u16* __restrict__ wre, const u16* __restrict__ wim, const u16* __restrict__ w1,
                                                const u16* __restrict__ b1, const u16* __restrict__ w2, const u16* __restrict__ b2,
                                                char* __restrict__ ws, u16* __restrict__ out) {
  extern __shared__ __attribute__((aligned(16))) char lds[];
  const unsigned long long tag = ((amd_dispatch_id() + 1ull) * 0x9E3779B97F4A7C15ull) ^ ((unsigned long long)__builtin_amdgcn_dispatch_ptr() * 0xC2B2AE3D27D4EB4Full) ^ 0x5851F42D4C957F2Dull;
  phase1(x, wre, wim, w1, b1, w2, cre, cim, ws, lds, out);
  BAR();
  if (threadIdx.x == 0) {
    const int b = blockIdx.x, rt = ((b & 7) >> 1) * 4 + ((b >> 3) & 3), cg = (b & 1) * 8 + (b >> 5);
    __hip_atomic_store((gu64*)(ws + WS_CNT) + ((cg >> 1) * 2 + (rt >> 3)) * 16 + (rt & 7) * 2 + (cg & 1), tag, __ATOMIC_RELAXED, __HIP_MEMORY_SCOPE_AGENT);
  }
  phase2(x, cre, cim, b2, ws, out, lds, tag);
}

extern "C" void kernel_launch(void* const* d_in, const int* in_sizes, int n_in, void* d_out, int out_size, void* d_ws, size_t ws_size,
                              hipStream_t stream) {
  (void)hipFuncSetAttribute((const void*)fused, hipFuncAttributeMaxDynamicSharedMemorySize, 163840);
  fused<<<256, 512, 163840, stream>>>((const u16*)d_in[0], (const u16*)d_in[1], (const u16*)d_in[2], (const u16*)d_in[3], (const u16*)d_in[4],
                                      (const u16*)d_in[6], (const u16*)d_in[7], (const u16*)d_in[8], (const u16*)d_in[9], (char*)d_ws, (u16*)d_out);
}
```

```cpp
#include <hip/hip_runtime.h>
#include <hip/hip_bf16.h>
#include <stdint.h>
#include <type_traits>

#define ABL 0
typedef unsigned short u16;
typedef short v4s __attribute__((ext_vector_type(4)));
typedef short bf16x8 __attribute__((ext_vector_type(8)));
typedef __bf16 v2bf __attribute__((ext_vector_type(2)));
typedef float f32x4 __attribute__((ext_vector_type(4)));
typedef float f32x16 __attribute__((ext_vector_type(16)));
typedef unsigned u32x4 __attribute__((ext_vector_type(4)));
typedef unsigned u32x2 __attribute__((ext_vector_type(2)));
#define LAS __attribute__((address_space(3)))
#define GAS __attribute__((address_space(1)))

constexpr int S_ = 2048, D_ = 512, F_ = 1376;
constexpr int NKK_B = 64;
constexpr int NSUB = 88;
constexpr size_t WS_B    = 0;
constexpr size_t WS_W2   = 4u << 20;
constexpr size_t WS_H    = 8u << 20;
constexpr size_t WS_SLAB = 16u << 20;
constexpr size_t WS_CNT  = 40u << 20;

__device__ __forceinline__ float bf2f(u16 v) { return __uint_as_float(((unsigned)v) << 16); }
__device__ __forceinline__ float bflo(unsigned v) { return __uint_as_float(v << 16); }
__device__ __forceinline__ float bfhi(unsigned v) { return __uint_as_float(v & 0xffff0000u); }
__device__ __forceinline__ unsigned pk_bf16(float lo, float hi) {
  typedef float f32x2_ __attribute__((ext_vector_type(2)));
  v2bf r = __builtin_convertvector((f32x2_){lo, hi}, v2bf);
  return __builtin_bit_cast(unsigned, r);
}
__device__ __forceinline__ float rbf(float f) { return bflo(pk_bf16(f, 0.f)); }
__device__ __forceinline__ int swap23(int r) { return (r & ~12) | ((r & 4) << 1) | ((r & 8) >> 1); }
__device__ __forceinline__ float dot2(unsigned a, unsigned b, float c) {
  return __builtin_amdgcn_fdot2_f32_bf16(__builtin_bit_cast(v2bf, a), __builtin_bit_cast(v2bf, b), c, false);
}
#define GLDS16(g, l) __builtin_amdgcn_global_load_lds((const GAS void*)(g), (LAS void*)(l), 16, 0, 0)
typedef int i32x4 __attribute__((ext_vector_type(4)));
__device__ __forceinline__ i32x4 mk_rsrc(const void* p) {
  const unsigned long long a = (unsigned long long)p;
  return (i32x4){(int)(unsigned)a, (int)((unsigned)(a >> 32) & 0xffffu), 0x7fffffff, 0x00020000};
}
__device__ __forceinline__ void dma16(i32x4 rsrc, unsigned lds_dst, int voff, int soff) {
  unsigned keep;
  asm volatile("s_mov_b32 %0, m0\n\ts_mov_b32 m0, %1\n\ts_nop 0\n\tbuffer_load_dwordx4 %2, %3, %4 offen lds\n\ts_mov_b32 m0, %0"
               : "=&s"(keep) : "s"(lds_dst), "v"(voff), "s"(rsrc), "s"(soff) : "memory");
}
__device__ __forceinline__ void touch4(i32x4 rsrc, unsigned lds_dst, int voff, int soff) {
  unsigned keep;
  asm volatile("s_mov_b32 %0, m0\n\ts_mov_b32 m0, %1\n\ts_nop 0\n\tbuffer_load_dword %2, %3, %4 offen lds\n\ts_mov_b32 m0, %0"
               : "=&s"(keep) : "s"(lds_dst), "v"(voff), "s"(rsrc), "s"(soff) : "memory");
}
template <int OFF> __device__ __forceinline__ void dma16x(i32x4 rsrc, unsigned lds_dst, int voff, int soff) {
  unsigned keep;
  asm volatile("s_mov_b32 %0, m0\n\ts_mov_b32 m0, %1\n\ts_nop 3\n\tbuffer_load_dwordx4 %2, %3, %4 offen offset:%5 lds\n\ts_mov_b32 m0, %0"
               : "=&s"(keep) : "s"(lds_dst), "v"(voff), "s"(rsrc), "s"(soff), "n"(OFF) : "memory");
}
template <int OFF> __device__ __forceinline__ void dma16x_sc1(i32x4 rsrc, unsigned lds_dst, int voff, int soff) {
  unsigned keep;
  asm volatile("s_mov_b32 %0, m0\n\ts_mov_b32 m0, %1\n\ts_nop 3\n\tbuffer_load_dwordx4 %2, %3, %4 offen offset:%5 sc1 lds\n\ts_mov_b32 m0, %0"
               : "=&s"(keep) : "s"(lds_dst), "v"(voff), "s"(rsrc), "s"(soff), "n"(OFF) : "memory");
}
template <int OFF> __device__ __forceinline__ void dma16x_nt(i32x4 rsrc, unsigned lds_dst, int voff, int soff) {
  unsigned keep;
  asm volatile("s_mov_b32 %0, m0\n\ts_mov_b32 m0, %1\n\ts_nop 3\n\tbuffer_load_dwordx4 %2, %3, %4 offen offset:%5 nt lds\n\ts_mov_b32 m0, %0"
               : "=&s"(keep) : "s"(lds_dst), "v"(voff), "s"(rsrc), "s"(soff), "n"(OFF) : "memory");
}
__device__ __forceinline__ void touch4_nt(i32x4 rsrc, unsigned lds_dst, int voff, int soff) {
  unsigned keep;
  asm volatile("s_mov_b32 %0, m0\n\ts_mov_b32 m0, %1\n\ts_nop 0\n\tbuffer_load_dword %2, %3, %4 offen nt lds\n\ts_mov_b32 m0, %0"
               : "=&s"(keep) : "s"(lds_dst), "v"(voff), "s"(rsrc), "s"(soff) : "memory");
}
#define WAIT_V(n) asm volatile("s_waitcnt vmcnt(%0)" ::"n"(n) : "memory")
#define WAIT_L0() asm volatile("s_waitcnt lgkmcnt(0)" ::: "memory")
#define BAR() __builtin_amdgcn_s_barrier()
#define ST_DECL
#define ST(k, i)
#define ST_FLUSH(k)

#define AUX_PF 2
#define PF_ON 0
#define PF_AT 3
#define AUX_ST 16
#define TOUCH 2
typedef float f32x2 __attribute__((ext_vector_type(2)));
constexpr int SL1 = 0, SL2 = 33280, SL3 = 98304, SL4 = 0, SLP = 131072, AROW = 1040;
constexpr int SCR = 66560;
constexpr int SC_PART = SCR;
constexpr int SC_HS   = SCR + 2048;
constexpr int SC_RS   = SCR + 3072;
constexpr int SC_MRS  = SCR + 3584;
constexpr int SC_CS   = SCR + 4096;
constexpr int SC_B1   = SCR + 7168;
constexpr int SC_DUM  = SCR + 8192;
#define PIN(x) asm volatile("" : "+v"(x))
#define SBAR() __builtin_amdgcn_sched_barrier(0)

template <int K> __device__ __forceinline__ void eh_op(const f32x16& ap, float (&v)[16], float (&t)[16], unsigned (&ow)[8], float scale, float center) {
  constexpr int st = K / 16, e = K % 16;
  if constexpr (st == 0) { v[e] = fmaf(-center, t[e], v[e]); PIN(v[e]); }
  else if constexpr (st == 1) { v[e] = fmaf(ap[e], scale, v[e]); PIN(v[e]); }
  else if constexpr (st == 2) { t[e] = v[e] * v[e]; PIN(t[e]); }
  else if constexpr (st == 3) { t[e] = fmaf(t[e], -0.1029432397f, -2.302208198f); PIN(t[e]); }
  else if constexpr (st == 4) { t[e] = t[e] * v[e]; PIN(t[e]); }
  else if constexpr (st == 5) { t[e] = __builtin_amdgcn_exp2f(t[e]); PIN(t[e]); }
  else if constexpr (st == 6) { t[e] = 1.f + t[e]; PIN(t[e]); }
  else if constexpr (st == 7) { t[e] = __builtin_amdgcn_rcpf(t[e]); PIN(t[e]); }
  else if constexpr (st == 8) { v[e] = v[e] * t[e]; PIN(v[e]); }
  else if constexpr (K < 152) { ow[e] = pk_bf16(v[2 * e], v[2 * e + 1]); PIN(ow[e]); }
}
template <int K> __device__ __forceinline__ void ea_op(const f32x16& ap, float (&v)[16], float (&t)[16], unsigned (&ow)[8], float cs) {
  constexpr int st = K / 16, e = K % 16;
  if constexpr (st == 0) { t[e] = ap[e] * t[e]; PIN(t[e]); }
  else if constexpr (st == 1) { t[e] = fmaf(-v[e], cs, t[e]); PIN(t[e]); }
  else if constexpr (K < 40) { ow[e] = pk_bf16(t[2 * e], t[2 * e + 1]); PIN(ow[e]); }
}
template <int ET, int K0, int K1> __device__ __forceinline__ void e_ops(const f32x16& ap, float (&v)[16], float (&t)[16], unsigned (&ow)[8], float s0, float s1) {
  if constexpr (K0 < K1) {
    if constexpr (ET == 1) eh_op<K0>(ap, v, t, ow, s0, s1); else if constexpr (ET == 2) ea_op<K0>(ap, v, t, ow, s0);
    e_ops<ET, K0 + 1, K1>(ap, v, t, ow, s0, s1);
  }
}
template <int MT> __device__ __forceinline__ bf16x8 wread(const char* pan, int ks) {
  if constexpr (MT == 1) { return *(const bf16x8*)(pan + ks * 32); }
  else {
    v4s t0 = __builtin_amdgcn_ds_read_tr16_b64_v4i16((LAS v4s*)(pan + ks * 1024));
    v4s t1 = __builtin_amdgcn_ds_read_tr16_b64_v4i16((LAS v4s*)(pan + ks * 1024 + 256));
    return (bf16x8)__builtin_shufflevector(t0, t1, 0, 1, 2, 3, 4, 5, 6, 7);
  }
}
__device__ __forceinline__ void row_stats(float sum, float sq, float& mean, float& inv, float& m_bf, float& scale, float& center) {
  mean = sum * (1.f / 512.f);
  const float var = fmaxf(sq * (1.f / 512.f) - mean * mean, 0.f);
  m_bf = rbf(mean);
  const float sd = rbf(sqrtf(rbf(rbf(var) + 1e-5f)));
  inv = 1.f / sd;
  const float m2 = rbf((mean - m_bf) * inv);
  const float v2 = rbf(var * inv * inv);
  const float sd2 = rbf(sqrtf(rbf(v2 + 1e-5f)));
  scale = inv / sd2; center = (m_bf + m2 * sd) * scale;
}
constexpr int WR = 8, WD = 3;
struct RowStat { float mean, var, m_bf, sd, inv, m2, v2, sd2, scale, center; };
template <int S> __device__ __forceinline__ void rs_stage(RowStat& r) {
  if constexpr (S == 0) { r.mean *= (1.f / 512.f); r.var = fmaxf(r.var * (1.f / 512.f) - r.mean * r.mean, 0.f); r.m_bf = rbf(r.mean); PIN(r.var); PIN(r.m_bf); }
  else if constexpr (S == 1) { r.sd = rbf(rbf(r.var) + 1e-5f); PIN(r.sd); }
  else if constexpr (S == 2) { r.sd = rbf(sqrtf(r.sd)); PIN(r.sd); }
  else if constexpr (S == 3) { r.inv = 1.f / r.sd; PIN(r.inv); }
  else if constexpr (S == 4) { r.m2 = rbf((r.mean - r.m_bf) * r.inv); r.v2 = rbf(rbf(r.var * r.inv * r.inv) + 1e-5f); PIN(r.m2); PIN(r.v2); }
  else if constexpr (S == 5) { r.sd2 = rbf(sqrtf(r.v2)); PIN(r.sd2); }
  else if constexpr (S == 6) { r.scale = r.inv / r.sd2; PIN(r.scale); }
  else if constexpr (S == 7) { r.center = (r.m_bf + r.m2 * r.sd) * r.scale; PIN(r.center); }
}
template <int MT, int ET, int NT, int G0, int KS, class Hook>
__device__ __forceinline__ void unit_steps(const char* pan, const char* pnx, const bf16x8 (&xf)[32], bf16x8 (&wf)[WR], f32x16& aw, const f32x16& ar,
                                           float (&v)[16], float (&t)[16], unsigned (&ow)[8], const float& s0, const float& s1, Hook&& hook) {
  if constexpr (KS < 32) {
    constexpr int NOPS = ET == 1 ? 152 : 40;
    if constexpr (KS == 0) {
      if constexpr (MT == 1) aw = __builtin_amdgcn_mfma_f32_32x32x16_bf16(xf[KS], wf[KS % WR], (f32x16){}, 0, 0, 0);
      else aw = __builtin_amdgcn_mfma_f32_32x32x16_bf16(wf[KS % WR], xf[KS], (f32x16){}, 0, 0, 0);
    } else {
      if constexpr (MT == 1) aw = __builtin_amdgcn_mfma_f32_32x32x16_bf16(xf[KS], wf[KS % WR], aw, 0, 0, 0);
      else aw = __builtin_amdgcn_mfma_f32_32x32x16_bf16(wf[KS % WR], xf[KS], aw, 0, 0, 0);
    }
    PIN(aw);
    SBAR();
    if constexpr (KS + WD < 32) wf[(KS + WD) % WR] = wread<MT>(pan, KS + WD);
    else if constexpr (NT >= 0) wf[(KS + WD) % WR] = wread<NT>(pnx, KS + WD - 32);
    asm volatile("" :: "v"(wf[KS % WR]), "v"(wf[(KS + WR - 1) % WR]));
    SBAR();
    hook(std::integral_constant<int, KS>{});
    constexpr int k0 = KS < G0 ? 0 : (KS - G0) * NOPS / (32 - G0), k1 = KS < G0 ? 0 : (KS + 1 - G0) * NOPS / (32 - G0);
    e_ops<ET, k0, k1>(ar, v, t, ow, s0, s1);
    SBAR();
    unit_steps<MT, ET, NT, G0, KS + 1>(pan, pnx, xf, wf, aw, ar, v, t, ow, s0, s1, hook);
  }
}

__device__ __forceinline__ void phase1(const u16* __restrict__ x, const u16* __restrict__ wre, const u16* __restrict__ wim,
                                       const u16* __restrict__ w1, const u16* __restrict__ b1, const u16* __restrict__ w2,
                                       const u16* __restrict__ cre, const u16* __restrict__ cim, char* __restrict__ ws, char* lds) {
  const int tid = threadIdx.x, lane = tid & 63, wid = __builtin_amdgcn_readfirstlane(tid >> 6);
  const int b = blockIdx.x, xcd = b & 7, idx = b >> 3;
  const int rt = (xcd >> 1) * 4 + (idx & 3);
  const int cg = (xcd & 1) * 8 + (idx >> 2);
  const int h0 = cg < 11 ? cg * 3 : 33 + (cg - 11) * 2;
  const bool h3 = cg < 11;
  const int a0 = cg * 2;
  const int m = wid & 3;
  const bool helper = wid >= 4;
  const int r31 = lane & 31, hh = lane >> 5;
  const unsigned lds0 = (unsigned)(size_t)(LAS char*)lds;
  const int prot = idx & 3;
  ST_DECL;
  ST(0, 0);

  const int hoff = (lane >> 2) * (F_ * 2) + (lane & 3) * 16;
  const i32x4 rw1 = mk_rsrc(w1);
  auto piece_h = [&](int u, int q, int slot_off) { dma16(rw1, lds0 + slot_off + q * 1024, hoff, q * 16 * (F_ * 2) + u * 64); };
  const i32x4 rx = mk_rsrc(x + (size_t)(rt * 128 + m * 32) * D_);
  int xo[4];
#pragma unroll
  for (int i = 0; i < 4; ++i) { const int r = i * 4 + (lane >> 4); xo[i] = r * 1024 + (((lane & 15) ^ r) * 16); }
  auto quarter = [&](int q) {
#pragma unroll
    for (int i = 0; i < 8; ++i) dma16(rx, lds0 + q * 32768 + m * 8192 + i * 1024, xo[i & 3], q * 256 + (i >> 2) * 16384);
    piece_h(h0, q * 8 + m * 2, SLP); piece_h(h0, q * 8 + m * 2 + 1, SLP);
  };
  if (helper) { quarter(0); quarter(1); }

  const auto rws = __builtin_amdgcn_make_buffer_rsrc((void*)ws, 0, 0x7fffffff, 0x00020000);
  const int xrow = m * 32 + swap23(r31);
  const unsigned xbase = xrow * 256 + ((hh ^ (xrow & 15)) * 16);
#define QWAIT(q) WAIT_V(9 + 10 * (3 - (q)))

  if (helper) {
    const auto rw2t = __builtin_amdgcn_make_buffer_rsrc((void*)(ws + WS_W2), 0, 16 * NSUB * 1024, 0x00020000);
    const i32x4 rwa = mk_rsrc((a0 < 16 ? wre : wim) + (size_t)(a0 & 15) * 32 * D_);
    auto panel_h = [&](int u, int slot_off) {
#pragma unroll
      for (int k = 0; k < 8; ++k) piece_h(u, m * 8 + ((k + 2 * prot) & 7), slot_off);
    };
    auto panel_a = [&](int w, int slot_off) {
#pragma unroll
      for (int k = 0; k < 8; ++k) { const int q = m * 8 + ((k + 2 * prot) & 7); dma16(rwa, lds0 + slot_off + q * AROW, lane * 16, (w * 32 + q) * 1024); }
    };
    const v4s ones = {0x3f80, 0x3f80, 0x3f80, 0x3f80};
    const char* pzh = lds + ((lane >> 2) & 15) * 64 + (m & 1) * 32 + (lane & 3) * 8;
    auto cs_h_part = [&](f32x4 (&c)[4], int slot_off, int j0, int n) {
#pragma unroll
      for (int j = 0; j < n; ++j)
        c[j & 3] = __builtin_amdgcn_mfma_f32_4x4x4bf16_1k(ones, __builtin_amdgcn_ds_read_tr16_b64_v4i16((LAS v4s*)(pzh + slot_off + (j0 + j) * 1024)), c[j & 3], 0, 0, 0);
    };
    auto cs_h_fin = [&](f32x4 (&c)[4], int i) {
      float r = (c[0][0] + c[1][0]) + (c[2][0] + c[3][0]);
      r += __shfl_xor(r, 16); r += __shfl_xor(r, 32);
      if (lane < 16) *(float*)(lds + SC_CS + i * 512 + (m & 1) * 64 + lane * 4) = r;
    };
    auto cs_h = [&](int i, int slot_off) {
      if ((m >> 1) == (i & 1)) { f32x4 c[4] = {}; cs_h_part(c, slot_off, 0, 32); cs_h_fin(c, i); }
    };
    auto cs_a = [&](int i, int slot_off) {
      const char* pz = lds + slot_off + swap23(r31) * AROW + hh * 16 + m * 256;
      f32x4 c0 = {}, c1 = {};
#pragma unroll
      for (int j = 0; j < 8; ++j) {
        const bf16x8 w = *(const bf16x8*)(pz + j * 32);
        c0 = __builtin_amdgcn_mfma_f32_4x4x4bf16_1k(ones, __builtin_shufflevector(w, w, 0, 1, 2, 3), c0, 0, 0, 0);
        c1 = __builtin_amdgcn_mfma_f32_4x4x4bf16_1k(ones, __builtin_shufflevector(w, w, 4, 5, 6, 7), c1, 0, 0, 0);
      }
      float r = c0[0] + c1[0]; r += __shfl_xor(r, 32);
      if (lane < 32) *(float*)(lds + SC_CS + i * 512 + m * 128 + lane * 4) = r;
    };
    auto prefetch_c = [&]() {
      const int c2 = xcd >> 1;
      const int row = (xcd & 1) * 1024 + idx * 32 + m * 8 + (lane >> 4);
      const i32x4 rc = mk_rsrc(c2 < 2 ? cre : cim);
      const int vo = row * 4096 + (lane & 15) * 128;
      if (AUX_PF == 2) { touch4_nt(rc, lds0 + SC_DUM + m * 256, vo, (c2 & 1) * 2048); touch4_nt(rc, lds0 + SC_DUM + m * 256, vo, (c2 & 1) * 2048 + 4 * 4096); }
      else { touch4(rc, lds0 + SC_DUM + m * 256, vo, (c2 & 1) * 2048); touch4(rc, lds0 + SC_DUM + m * 256, vo, (c2 & 1) * 2048 + 4 * 4096); }
    };
    __builtin_amdgcn_s_setprio(2);
    {
      f32x4 c[4] = {};
      WAIT_V(10); ST(0, 1); BAR();
      quarter(2);
      if (m < 2) cs_h_part(c, SLP, 0, 8);
      WAIT_V(10); BAR();
      quarter(3);
      panel_h(h0 + 1, SL1);
      if (m < 2) cs_h_part(c, SLP, 8, 8);
      WAIT_V(18); BAR();
      unsigned w2v[2][8], b1v;
      int w2dst[2];
      {
        i32x4 rw2 = mk_rsrc(w2); rw2.z = F_ * D_ * 2;
#pragma unroll
        for (int r = 0; r < 2; ++r) {
          const int hl = r * 256 + (tid - 256), cc = b * 352 + hl;
          const int ln = cc & 63, rest = cc >> 6, ct = rest / NSUB, sub = rest - ct * NSUB;
          const int off = hl < 352 ? ((16 * sub + 4 * (ln >> 5)) * D_ + 32 * ct + swap23(ln & 31)) * 2 : 0x7ff00000;
          w2dst[r] = hl < 352 ? cc * 16 : 0x7ffffff0;
          asm volatile("buffer_load_ushort %0, %8, %10, 0 offen\n\tbuffer_load_ushort %1, %8, %10, 0 offen offset:1024\n\t"
                       "buffer_load_ushort %2, %8, %10, 0 offen offset:2048\n\tbuffer_load_ushort %3, %8, %10, 0 offen offset:3072\n\t"
                       "buffer_load_ushort %4, %9, %10, 0 offen\n\tbuffer_load_ushort %5, %9, %10, 0 offen offset:1024\n\t"
                       "buffer_load_ushort %6, %9, %10, 0 offen offset:2048\n\tbuffer_load_ushort %7, %9, %10, 0 offen offset:3072"
                       : "=&v"(w2v[r][0]), "=&v"(w2v[r][1]), "=&v"(w2v[r][2]), "=&v"(w2v[r][3]), "=&v"(w2v[r][4]), "=&v"(w2v[r][5]), "=&v"(w2v[r][6]), "=&v"(w2v[r][7])
                       : "v"(off), "v"(off + 8192), "s"(rw2) : "memory");
        }
        const int bo = h0 * 32 + lane * 2;
        asm volatile("buffer_load_dword %0, %1, %2, 0 offen" : "=&v"(b1v) : "v"((bo < F_ - 2 ? bo : F_ - 2) * 2), "s"(mk_rsrc(b1)) : "memory");
      }
      if (m < 2) cs_h_part(c, SLP, 16, 8);
      WAIT_V(25); BAR();
      ST(0, 2);
      panel_a(0, SL2);
      if (m < 2) { cs_h_part(c, SLP, 24, 8); cs_h_fin(c, 0); }
      WAIT_V(8);
      asm volatile("" : "+v"(w2v[0][0]), "+v"(w2v[0][1]), "+v"(w2v[0][2]), "+v"(w2v[0][3]), "+v"(w2v[0][4]), "+v"(w2v[0][5]), "+v"(w2v[0][6]), "+v"(w2v[0][7]), "+v"(b1v));
      asm volatile("" : "+v"(w2v[1][0]), "+v"(w2v[1][1]), "+v"(w2v[1][2]), "+v"(w2v[1][3]), "+v"(w2v[1][4]), "+v"(w2v[1][5]), "+v"(w2v[1][6]), "+v"(w2v[1][7]));
      if (m == 3 && lane < 48) *(f32x2*)(lds + SC_B1 + lane * 8) = (f32x2){bflo(b1v), bfhi(b1v)};
#pragma unroll
      for (int r = 0; r < 2; ++r) {
        const u32x4 w = {w2v[r][0] | (w2v[r][1] << 16), w2v[r][2] | (w2v[r][3] << 16), w2v[r][4] | (w2v[r][5] << 16), w2v[r][6] | (w2v[r][7] << 16)};
        __builtin_amdgcn_raw_buffer_store_b128(w, rw2t, w2dst[r], 0, AUX_ST);
      }
    }
    ST(0, 3);
    WAIT_V(2); WAIT_L0(); BAR();
    panel_h(h3 ? h0 + 2 : h0, SL3);
    cs_h(1, SL1);
    {
      float mean, inv, m_bf, sc_, ce_;
      const f32x2 pr = *(const f32x2*)(lds + SC_PART + (m * 32 + r31) * 8);
      row_stats(pr.x, pr.y, mean, inv, m_bf, sc_, ce_);
      const float sg = a0 < 16 ? inv : -inv;
      if (lane < 32) { *(float*)(lds + SC_RS + (m * 32 + lane) * 4) = sg; *(float*)(lds + SC_MRS + (m * 32 + lane) * 4) = m_bf * sg; }
    }
    WAIT_V(0); WAIT_L0(); BAR();
    panel_a(1, SL4);
    if (PF_ON && PF_AT == 3) prefetch_c();
    cs_a(2, SL2);
    WAIT_V(0);
    WAIT_L0(); BAR();
    if (PF_ON && PF_AT == 4) prefetch_c();
    cs_h(3, SL3);
    WAIT_L0(); BAR();
    cs_a(4, SL4);
    WAIT_L0(); BAR();
    __builtin_amdgcn_s_setprio(0);
    WAIT_V(0);
    ST(0, 5); ST_FLUSH(0);
    return;
  }

  bf16x8 xf[32];
  unsigned long long clk0 = 0; (void)clk0;
  const int wrow = swap23(r31);
  const unsigned wlA = (unsigned)(wrow * AROW + hh * 16);
  const unsigned wlH = (unsigned)((8 * hh + ((lane & 15) >> 2)) * 64 + ((lane >> 4) & 1) * 32 + (lane & 3) * 8);
  f32x16 acc, ac2;
  bf16x8 wf[WR];
  float v[16], t[16]; unsigned ow[8];
  auto nohook = [](auto) {};
  RowStat rs;
  const int hstore = (swap23(r31) + 32 * hh) * 16;
  const int s0r = rt * 128 + m * 32;

  auto tables_h = [&](int i, int hu) {
#pragma unroll
    for (int g4 = 0; g4 < 4; ++g4) {
      const f32x4 c = *(const f32x4*)(lds + SC_CS + i * 512 + (8 * g4 + 4 * hh) * 4), bq = *(const f32x4*)(lds + SC_B1 + hu * 128 + (8 * g4 + 4 * hh) * 4);
#pragma unroll
      for (int e = 0; e < 4; ++e) { t[g4 * 4 + e] = c[e]; v[g4 * 4 + e] = bq[e]; }
    }
  };
  auto tables_a = [&](int i) -> float {
#pragma unroll
    for (int g4 = 0; g4 < 4; ++g4) {
      const f32x4 c = *(const f32x4*)(lds + SC_RS + (m * 32 + 8 * g4 + 4 * hh) * 4), bq = *(const f32x4*)(lds + SC_MRS + (m * 32 + 8 * g4 + 4 * hh) * 4);
#pragma unroll
      for (int e = 0; e < 4; ++e) { t[g4 * 4 + e] = c[e]; v[g4 * 4 + e] = bq[e]; }
    }
    const char* cq = lds + SC_CS + i * 512 + r31 * 4;
    return (*(const float*)cq + *(const float*)(cq + 128)) + (*(const float*)(cq + 256) + *(const float*)(cq + 384));
  };
  auto tables_a4 = [&](int i, float (&cp)[4]) {
#pragma unroll
    for (int g4 = 0; g4 < 4; ++g4) {
      const f32x4 c = *(const f32x4*)(lds + SC_RS + (m * 32 + 8 * g4 + 4 * hh) * 4), bq = *(const f32x4*)(lds + SC_MRS + (m * 32 + 8 * g4 + 4 * hh) * 4);
#pragma unroll
      for (int e = 0; e < 4; ++e) { t[g4 * 4 + e] = c[e]; v[g4 * 4 + e] = bq[e]; }
      cp[g4] = *(const float*)(lds + SC_CS + i * 512 + g4 * 128 + r31 * 4);
    }
  };
  auto store_h = [&](int hu, const unsigned (&ow)[8]) {
    const int dst = (int)WS_H + (((rt * 4 + m) * NSUB + 2 * (h0 + hu)) << 10);
    if (hu < 2 || h3) {
      __builtin_amdgcn_raw_buffer_store_b128((u32x4){ow[0], ow[1], ow[2], ow[3]}, rws, hstore, dst, AUX_ST);
      __builtin_amdgcn_raw_buffer_store_b128((u32x4){ow[4], ow[5], ow[6], ow[7]}, rws, hstore + 1024, dst, AUX_ST);
    }
    if (h0 + hu == 42 && (hu < 2 || h3)) { u32x4 z = {0u, 0u, 0u, 0u}; __builtin_amdgcn_raw_buffer_store_b128(z, rws, lane * 16 + 2048, dst, AUX_ST); __builtin_amdgcn_raw_buffer_store_b128(z, rws, lane * 16 + 3072, dst, AUX_ST); }
  };
  auto store_a = [&](int w, const unsigned (&ow)[8]) {
    const int u = a0 + w, et = u & 15, j = et >> 2, nt = et & 3;
    const int kk = (u >> 4) * 32 + (s0r >> 6), sub = (s0r & 63) >> 4;
    const int dst = (int)WS_B + (((j * NKK_B + kk) * 16 + nt * 4 + sub) << 10);
    __builtin_amdgcn_raw_buffer_store_b128((u32x4){ow[0], ow[1], ow[2], ow[3]}, rws, lane * 16, dst, AUX_ST);
    __builtin_amdgcn_raw_buffer_store_b128((u32x4){ow[4], ow[5], ow[6], ow[7]}, rws, lane * 16 + 1024, dst, AUX_ST);
  };
  {
    f32x4 ss0 = {}, ss1 = {}, sq0 = {}, sq1 = {};
    const v4s ones4 = {0x3f80, 0x3f80, 0x3f80, 0x3f80};
    acc = (f32x16){};
    const char* pan = lds + SLP + wlH;
#pragma unroll
    for (int q = 0; q < 4; ++q) {
      if (q == 0) ST(0, 1);
      BAR();
      if (q == 3) ST(0, 2);
      bf16x8 w8[8];
#pragma unroll
      for (int k = 0; k < 8; ++k) xf[q * 8 + k] = *(const bf16x8*)(lds + q * 32768 + (xbase ^ (k * 32)));
#pragma unroll
      for (int k = 0; k < 8; ++k) w8[k] = wread<0>(pan, q * 8 + k);
      SBAR();
#pragma unroll
      for (int k = 0; k < 8; ++k) {
        const v4s lo4 = __builtin_shufflevector(xf[q * 8 + k], xf[q * 8 + k], 0, 1, 2, 3), hi4 = __builtin_shufflevector(xf[q * 8 + k], xf[q * 8 + k], 4, 5, 6, 7);
        ss0 = __builtin_amdgcn_mfma_f32_4x4x4bf16_1k(ones4, lo4, ss0, 0, 0, 0); sq0 = __builtin_amdgcn_mfma_f32_4x4x4bf16_1k(lo4, lo4, sq0, 0, 0, 0);
        ss1 = __builtin_amdgcn_mfma_f32_4x4x4bf16_1k(ones4, hi4, ss1, 0, 0, 0); sq1 = __builtin_amdgcn_mfma_f32_4x4x4bf16_1k(hi4, hi4, sq1, 0, 0, 0);
      }
      asm volatile("" : "+v"(ss0), "+v"(ss1), "+v"(sq0), "+v"(sq1));
      SBAR();
#pragma unroll
      for (int k = 0; k < 8; ++k) { acc = __builtin_amdgcn_mfma_f32_32x32x16_bf16(w8[k], xf[q * 8 + k], acc, 0, 0, 0); PIN(acc); }
      WAIT_L0(); SBAR();
    }
    const f32x4 sqv = sq0 + sq1;
    float sum = ss0[0] + ss1[0], sq = (lane & 2) ? ((lane & 1) ? sqv[3] : sqv[2]) : ((lane & 1) ? sqv[1] : sqv[0]);
    sum += __shfl_xor(sum, 32); sq += __shfl_xor(sq, 32);
    if (lane < 32) *(f32x2*)(lds + SC_PART + (m * 32 + lane) * 8) = (f32x2){sum, sq};
    rs.mean = sum; rs.var = sq;
  }
  ST(0, 8);
  WAIT_L0(); BAR();
  ST(0, 4);
  const char* pH1 = lds + SL1 + wlH; const char* pA0 = lds + SL2 + wlA;
  const char* p3H = lds + SL3 + wlH; const char* pA1 = lds + SL4 + wlA;
#pragma unroll
  for (int e = 0; e < WD; ++e) wf[e] = wread<0>(pH1, e);
  wf[WR - 1] = wf[0];
  tables_h(0, 0);
  float csa = 0.f;
  unit_steps<0, 1, 1, 8, 0>(pH1, pA0, xf, wf, ac2, acc, v, t, ow, rs.scale, rs.center, [&](auto g) {
    constexpr int G = decltype(g)::value;
    if constexpr (G < 8) rs_stage<G>(rs);
  });
  store_h(0, ow);
  ST(0, 9);
  BAR();
  ST(0, 6);
  {
    tables_h(1, 1);
    unit_steps<1, 1, 0, 3, 0>(pA0, p3H, xf, wf, acc, ac2, v, t, ow, rs.scale, rs.center, nohook); store_h(1, ow);
    ST(0, 10);
    BAR();
    ST(0, 7);
    float cp[4]; tables_a4(2, cp);
    unit_steps<0, 2, 1, 3, 0>(p3H, pA1, xf, wf, ac2, acc, v, t, ow, csa, csa, [&](auto g) { if constexpr (decltype(g)::value == 2) { csa = (cp[0] + cp[1]); PIN(csa); float c2 = cp[2] + cp[3]; PIN(c2); csa += c2; PIN(csa); } }); store_a(0, ow);
    ST(0, 11);
    BAR();
    ST(0, 13);
    tables_h(3, 2);
    unit_steps<1, 1, -1, 3, 0>(pA1, pA1, xf, wf, acc, ac2, v, t, ow, rs.scale, rs.center, nohook); store_h(2, ow);
    ST(0, 12);
    BAR();
    csa = tables_a(4);
    e_ops<2, 0, 40>(acc, v, t, ow, csa, csa);
    store_a(1, ow);
  }
  WAIT_V(0);
  ST(0, 5); ST_FLUSH(0);
}

#define AUX_C 2
typedef __attribute__((address_space(1))) unsigned long long gu64;
__device__ __forceinline__ void phase2(const u16* __restrict__ x, const u16* __restrict__ cre, const u16* __restrict__ cim,
                                       const u16* __restrict__ b2, char* __restrict__ ws, u16* __restrict__ out, char* lds, const unsigned long long tag) {
  const int tid = threadIdx.x, lane = tid & 63, wid = __builtin_amdgcn_readfirstlane(tid >> 6);
  const int b = blockIdx.x, xcd = b & 7, idx = b >> 3;
#define MAP2 0
  const int c = MAP2 ? (xcd >> 2) * 2 + (idx >> 4) : xcd >> 1, rt = MAP2 ? (xcd & 3) * 4 + ((idx >> 2) & 3) : (xcd & 1) * 8 + (idx >> 2), j = idx & 3;
  ST_DECL;
  ST(1, 0);

#define SB() __builtin_amdgcn_sched_barrier(0)

  if (wid >= 4) {
    const int lw = wid - 4;
    int aoff[4];
#pragma unroll
    for (int i = 0; i < 4; ++i) { int row = lw * 32 + i * 8 + (lane >> 3); aoff[i] = row * 4096 + (((lane & 7) ^ ((row >> 1) & 7)) * 16) - i * 1024 + 4096; }
    const unsigned ldsw = (unsigned)(size_t)(LAS char*)lds + lw * 4096;
    const i32x4 ra_s = mk_rsrc((const char*)(c < 2 ? cre : cim) + (size_t)rt * 128 * 4096 + (c & 1) * 2048 - 4096);
    const i32x4 rb_s = mk_rsrc(ws + WS_B + ((size_t)(j * NKK_B + c * 16) << 14) + lw * 4096);
    const i32x4 ra_o = mk_rsrc(ws + WS_H + ((size_t)((rt * 4 + 2 * (lw >> 1) + (c >> 1)) * NSUB) << 10) + (lw & 1) * 4096 - (16 << 13));
    const i32x4 rb_o = mk_rsrc(ws + WS_W2 + ((size_t)((j * 4 + 2 * (lw >> 1) + (c & 1)) * NSUB) << 10) + (lw & 1) * 4096 - (16 << 13));
    const int voff = lane * 16;
    const int r8 = idx >> 2;
#define LEAD 0
    const int ea = LEAD ? __builtin_amdgcn_readfirstlane(lw == j ? 1 : 0) : 1, eb0 = LEAD ? __builtin_amdgcn_readfirstlane(r8 == 2 * lw ? 1 : 0) : 1,
              eb1 = LEAD ? __builtin_amdgcn_readfirstlane(r8 == 2 * lw + 1 ? 1 : 0) : 1;
    auto issue_a = [&](int T) {
      const unsigned dst = __builtin_amdgcn_readfirstlane(ldsw + (T & 3) * 32768);
      if (T < 16) { const int so = __builtin_amdgcn_readfirstlane(T * 128);
                    dma16x_nt<0>(ra_s, dst, aoff[0], so); dma16x_nt<1024>(ra_s, dst, aoff[1], so); dma16x_nt<2048>(ra_s, dst, aoff[2], so); dma16x_nt<3072>(ra_s, dst, aoff[3], so); }
      else { i32x4 r = ra_o; r.z = __builtin_amdgcn_readfirstlane(T < 27 ? 0x7fffffff : 0); const int so = __builtin_amdgcn_readfirstlane(T << 13);
             dma16x_sc1<0>(r, dst, voff, so); dma16x_sc1<1024>(r, dst, voff, so); dma16x_sc1<2048>(r, dst, voff, so); dma16x_sc1<3072>(r, dst, voff, so); }
    };
    auto issue_b = [&](int T, int half) {
      const unsigned dst = __builtin_amdgcn_readfirstlane(ldsw + (T & 3) * 32768 + 16384 + half * 2048);
      i32x4 r = T < 16 ? rb_s : rb_o; r.z = T < 27 ? 0x7fffffff : 0;
      const int so = __builtin_amdgcn_readfirstlane((T < 16 ? T << 14 : T << 13) + half * 2048);
      dma16x_sc1<0>(r, dst, voff, so); dma16x_sc1<1024>(r, dst, voff, so);
    };
    issue_a(0); issue_a(1); issue_a(2);
    BAR();
    issue_b(0, 0); issue_b(0, 1); issue_b(1, 0); issue_b(1, 1); issue_b(2, 0); issue_b(2, 1);
    for (int u = 0; u < 27; ++u) {
      if (u == 0) WAIT_V(8); else if (u == 1) WAIT_V(12); else WAIT_V(16);
      BAR();
      issue_a(u + 3); issue_b(u + 3, 0); issue_b(u + 3, 1);
    }
    WAIT_V(0);
    return;
  }

  const int r31 = lane & 31, hh = lane >> 5;
  const int wm = wid >> 1, wn = wid & 1;
  unsigned ab[4];
#pragma unroll
  for (int s = 0; s < 4; ++s) ab[s] = wm * 8192 + (unsigned)(r31 * 128 + (((2 * s + hh) ^ ((r31 >> 1) & 7)) * 16));
  const unsigned bb = 16384 + wn * 8192 + lane * 16;
  const unsigned ao = wm * 8192 + lane * 16;

  f32x16 acc[2][2] = {};
  f32x16 acco = {};
  bf16x8 F0[16], F1[16];
#define RD(F, s) do { F[4 * (s) + 0] = *(const bf16x8*)(sl + ab[s]); F[4 * (s) + 1] = *(const bf16x8*)(sl + ab[s] + 4096); \
                      F[4 * (s) + 2] = *(const bf16x8*)(sl + bb + (s) * 1024); F[4 * (s) + 3] = *(const bf16x8*)(sl + bb + (s) * 1024 + 4096); } while (0)
#define RDO(F, s) do { F[4 * (s) + 0] = *(const bf16x8*)(sl + ao + (s) * 2048); F[4 * (s) + 1] = *(const bf16x8*)(sl + bb + (s) * 2048); \
                       F[4 * (s) + 2] = *(const bf16x8*)(sl + ao + (s) * 2048 + 1024); F[4 * (s) + 3] = *(const bf16x8*)(sl + bb + (s) * 2048 + 1024); } while (0)
#define MM0(F, s) acc[0][0] = __builtin_amdgcn_mfma_f32_32x32x16_bf16(F[4 * (s) + 2], F[4 * (s) + 0], acc[0][0], 0, 0, 0)
#define MM1(F, s) acc[0][1] = __builtin_amdgcn_mfma_f32_32x32x16_bf16(F[4 * (s) + 3], F[4 * (s) + 0], acc[0][1], 0, 0, 0)
#define MM2(F, s) acc[1][0] = __builtin_amdgcn_mfma_f32_32x32x16_bf16(F[4 * (s) + 2], F[4 * (s) + 1], acc[1][0], 0, 0, 0)
#define MM3(F, s) acc[1][1] = __builtin_amdgcn_mfma_f32_32x32x16_bf16(F[4 * (s) + 3], F[4 * (s) + 1], acc[1][1], 0, 0, 0)
#define MO(F, s) do { acco = __builtin_amdgcn_mfma_f32_32x32x16_bf16(F[4 * (s) + 1], F[4 * (s) + 0], acco, 0, 0, 0); \
                      acco = __builtin_amdgcn_mfma_f32_32x32x16_bf16(F[4 * (s) + 3], F[4 * (s) + 2], acco, 0, 0, 0); } while (0)
#define PERIOD(u, RDX, FR, FM) do { \
    const char* sl = lds + ((u) & 3) * 32768; \
    BAR(); SB(); \
    MM0(FM, 0); SB(); RDX(FR, 0); SB(); MM1(FM, 0); SB(); MM2(FM, 0); SB(); RDX(FR, 1); SB(); MM3(FM, 0); SB(); \
    MM0(FM, 1); SB(); RDX(FR, 2); SB(); MM1(FM, 1); SB(); MM2(FM, 1); SB(); RDX(FR, 3); SB(); MM3(FM, 1); SB(); \
    MM0(FM, 2); MM1(FM, 2); MM2(FM, 2); MM3(FM, 2); \
    MM0(FM, 3); MM1(FM, 3); MM2(FM, 3); SB(); WAIT_L0(); SB(); MM3(FM, 3); SB(); \
  } while (0)
#define PERIODO(u, FR, FM) do { \
    const char* sl = lds + ((u) & 3) * 32768; \
    BAR(); SB(); \
    RDO(FR, 0); SB(); MO(FM, 0); SB(); RDO(FR, 1); SB(); MO(FM, 1); SB(); RDO(FR, 2); SB(); MO(FM, 2); SB(); RDO(FR, 3); SB(); MO(FM, 3); SB(); \
    WAIT_L0(); SB(); \
  } while (0)
#define K2PF 0
#define K2PF_POS 0
  const unsigned dum = (unsigned)(size_t)(LAS char*)lds + 131072 + wid * 256;
  auto touches = [&]() {
    if (K2PF & 1) {
      const i32x4 rb = mk_rsrc(ws + WS_B + ((size_t)(j * NKK_B + c * 16) << 14));
      touch4(rb, dum, ((lane >> 4) + 4 * wid) * 16384 + ((idx >> 2) * 16 + (lane & 15)) * 128, 0);
    }
    if (K2PF & 2) {
      const i32x4 rc = mk_rsrc((const char*)(c < 2 ? cre : cim) + (size_t)(rt * 128 + j * 32) * 4096 + (c & 1) * 2048);
      const int vo = r31 * 4096 + (2 * wid + hh) * 128;
      touch4_nt(rc, dum, vo, 0); touch4_nt(rc, dum, vo, 1024);
    }
  };
  gu64* fl = (gu64*)(ws + WS_CNT);
  if (wid == 0) {
    gu64* f16 = fl + ((4 * (c >> 1) + j) * 2 + (c & 1)) * 16 + (lane & 15);
    for (unsigned spins = 0;; ++spins) {
      const unsigned long long f = __hip_atomic_load(f16, __ATOMIC_RELAXED, __HIP_MEMORY_SCOPE_AGENT);
      if (__all(f == tag) || spins > (1u << 20)) break;
      __builtin_amdgcn_s_sleep(1);
    }
    asm volatile("" ::: "memory");
    ST(1, 7);
  }
  BAR();
  {
    const char* sl = lds;
    BAR(); SB(); ST(1, 1);
    if (K2PF_POS == 1) touches();
    RD(F0, 0); RD(F0, 1); RD(F0, 2); RD(F0, 3); SB();
    WAIT_L0(); SB();
  }
  for (int u = 1; u < 15; u += 2) {
    if ((K2PF & 4) && u == 7) {
      const i32x4 rh = mk_rsrc(ws + WS_H + ((size_t)((rt * 4 + 2 * (wid & 1) + (c >> 1)) * NSUB) << 10) + j * 22528);
      const int l2 = (wid >> 1) * 88 + lane;
      touch4(rh, dum, l2 * 128, 0);
      touch4(rh, dum, (lane < 24 ? l2 + 64 : l2) * 128, 0);
      const i32x4 rw = mk_rsrc(ws + WS_W2 + ((size_t)((j * 4 + 2 * (wid & 1) + (c & 1)) * NSUB) << 10) + (idx >> 2) * 11264);
      touch4(rw, dum, ((wid >> 1) * 44 + (lane < 44 ? lane : 0)) * 128, 0);
    }
    PERIOD(u, RD, F1, F0);
    PERIOD(u + 1, RD, F0, F1);
    if (u == 11 && wid == 0) {
      for (unsigned spins = 0;; ++spins) {
        const unsigned long long f0 = __hip_atomic_load(fl + lane * 4, __ATOMIC_RELAXED, __HIP_MEMORY_SCOPE_AGENT), f1 = __hip_atomic_load(fl + lane * 4 + 1, __ATOMIC_RELAXED, __HIP_MEMORY_SCOPE_AGENT),
                                 f2 = __hip_atomic_load(fl + lane * 4 + 2, __ATOMIC_RELAXED, __HIP_MEMORY_SCOPE_AGENT), f3 = __hip_atomic_load(fl + lane * 4 + 3, __ATOMIC_RELAXED, __HIP_MEMORY_SCOPE_AGENT);
        if (__all(f0 == tag && f1 == tag && f2 == tag && f3 == tag) || spins > (1u << 20)) break;
        __builtin_amdgcn_s_sleep(1);
      }
      asm volatile("" ::: "memory");
    }
  }
  PERIOD(15, RD, F1, F0);
  PERIOD(16, RDO, F0, F1);
  ST(1, 2);
  const int tile = rt * 4 + j;
  const auto rs = __builtin_amdgcn_make_buffer_rsrc(ws + WS_SLAB + ((size_t)tile << 17), 0, (ABL & 16) ? 0 : (1 << 17), 0x00020000);
  const unsigned lo = wid * 2048 + lane * 16;
#pragma unroll
  for (int q = 0; q < 4; ++q) {
    if (q != c) {
      const f32x16 v = acc[q >> 1][q & 1];
#pragma unroll
      for (int hf = 0; hf < 2; ++hf) {
        const u32x4 w = {pk_bf16(v[hf * 8], v[hf * 8 + 1]), pk_bf16(v[hf * 8 + 2], v[hf * 8 + 3]), pk_bf16(v[hf * 8 + 4], v[hf * 8 + 5]), pk_bf16(v[hf * 8 + 6], v[hf * 8 + 7])};
        __builtin_amdgcn_raw_buffer_store_b128(w, rs, (q * 4 + c) * 8192 + lo + hf * 1024, 0, 16);
      }
    }
  }
  const f32x16 own = c == 0 ? acc[0][0] : (c == 1 ? acc[0][1] : (c == 2 ? acc[1][0] : acc[1][1]));
  const int trow = rt * 128 + wm * 64 + (c >> 1) * 32 + r31;
  const int ecol = j * 128 + wn * 64 + (c & 1) * 32 + 8 * hh;
  u32x4 xv[2], bv[2];
#pragma unroll
  for (int half = 0; half < 2; ++half) { xv[half] = *(const u32x4*)(x + (size_t)trow * D_ + ecol + 16 * half); bv[half] = *(const u32x4*)(b2 + ecol + 16 * half); }
  SB();
  gu64* xfl = (gu64*)(ws + WS_CNT + 4096 + tile * 128);
  u32x4 pv[4][2];
#pragma unroll
  for (int s = 0; s < 4; ++s) { pv[s][0] = (u32x4){}; pv[s][1] = (u32x4){}; }
#pragma unroll 1
  for (int u = 17; u < 27; u += 2) {
    PERIODO(u, F1, F0);
    if (u == 19) { WAIT_V(4); ST(1, 3); }
    if (u == 23) {
      asm volatile("" ::: "memory");
#pragma unroll
      for (int s = 0; s < 4; ++s)
#pragma unroll
        for (int hf = 0; hf < 2; ++hf)
          pv[s][hf] = __builtin_amdgcn_raw_buffer_load_b128(rs, (c * 4 + s) * 8192 + lo + hf * 1024, 0, 16);
      SB();
    }
    PERIODO(u + 1, F0, F1);
    if (u == 19 && tid == 0) __hip_atomic_store(xfl + c, tag, __ATOMIC_RELAXED, __HIP_MEMORY_SCOPE_AGENT);
    if (u == 21) {
      if (wid == 0) {
        for (unsigned spins = 0;; ++spins) {
          const unsigned long long f = __hip_atomic_load(xfl + (lane & 3), __ATOMIC_RELAXED, __HIP_MEMORY_SCOPE_AGENT);
          if (__all(f == tag) || spins > (1u << 20)) break;
          __builtin_amdgcn_s_sleep(1);
        }
      }
      asm volatile("" ::: "memory");
      ST(1, 4);
    }
  }
  MO(F0, 0); MO(F0, 1); MO(F0, 2); MO(F0, 3);
  ST(1, 6);
#pragma unroll
  for (int half = 0; half < 2; ++half) {
    float o[8];
#pragma unroll
    for (int e = 0; e < 8; ++e) {
      const int rg = half * 8 + e;
      float p[4];
#pragma unroll
      for (int s = 0; s < 4; ++s) { const unsigned w = pv[s][half][e >> 1]; p[s] = c == s ? own[rg] : ((e & 1) ? bfhi(w) : bflo(w)); }
      const float s = (p[0] + p[1]) + (p[2] + p[3]);
      const unsigned xw = xv[half][e >> 1], bw = bv[half][e >> 1];
      const float xx = (e & 1) ? bfhi(xw) : bflo(xw), bb2 = (e & 1) ? bfhi(bw) : bflo(bw);
      const float v = acco[rg] + bb2;
      const float g = v * __builtin_amdgcn_rcpf(1.f + __builtin_amdgcn_exp2f(-1.442695041f * v));
      o[e] = xx + s * g;
    }
    u32x4 ov = {pk_bf16(o[0], o[1]), pk_bf16(o[2], o[3]), pk_bf16(o[4], o[5]), pk_bf16(o[6], o[7])};
    *(u32x4*)(out + (size_t)trow * D_ + ecol + 16 * half) = ov;
  }
  ST(1, 5); ST_FLUSH(1);
}

extern "C" __device__ unsigned long long amd_dispatch_id(void) __asm("llvm.amdgcn.dispatch.id");

__global__ __launch_bounds__(512, 2) void fused(const u16* __restrict__ x, const u16* __restrict__ cre, const u16* __restrict__ cim,
                                                const u16* __restrict__ wre, const u16* __restrict__ wim, const u16* __restrict__ w1,
                                                const u16* __restrict__ b1, const u16* __restrict__ w2, const u16* __restrict__ b2,
                                                char* __restrict__ ws, u16* __restrict__ out) {
  extern __shared__ __attribute__((aligned(16))) char lds[];
  const unsigned long long tag = ((amd_dispatch_id() + 1ull) * 0x9E3779B97F4A7C15ull) ^ ((unsigned long long)__builtin_amdgcn_dispatch_ptr() * 0xC2B2AE3D27D4EB4Full) ^ 0x5851F42D4C957F2Dull;
  const unsigned long long t0_ = __builtin_amdgcn_s_memrealtime();
  phase1(x, wre, wim, w1, b1, w2, cre, cim, ws, lds);
  BAR();
  if (threadIdx.x == 0) {
    const int b = blockIdx.x, rt = ((b & 7) >> 1) * 4 + ((b >> 3) & 3), cg = (b & 1) * 8 + (b >> 5);
    __hip_atomic_store((gu64*)(ws + WS_CNT) + ((cg >> 1) * 2 + (rt >> 3)) * 16 + (rt & 7) * 2 + (cg & 1), tag, __ATOMIC_RELAXED, __HIP_MEMORY_SCOPE_AGENT);
  }
  phase2(x, cre, cim, b2, ws, out, lds, tag);
  while (__builtin_amdgcn_s_memrealtime() - t0_ < 2800ull) __builtin_amdgcn_s_sleep(1);
}

extern "C" void kernel_launch(void* const* d_in, const int* in_sizes, int n_in, void* d_out, int out_size, void* d_ws, size_t ws_size,
                              hipStream_t stream) {
  (void)hipFuncSetAttribute((const void*)fused, hipFuncAttributeMaxDynamicSharedMemorySize, 163840);
  fused<<<256, 512, 163840, stream>>>((const u16*)d_in[0], (const u16*)d_in[1], (const u16*)d_in[2], (const u16*)d_in[3], (const u16*)d_in[4],
                                      (const u16*)d_in[6], (const u16*)d_in[7], (const u16*)d_in[8], (const u16*)d_in[9], (char*)d_ws, (u16*)d_out);
}
```

```cpp
#include <hip/hip_runtime.h>
#include <hip/hip_bf16.h>
#include <stdint.h>
#include <type_traits>

#define ABL 0
typedef unsigned short u16;
typedef short v4s __attribute__((ext_vector_type(4)));
typedef short bf16x8 __attribute__((ext_vector_type(8)));
typedef __bf16 v2bf __attribute__((ext_vector_type(2)));
typedef float f32x4 __attribute__((ext_vector_type(4)));
typedef float f32x16 __attribute__((ext_vector_type(16)));
typedef unsigned u32x4 __attribute__((ext_vector_type(4)));
typedef unsigned u32x2 __attribute__((ext_vector_type(2)));
#define LAS __attribute__((address_space(3)))
#define GAS __attribute__((address_space(1)))

constexpr int S_ = 2048, D_ = 512, F_ = 1376;
constexpr int NKK_B = 64;
constexpr int NSUB = 88;
constexpr size_t WS_B    = 0;
constexpr size_t WS_W2   = 4u << 20;
constexpr size_t WS_H    = 8u << 20;
constexpr size_t WS_SLAB = 16u << 20;
constexpr size_t WS_CNT  = 40u << 20;

__device__ __forceinline__ float bf2f(u16 v) { return __uint_as_float(((unsigned)v) << 16); }
__device__ __forceinline__ float bflo(unsigned v) { return __uint_as_float(v << 16); }
__device__ __forceinline__ float bfhi(unsigned v) { return __uint_as_float(v & 0xffff0000u); }
__device__ __forceinline__ unsigned pk_bf16(float lo, float hi) {
  typedef float f32x2_ __attribute__((ext_vector_type(2)));
  v2bf r = __builtin_convertvector((f32x2_){lo, hi}, v2bf);
  return __builtin_bit_cast(unsigned, r);
}
__device__ __forceinline__ float rbf(float f) { return bflo(pk_bf16(f, 0.f)); }
__device__ __forceinline__ int swap23(int r) { return (r & ~12) | ((r & 4) << 1) | ((r & 8) >> 1); }
__device__ __forceinline__ float dot2(unsigned a, unsigned b, float c) {
  return __builtin_amdgcn_fdot2_f32_bf16(__builtin_bit_cast(v2bf, a), __builtin_bit_cast(v2bf, b), c, false);
}
#define GLDS16(g, l) __builtin_amdgcn_global_load_lds((const GAS void*)(g), (LAS void*)(l), 16, 0, 0)
typedef int i32x4 __attribute__((ext_vector_type(4)));
__device__ __forceinline__ i32x4 mk_rsrc(const void* p) {
  const unsigned long long a = (unsigned long long)p;
  return (i32x4){(int)(unsigned)a, (int)((unsigned)(a >> 32) & 0xffffu), 0x7fffffff, 0x00020000};
}
__device__ __forceinline__ void dma16(i32x4 rsrc, unsigned lds_dst, int voff, int soff) {
  unsigned keep;
  asm volatile("s_mov_b32 %0, m0\n\ts_mov_b32 m0, %1\n\ts_nop 0\n\tbuffer_load_dwordx4 %2, %3, %4 offen lds\n\ts_mov_b32 m0, %0"
               : "=&s"(keep) : "s"(lds_dst), "v"(voff), "s"(rsrc), "s"(soff) : "memory");
}
__device__ __forceinline__ void touch4(i32x4 rsrc, unsigned lds_dst, int voff, int soff) {
  unsigned keep;
  asm volatile("s_mov_b32 %0, m0\n\ts_mov_b32 m0, %1\n\ts_nop 0\n\tbuffer_load_dword %2, %3, %4 offen lds\n\ts_mov_b32 m0, %0"
               : "=&s"(keep) : "s"(lds_dst), "v"(voff), "s"(rsrc), "s"(soff) : "memory");
}
template <int OFF> __device__ __forceinline__ void dma16x(i32x4 rsrc, unsigned lds_dst, int voff, int soff) {
  unsigned keep;
  asm volatile("s_mov_b32 %0, m0\n\ts_mov_b32 m0, %1\n\ts_nop 3\n\tbuffer_load_dwordx4 %2, %3, %4 offen offset:%5 lds\n\ts_mov_b32 m0, %0"
               : "=&s"(keep) : "s"(lds_dst), "v"(voff), "s"(rsrc), "s"(soff), "n"(OFF) : "memory");
}
template <int OFF> __device__ __forceinline__ void dma16x_sc1(i32x4 rsrc, unsigned lds_dst, int voff, int soff) {
  unsigned keep;
  asm volatile("s_mov_b32 %0, m0\n\ts_mov_b32 m0, %1\n\ts_nop 3\n\tbuffer_load_dwordx4 %2, %3, %4 offen offset:%5 sc1 lds\n\ts_mov_b32 m0, %0"
               : "=&s"(keep) : "s"(lds_dst), "v"(voff), "s"(rsrc), "s"(soff), "n"(OFF) : "memory");
}
template <int OFF> __device__ __forceinline__ void dma16x_nt(i32x4 rsrc, unsigned lds_dst, int voff, int soff) {
  unsigned keep;
  asm volatile("s_mov_b32 %0, m0\n\ts_mov_b32 m0, %1\n\ts_nop 3\n\tbuffer_load_dwordx4 %2, %3, %4 offen offset:%5 nt lds\n\ts_mov_b32 m0, %0"
               : "=&s"(keep) : "s"(lds_dst), "v"(voff), "s"(rsrc), "s"(soff), "n"(OFF) : "memory");
}
__device__ __forceinline__ void touch4_nt(i32x4 rsrc, unsigned lds_dst, int voff, int soff) {
  unsigned keep;
  asm volatile("s_mov_b32 %0, m0\n\ts_mov_b32 m0, %1\n\ts_nop 0\n\tbuffer_load_dword %2, %3, %4 offen nt lds\n\ts_mov_b32 m0, %0"
               : "=&s"(keep) : "s"(lds_dst), "v"(voff), "s"(rsrc), "s"(soff) : "memory");
}
#define WAIT_V(n) asm volatile("s_waitcnt vmcnt(%0)" ::"n"(n) : "memory")
#define WAIT_L0() asm volatile("s_waitcnt lgkmcnt(0)" ::: "memory")
#define BAR() __builtin_amdgcn_s_barrier()
#define ST_DECL
#define ST(k, i)
#define ST_FLUSH(k)

#define AUX_PF 2
#define PF_ON 0
#define PF_AT 3
#define AUX_ST 16
#define TOUCH 2
typedef float f32x2 __attribute__((ext_vector_type(2)));
constexpr int SL1 = 0, SL2 = 33280, SL3 = 98304, SL4 = 0, SLP = 131072, AROW = 1040;
constexpr int SCR = 66560;
constexpr int SC_PART = SCR;
constexpr int SC_HS   = SCR + 2048;
constexpr int SC_RS   = SCR + 3072;
constexpr int SC_MRS  = SCR + 3584;
constexpr int SC_CS   = SCR + 4096;
constexpr int SC_B1   = SCR + 7168;
constexpr int SC_DUM  = SCR + 8192;
#define PIN(x) asm volatile("" : "+v"(x))
#define SBAR() __builtin_amdgcn_sched_barrier(0)

template <int K> __device__ __forceinline__ void eh_op(const f32x16& ap, float (&v)[16], float (&t)[16], unsigned (&ow)[8], float scale, float center) {
  constexpr int st = K / 16, e = K % 16;
  if constexpr (st == 0) { v[e] = fmaf(-center, t[e], v[e]); PIN(v[e]); }
  else if constexpr (st == 1) { v[e] = fmaf(ap[e], scale, v[e]); PIN(v[e]); }
  else if constexpr (st == 2) { t[e] = v[e] * v[e]; PIN(t[e]); }
  else if constexpr (st == 3) { t[e] = fmaf(t[e], -0.1029432397f, -2.302208198f); PIN(t[e]); }
  else if constexpr (st == 4) { t[e] = t[e] * v[e]; PIN(t[e]); }
  else if constexpr (st == 5) { t[e] = __builtin_amdgcn_exp2f(t[e]); PIN(t[e]); }
  else if constexpr (st == 6) { t[e] = 1.f + t[e]; PIN(t[e]); }
  else if constexpr (st == 7) { t[e] = __builtin_amdgcn_rcpf(t[e]); PIN(t[e]); }
  else if constexpr (st == 8) { v[e] = v[e] * t[e]; PIN(v[e]); }
  else if constexpr (K < 152) { ow[e] = pk_bf16(v[2 * e], v[2 * e + 1]); PIN(ow[e]); }
}
template <int K> __device__ __forceinline__ void ea_op(const f32x16& ap, float (&v)[16], float (&t)[16], unsigned (&ow)[8], float cs) {
  constexpr int st = K / 16, e = K % 16;
  if constexpr (st == 0) { t[e] = ap[e] * t[e]; PIN(t[e]); }
  else if constexpr (st == 1) { t[e] = fmaf(-v[e], cs, t[e]); PIN(t[e]); }
  else if constexpr (K < 40) { ow[e] = pk_bf16(t[2 * e], t[2 * e + 1]); PIN(ow[e]); }
}
template <int ET, int K0, int K1> __device__ __forceinline__ void e_ops(const f32x16& ap, float (&v)[16], float (&t)[16], unsigned (&ow)[8], float s0, float s1) {
  if constexpr (K0 < K1) {
    if constexpr (ET == 1) eh_op<K0>(ap, v, t, ow, s0, s1); else if constexpr (ET == 2) ea_op<K0>(ap, v, t, ow, s0);
    e_ops<ET, K0 + 1, K1>(ap, v, t, ow, s0, s1);
  }
}
template <int MT> __device__ __forceinline__ bf16x8 wread(const char* pan, int ks) {
  if constexpr (MT == 1) { return *(const bf16x8*)(pan + ks * 32); }
  else {
    v4s t0 = __builtin_amdgcn_ds_read_tr16_b64_v4i16((LAS v4s*)(pan + ks * 1024));
    v4s t1 = __builtin_amdgcn_ds_read_tr16_b64_v4i16((LAS v4s*)(pan + ks * 1024 + 256));
    return (bf16x8)__builtin_shufflevector(t0, t1, 0, 1, 2, 3, 4, 5, 6, 7);
  }
}
__device__ __forceinline__ void row_stats(float sum, float sq, float& mean, float& inv, float& m_bf, float& scale, float& center) {
  mean = sum * (1.f / 512.f);
  const float var = fmaxf(sq * (1.f / 512.f) - mean * mean, 0.f);
  m_bf = rbf(mean);
  const float sd = rbf(sqrtf(rbf(rbf(var) + 1e-5f)));
  inv = 1.f / sd;
  const float m2 = rbf((mean - m_bf) * inv);
  const float v2 = rbf(var * inv * inv);
  const float sd2 = rbf(sqrtf(rbf(v2 + 1e-5f)));
  scale = inv / sd2; center = (m_bf + m2 * sd) * scale;
}
constexpr int WR = 8, WD = 3;
struct RowStat { float mean, var, m_bf, sd, inv, m2, v2, sd2, scale, center; };
template <int S> __device__ __forceinline__ void rs_stage(RowStat& r) {
  if constexpr (S == 0) { r.mean *= (1.f / 512.f); r.var = fmaxf(r.var * (1.f / 512.f) - r.mean * r.mean, 0.f); r.m_bf = rbf(r.mean); PIN(r.var); PIN(r.m_bf); }
  else if constexpr (S == 1) { r.sd = rbf(rbf(r.var) + 1e-5f); PIN(r.sd); }
  else if constexpr (S == 2) { r.sd = rbf(sqrtf(r.sd)); PIN(r.sd); }
  else if constexpr (S == 3) { r.inv = 1.f / r.sd; PIN(r.inv); }
  else if constexpr (S == 4) { r.m2 = rbf((r.mean - r.m_bf) * r.inv); r.v2 = rbf(rbf(r.var * r.inv * r.inv) + 1e-5f); PIN(r.m2); PIN(r.v2); }
  else if constexpr (S == 5) { r.sd2 = rbf(sqrtf(r.v2)); PIN(r.sd2); }
  else if constexpr (S == 6) { r.scale = r.inv / r.sd2; PIN(r.scale); }
  else if constexpr (S == 7) { r.center = (r.m_bf + r.m2 * r.sd) * r.scale; PIN(r.center); }
}
template <int MT, int ET, int NT, int G0, int KS, class Hook>
__device__ __forceinline__ void unit_steps(const char* pan, const char* pnx, const bf16x8 (&xf)[32], bf16x8 (&wf)[WR], f32x16& aw, const f32x16& ar,
                                           float (&v)[16], float (&t)[16], unsigned (&ow)[8], const float& s0, const float& s1, Hook&& hook) {
  if constexpr (KS < 32) {
    constexpr int NOPS = ET == 1 ? 152 : 40;
    if constexpr (KS == 0) {
      if constexpr (MT == 1) aw = __builtin_amdgcn_mfma_f32_32x32x16_bf16(xf[KS], wf[KS % WR], (f32x16){}, 0, 0, 0);
      else aw = __builtin_amdgcn_mfma_f32_32x32x16_bf16(wf[KS % WR], xf[KS], (f32x16){}, 0, 0, 0);
    } else {
      if constexpr (MT == 1) aw = __builtin_amdgcn_mfma_f32_32x32x16_bf16(xf[KS], wf[KS % WR], aw, 0, 0, 0);
      else aw = __builtin_amdgcn_mfma_f32_32x32x16_bf16(wf[KS % WR], xf[KS], aw, 0, 0, 0);
    }
    PIN(aw);
    SBAR();
    if constexpr (KS + WD < 32) wf[(KS + WD) % WR] = wread<MT>(pan, KS + WD);
    else if constexpr (NT >= 0) wf[(KS + WD) % WR] = wread<NT>(pnx, KS + WD - 32);
    asm volatile("" :: "v"(wf[KS % WR]), "v"(wf[(KS + WR - 1) % WR]));
    SBAR();
    hook(std::integral_constant<int, KS>{});
    constexpr int k0 = KS < G0 ? 0 : (KS - G0) * NOPS / (32 - G0), k1 = KS < G0 ? 0 : (KS + 1 - G0) * NOPS / (32 - G0);
    e_ops<ET, k0, k1>(ar, v, t, ow, s0, s1);
    SBAR();
    unit_steps<MT, ET, NT, G0, KS + 1>(pan, pnx, xf, wf, aw, ar, v, t, ow, s0, s1, hook);
  }
}

__device__ __forceinline__ void phase1(const u16* __restrict__ x, const u16* __restrict__ wre, const u16* __restrict__ wim,
                                       const u16* __restrict__ w1, const u16* __restrict__ b1, const u16* __restrict__ w2,
                                       const u16* __restrict__ cre, const u16* __restrict__ cim, char* __restrict__ ws, char* lds) {
  const int tid = threadIdx.x, lane = tid & 63, wid = __builtin_amdgcn_readfirstlane(tid >> 6);
  const int b = blockIdx.x, xcd = b & 7, idx = b >> 3;
  const int rt = (xcd >> 1) * 4 + (idx & 3);
  const int cg = (xcd & 1) * 8 + (idx >> 2);
  const int h0 = cg < 11 ? cg * 3 : 33 + (cg - 11) * 2;
  const bool h3 = cg < 11;
  const int a0 = cg * 2;
  const int m = wid & 3;
  const bool helper = wid >= 4;
  const int r31 = lane & 31, hh = lane >> 5;
  const unsigned lds0 = (unsigned)(size_t)(LAS char*)lds;
  const int prot = idx & 3;
  ST_DECL;
  ST(0, 0);

  const int hoff = (lane >> 2) * (F_ * 2) + (lane & 3) * 16;
  const i32x4 rw1 = mk_rsrc(w1);
  auto piece_h = [&](int u, int q, int slot_off) { dma16(rw1, lds0 + slot_off + q * 1024, hoff, q * 16 * (F_ * 2) + u * 64); };
  const i32x4 rx = mk_rsrc(x + (size_t)(rt * 128 + m * 32) * D_);
  int xo[4];
#pragma unroll
  for (int i = 0; i < 4; ++i) { const int r = i * 4 + (lane >> 4); xo[i] = r * 1024 + (((lane & 15) ^ r) * 16); }
  auto quarter = [&](int q) {
#pragma unroll
    for (int i = 0; i < 8; ++i) dma16(rx, lds0 + q * 32768 + m * 8192 + i * 1024, xo[i & 3], q * 256 + (i >> 2) * 16384);
    piece_h(h0, q * 8 + m * 2, SLP); piece_h(h0, q * 8 + m * 2 + 1, SLP);
  };
  if (helper) { quarter(0); quarter(1); }

  const auto rws = __builtin_amdgcn_make_buffer_rsrc((void*)ws, 0, 0x7fffffff, 0x00020000);
  const int xrow = m * 32 + swap23(r31);
  const unsigned xbase = xrow * 256 + ((hh ^ (xrow & 15)) * 16);
#define QWAIT(q) WAIT_V(9 + 10 * (3 - (q)))

  if (helper) {
    const auto rw2t = __builtin_amdgcn_make_buffer_rsrc((void*)(ws + WS_W2), 0, 16 * NSUB * 1024, 0x00020000);
    const i32x4 rwa = mk_rsrc((a0 < 16 ? wre : wim) + (size_t)(a0 & 15) * 32 * D_);
    auto panel_h = [&](int u, int slot_off) {
#pragma unroll
      for (int k = 0; k < 8; ++k) piece_h(u, m * 8 + ((k + 2 * prot) & 7), slot_off);
    };
    auto panel_a = [&](int w, int slot_off) {
#pragma unroll
      for (int k = 0; k < 8; ++k) { const int q = m * 8 + ((k + 2 * prot) & 7); dma16(rwa, lds0 + slot_off + q * AROW, lane * 16, (w * 32 + q) * 1024); }
    };
    const v4s ones = {0x3f80, 0x3f80, 0x3f80, 0x3f80};
    const char* pzh = lds + ((lane >> 2) & 15) * 64 + (m & 1) * 32 + (lane & 3) * 8;
    auto cs_h_part = [&](f32x4 (&c)[4], int slot_off, int j0, int n) {
#pragma unroll
      for (int j = 0; j < n; ++j)
        c[j & 3] = __builtin_amdgcn_mfma_f32_4x4x4bf16_1k(ones, __builtin_amdgcn_ds_read_tr16_b64_v4i16((LAS v4s*)(pzh + slot_off + (j0 + j) * 1024)), c[j & 3], 0, 0, 0);
    };
    auto cs_h_fin = [&](f32x4 (&c)[4], int i) {
      float r = (c[0][0] + c[1][0]) + (c[2][0] + c[3][0]);
      r += __shfl_xor(r, 16); r += __shfl_xor(r, 32);
      if (lane < 16) *(float*)(lds + SC_CS + i * 512 + (m & 1) * 64 + lane * 4) = r;
    };
    auto cs_h = [&](int i, int slot_off) {
      if ((m >> 1) == (i & 1)) { f32x4 c[4] = {}; cs_h_part(c, slot_off, 0, 32); cs_h_fin(c, i); }
    };
    auto cs_a = [&](int i, int slot_off) {
      const char* pz = lds + slot_off + swap23(r31) * AROW + hh * 16 + m * 256;
      f32x4 c0 = {}, c1 = {};
#pragma unroll
      for (int j = 0; j < 8; ++j) {
        const bf16x8 w = *(const bf16x8*)(pz + j * 32);
        c0 = __builtin_amdgcn_mfma_f32_4x4x4bf16_1k(ones, __builtin_shufflevector(w, w, 0, 1, 2, 3), c0, 0, 0, 0);
        c1 = __builtin_amdgcn_mfma_f32_4x4x4bf16_1k(ones, __builtin_shufflevector(w, w, 4, 5, 6, 7), c1, 0, 0, 0);
      }
      float r = c0[0] + c1[0]; r += __shfl_xor(r, 32);
      if (lane < 32) *(float*)(lds + SC_CS + i * 512 + m * 128 + lane * 4) = r;
    };
    auto prefetch_c = [&]() {
      const int c2 = xcd >> 1;
      const int row = (xcd & 1) * 1024 + idx * 32 + m * 8 + (lane >> 4);
      const i32x4 rc = mk_rsrc(c2 < 2 ? cre : cim);
      const int vo = row * 4096 + (lane & 15) * 128;
      if (AUX_PF == 2) { touch4_nt(rc, lds0 + SC_DUM + m * 256, vo, (c2 & 1) * 2048); touch4_nt(rc, lds0 + SC_DUM + m * 256, vo, (c2 & 1) * 2048 + 4 * 4096); }
      else { touch4(rc, lds0 + SC_DUM + m * 256, vo, (c2 & 1) * 2048); touch4(rc, lds0 + SC_DUM + m * 256, vo, (c2 & 1) * 2048 + 4 * 4096); }
    };
    __builtin_amdgcn_s_setprio(2);
    {
      f32x4 c[4] = {};
      WAIT_V(10); ST(0, 1); BAR();
      quarter(2);
      if (m < 2) cs_h_part(c, SLP, 0, 8);
      WAIT_V(10); BAR();
      quarter(3);
      panel_h(h0 + 1, SL1);
      if (m < 2) cs_h_part(c, SLP, 8, 8);
      WAIT_V(18); BAR();
      unsigned w2v[2][8], b1v;
      int w2dst[2];
      {
        i32x4 rw2 = mk_rsrc(w2); rw2.z = F_ * D_ * 2;
#pragma unroll
        for (int r = 0; r < 2; ++r) {
          const int hl = r * 256 + (tid - 256), cc = b * 352 + hl;
          const int ln = cc & 63, rest = cc >> 6, ct = rest / NSUB, sub = rest - ct * NSUB;
          const int off = hl < 352 ? ((16 * sub + 4 * (ln >> 5)) * D_ + 32 * ct + swap23(ln & 31)) * 2 : 0x7ff00000;
          w2dst[r] = hl < 352 ? cc * 16 : 0x7ffffff0;
          asm volatile("buffer_load_ushort %0, %8, %10, 0 offen\n\tbuffer_load_ushort %1, %8, %10, 0 offen offset:1024\n\t"
                       "buffer_load_ushort %2, %8, %10, 0 offen offset:2048\n\tbuffer_load_ushort %3, %8, %10, 0 offen offset:3072\n\t"
                       "buffer_load_ushort %4, %9, %10, 0 offen\n\tbuffer_load_ushort %5, %9, %10, 0 offen offset:1024\n\t"
                       "buffer_load_ushort %6, %9, %10, 0 offen offset:2048\n\tbuffer_load_ushort %7, %9, %10, 0 offen offset:3072"
                       : "=&v"(w2v[r][0]), "=&v"(w2v[r][1]), "=&v"(w2v[r][2]), "=&v"(w2v[r][3]), "=&v"(w2v[r][4]), "=&v"(w2v[r][5]), "=&v"(w2v[r][6]), "=&v"(w2v[r][7])
                       : "v"(off), "v"(off + 8192), "s"(rw2) : "memory");
        }
        const int bo = h0 * 32 + lane * 2;
        asm volatile("buffer_load_dword %0, %1, %2, 0 offen" : "=&v"(b1v) : "v"((bo < F_ - 2 ? bo : F_ - 2) * 2), "s"(mk_rsrc(b1)) : "memory");
      }
      if (m < 2) cs_h_part(c, SLP, 16, 8);
      WAIT_V(25); BAR();
      ST(0, 2);
      panel_a(0, SL2);
      if (m < 2) { cs_h_part(c, SLP, 24, 8); cs_h_fin(c, 0); }
      WAIT_V(8);
      asm volatile("" : "+v"(w2v[0][0]), "+v"(w2v[0][1]), "+v"(w2v[0][2]), "+v"(w2v[0][3]), "+v"(w2v[0][4]), "+v"(w2v[0][5]), "+v"(w2v[0][6]), "+v"(w2v[0][7]), "+v"(b1v));
      asm volatile("" : "+v"(w2v[1][0]), "+v"(w2v[1][1]), "+v"(w2v[1][2]), "+v"(w2v[1][3]), "+v"(w2v[1][4]), "+v"(w2v[1][5]), "+v"(w2v[1][6]), "+v"(w2v[1][7]));
      if (m == 3 && lane < 48) *(f32x2*)(lds + SC_B1 + lane * 8) = (f32x2){bflo(b1v), bfhi(b1v)};
#pragma unroll
      for (int r = 0; r < 2; ++r) {
        const u32x4 w = {w2v[r][0] | (w2v[r][1] << 16), w2v[r][2] | (w2v[r][3] << 16), w2v[r][4] | (w2v[r][5] << 16), w2v[r][6] | (w2v[r][7] << 16)};
        __builtin_amdgcn_raw_buffer_store_b128(w, rw2t, w2dst[r], 0, AUX_ST);
      }
    }
    ST(0, 3);
    WAIT_V(2); WAIT_L0(); BAR();
    panel_h(h3 ? h0 + 2 : h0, SL3);
    cs_h(1, SL1);
    {
      float mean, inv, m_bf, sc_, ce_;
      const f32x2 pr = *(const f32x2*)(lds + SC_PART + (m * 32 + r31) * 8);
      row_stats(pr.x, pr.y, mean, inv, m_bf, sc_, ce_);
      const float sg = a0 < 16 ? inv : -inv;
      if (lane < 32) { *(float*)(lds + SC_RS + (m * 32 + lane) * 4) = sg; *(float*)(lds + SC_MRS + (m * 32 + lane) * 4) = m_bf * sg; }
    }
    WAIT_V(0); WAIT_L0(); BAR();
    panel_a(1, SL4);
    if (PF_ON && PF_AT == 3) prefetch_c();
    cs_a(2, SL2);
    WAIT_V(0);
    WAIT_L0(); BAR();
    if (PF_ON && PF_AT == 4) prefetch_c();
    cs_h(3, SL3);
    WAIT_L0(); BAR();
    cs_a(4, SL4);
    WAIT_L0(); BAR();
    __builtin_amdgcn_s_setprio(0);
    WAIT_V(0);
    ST(0, 5); ST_FLUSH(0);
    return;
  }

  bf16x8 xf[32];
  unsigned long long clk0 = 0; (void)clk0;
  const int wrow = swap23(r31);
  const unsigned wlA = (unsigned)(wrow * AROW + hh * 16);
  const unsigned wlH = (unsigned)((8 * hh + ((lane & 15) >> 2)) * 64 + ((lane >> 4) & 1) * 32 + (lane & 3) * 8);
  f32x16 acc, ac2;
  bf16x8 wf[WR];
  float v[16], t[16]; unsigned ow[8];
  auto nohook = [](auto) {};
  RowStat rs;
  const int hstore = (swap23(r31) + 32 * hh) * 16;
  const int s0r = rt * 128 + m * 32;

  auto tables_h = [&](int i, int hu) {
#pragma unroll
    for (int g4 = 0; g4 < 4; ++g4) {
      const f32x4 c = *(const f32x4*)(lds + SC_CS + i * 512 + (8 * g4 + 4 * hh) * 4), bq = *(const f32x4*)(lds + SC_B1 + hu * 128 + (8 * g4 + 4 * hh) * 4);
#pragma unroll
      for (int e = 0; e < 4; ++e) { t[g4 * 4 + e] = c[e]; v[g4 * 4 + e] = bq[e]; }
    }
  };
  auto tables_a = [&](int i) -> float {
#pragma unroll
    for (int g4 = 0; g4 < 4; ++g4) {
      const f32x4 c = *(const f32x4*)(lds + SC_RS + (m * 32 + 8 * g4 + 4 * hh) * 4), bq = *(const f32x4*)(lds + SC_MRS + (m * 32 + 8 * g4 + 4 * hh) * 4);
#pragma unroll
      for (int e = 0; e < 4; ++e) { t[g4 * 4 + e] = c[e]; v[g4 * 4 + e] = bq[e]; }
    }
    const char* cq = lds + SC_CS + i * 512 + r31 * 4;
    return (*(const float*)cq + *(const float*)(cq + 128)) + (*(const float*)(cq + 256) + *(const float*)(cq + 384));
  };
  auto tables_a4 = [&](int i, float (&cp)[4]) {
#pragma unroll
    for (int g4 = 0; g4 < 4; ++g4) {
      const f32x4 c = *(const f32x4*)(lds + SC_RS + (m * 32 + 8 * g4 + 4 * hh) * 4), bq = *(const f32x4*)(lds + SC_MRS + (m * 32 + 8 * g4 + 4 * hh) * 4);
#pragma unroll
      for (int e = 0; e < 4; ++e) { t[g4 * 4 + e] = c[e]; v[g4 * 4 + e] = bq[e]; }
      cp[g4] = *(const float*)(lds + SC_CS + i * 512 + g4 * 128 + r31 * 4);
    }
  };
  auto store_h = [&](int hu, const unsigned (&ow)[8]) {
    const int dst = (int)WS_H + (((rt * 4 + m) * NSUB + 2 * (h0 + hu)) << 10);
    if (hu < 2 || h3) {
      __builtin_amdgcn_raw_buffer_store_b128((u32x4){ow[0], ow[1], ow[2], ow[3]}, rws, hstore, dst, AUX_ST);
      __builtin_amdgcn_raw_buffer_store_b128((u32x4){ow[4], ow[5], ow[6], ow[7]}, rws, hstore + 1024, dst, AUX_ST);
    }
    if (h0 + hu == 42 && (hu < 2 || h3)) { u32x4 z = {0u, 0u, 0u, 0u}; __builtin_amdgcn_raw_buffer_store_b128(z, rws, lane * 16 + 2048, dst, AUX_ST); __builtin_amdgcn_raw_buffer_store_b128(z, rws, lane * 16 + 3072, dst, AUX_ST); }
  };
  auto store_a = [&](int w, const unsigned (&ow)[8]) {
    const int u = a0 + w, et = u & 15, j = et >> 2, nt = et & 3;
    const int kk = (u >> 4) * 32 + (s0r >> 6), sub = (s0r & 63) >> 4;
    const int dst = (int)WS_B + (((j * NKK_B + kk) * 16 + nt * 4 + sub) << 10);
    __builtin_amdgcn_raw_buffer_store_b128((u32x4){ow[0], ow[1], ow[2], ow[3]}, rws, lane * 16, dst, AUX_ST);
    __builtin_amdgcn_raw_buffer_store_b128((u32x4){ow[4], ow[5], ow[6], ow[7]}, rws, lane * 16 + 1024, dst, AUX_ST);
  };
  {
    f32x4 ss0 = {}, ss1 = {}, sq0 = {}, sq1 = {};
    const v4s ones4 = {0x3f80, 0x3f80, 0x3f80, 0x3f80};
    acc = (f32x16){};
    const char* pan = lds + SLP + wlH;
#pragma unroll
    for (int q = 0; q < 4; ++q) {
      if (q == 0) ST(0, 1);
      BAR();
      if (q == 3) ST(0, 2);
      bf16x8 w8[8];
#pragma unroll
      for (int k = 0; k < 8; ++k) xf[q * 8 + k] = *(const bf16x8*)(lds + q * 32768 + (xbase ^ (k * 32)));
#pragma unroll
      for (int k = 0; k < 8; ++k) w8[k] = wread<0>(pan, q * 8 + k);
      SBAR();
#pragma unroll
      for (int k = 0; k < 8; ++k) {
        const v4s lo4 = __builtin_shufflevector(xf[q * 8 + k], xf[q * 8 + k], 0, 1, 2, 3), hi4 = __builtin_shufflevector(xf[q * 8 + k], xf[q * 8 + k], 4, 5, 6, 7);
        ss0 = __builtin_amdgcn_mfma_f32_4x4x4bf16_1k(ones4, lo4, ss0, 0, 0, 0); sq0 = __builtin_amdgcn_mfma_f32_4x4x4bf16_1k(lo4, lo4, sq0, 0, 0, 0);
        ss1 = __builtin_amdgcn_mfma_f32_4x4x4bf16_1k(ones4, hi4, ss1, 0, 0, 0); sq1 = __builtin_amdgcn_mfma_f32_4x4x4bf16_1k(hi4, hi4, sq1, 0, 0, 0);
      }
      asm volatile("" : "+v"(ss0), "+v"(ss1), "+v"(sq0), "+v"(sq1));
      SBAR();
#pragma unroll
      for (int k = 0; k < 8; ++k) { acc = __builtin_amdgcn_mfma_f32_32x32x16_bf16(w8[k], xf[q * 8 + k], acc, 0, 0, 0); PIN(acc); }
      WAIT_L0(); SBAR();
    }
    const f32x4 sqv = sq0 + sq1;
    float sum = ss0[0] + ss1[0], sq = (lane & 2) ? ((lane & 1) ? sqv[3] : sqv[2]) : ((lane & 1) ? sqv[1] : sqv[0]);
    sum += __shfl_xor(sum, 32); sq += __shfl_xor(sq, 32);
    if (lane < 32) *(f32x2*)(lds + SC_PART + (m * 32 + lane) * 8) = (f32x2){sum, sq};
    rs.mean = sum; rs.var = sq;
  }
  ST(0, 8);
  WAIT_L0(); BAR();
  ST(0, 4);
  const char* pH1 = lds + SL1 + wlH; const char* pA0 = lds + SL2 + wlA;
  const char* p3H = lds + SL3 + wlH; const char* pA1 = lds + SL4 + wlA;
#pragma unroll
  for (int e = 0; e < WD; ++e) wf[e] = wread<0>(pH1, e);
  wf[WR - 1] = wf[0];
  tables_h(0, 0);
  float csa = 0.f;
  unit_steps<0, 1, 1, 8, 0>(pH1, pA0, xf, wf, ac2, acc, v, t, ow, rs.scale, rs.center, [&](auto g) {
    constexpr int G = decltype(g)::value;
    if constexpr (G < 8) rs_stage<G>(rs);
  });
  store_h(0, ow);
  ST(0, 9);
  BAR();
  ST(0, 6);
  {
    tables_h(1, 1);
    unit_steps<1, 1, 0, 3, 0>(pA0, p3H, xf, wf, acc, ac2, v, t, ow, rs.scale, rs.center, nohook); store_h(1, ow);
    ST(0, 10);
    BAR();
    ST(0, 7);
    float cp[4]; tables_a4(2, cp);
    unit_steps<0, 2, 1, 3, 0>(p3H, pA1, xf, wf, ac2, acc, v, t, ow, csa, csa, [&](auto g) { if constexpr (decltype(g)::value == 2) { csa = (cp[0] + cp[1]); PIN(csa); float c2 = cp[2] + cp[3]; PIN(c2); csa += c2; PIN(csa); } }); store_a(0, ow);
    ST(0, 11);
    BAR();
    ST(0, 13);
    tables_h(3, 2);
    unit_steps<1, 1, -1, 3, 0>(pA1, pA1, xf, wf, acc, ac2, v, t, ow, rs.scale, rs.center, nohook); store_h(2, ow);
    ST(0, 12);
    BAR();
    csa = tables_a(4);
    e_ops<2, 0, 40>(acc, v, t, ow, csa, csa);
    store_a(1, ow);
  }
  WAIT_V(0);
  ST(0, 5); ST_FLUSH(0);
}

#define AUX_C 2
typedef __attribute__((address_space(1))) unsigned long long gu64;
__device__ __forceinline__ void phase2(const u16* __restrict__ x, const u16* __restrict__ cre, const u16* __restrict__ cim,
                                       const u16* __restrict__ b2, char* __restrict__ ws, u16* __restrict__ out, char* lds, const unsigned long long tag) {
  const int tid = threadIdx.x, lane = tid & 63, wid = __builtin_amdgcn_readfirstlane(tid >> 6);
  const int b = blockIdx.x, xcd = b & 7, idx = b >> 3;
#define MAP2 0
  const int c = MAP2 ? (xcd >> 2) * 2 + (idx >> 4) : xcd >> 1, rt = MAP2 ? (xcd & 3) * 4 + ((idx >> 2) & 3) : (xcd & 1) * 8 + (idx >> 2), j = idx & 3;
  ST_DECL;
  ST(1, 0);

#define SB() __builtin_amdgcn_sched_barrier(0)

  if (wid >= 4) {
    const int lw = wid - 4;
    int aoff[4];
#pragma unroll
    for (int i = 0; i < 4; ++i) { int row = lw * 32 + i * 8 + (lane >> 3); aoff[i] = row * 4096 + (((lane & 7) ^ ((row >> 1) & 7)) * 16) - i * 1024 + 4096; }
    const unsigned ldsw = (unsigned)(size_t)(LAS char*)lds + lw * 4096;
    const i32x4 ra_s = mk_rsrc((const char*)(c < 2 ? cre : cim) + (size_t)rt * 128 * 4096 + (c & 1) * 2048 - 4096);
    const i32x4 rb_s = mk_rsrc(ws + WS_B + ((size_t)(j * NKK_B + c * 16) << 14) + lw * 4096);
    const i32x4 ra_o = mk_rsrc(ws + WS_H + ((size_t)((rt * 4 + 2 * (lw >> 1) + (c >> 1)) * NSUB) << 10) + (lw & 1) * 4096 - (16 << 13));
    const i32x4 rb_o = mk_rsrc(ws + WS_W2 + ((size_t)((j * 4 + 2 * (lw >> 1) + (c & 1)) * NSUB) << 10) + (lw & 1) * 4096 - (16 << 13));
    const int voff = lane * 16;
    const int r8 = idx >> 2;
#define LEAD 0
    const int ea = LEAD ? __builtin_amdgcn_readfirstlane(lw == j ? 1 : 0) : 1, eb0 = LEAD ? __builtin_amdgcn_readfirstlane(r8 == 2 * lw ? 1 : 0) : 1,
              eb1 = LEAD ? __builtin_amdgcn_readfirstlane(r8 == 2 * lw + 1 ? 1 : 0) : 1;
    auto issue_a = [&](int T) {
      const unsigned dst = __builtin_amdgcn_readfirstlane(ldsw + (T & 3) * 32768);
      if (T < 16) { const int so = __builtin_amdgcn_readfirstlane(T * 128);
                    dma16x_nt<0>(ra_s, dst, aoff[0], so); dma16x_nt<1024>(ra_s, dst, aoff[1], so); dma16x_nt<2048>(ra_s, dst, aoff[2], so); dma16x_nt<3072>(ra_s, dst, aoff[3], so); }
      else { i32x4 r = ra_o; r.z = __builtin_amdgcn_readfirstlane(T < 27 ? 0x7fffffff : 0); const int so = __builtin_amdgcn_readfirstlane(T << 13);
             dma16x_sc1<0>(r, dst, voff, so); dma16x_sc1<1024>(r, dst, voff, so); dma16x_sc1<2048>(r, dst, voff, so); dma16x_sc1<3072>(r, dst, voff, so); }
    };
    auto issue_b = [&](int T, int half) {
      const unsigned dst = __builtin_amdgcn_readfirstlane(ldsw + (T & 3) * 32768 + 16384 + half * 2048);
      i32x4 r = T < 16 ? rb_s : rb_o; r.z = T < 27 ? 0x7fffffff : 0;
      const int so = __builtin_amdgcn_readfirstlane((T < 16 ? T << 14 : T << 13) + half * 2048);
      dma16x_sc1<0>(r, dst, voff, so); dma16x_sc1<1024>(r, dst, voff, so);
    };
    issue_a(0); issue_a(1); issue_a(2);
    BAR();
    issue_b(0, 0); issue_b(0, 1); issue_b(1, 0); issue_b(1, 1); issue_b(2, 0); issue_b(2, 1);
    for (int u = 0; u < 27; ++u) {
      if (u == 0) WAIT_V(8); else if (u == 1) WAIT_V(12); else WAIT_V(16);
      BAR();
      issue_a(u + 3); issue_b(u + 3, 0); issue_b(u + 3, 1);
    }
    WAIT_V(0);
    return;
  }

  const int r31 = lane & 31, hh = lane >> 5;
  const int wm = wid >> 1, wn = wid & 1;
  unsigned ab[4];
#pragma unroll
  for (int s = 0; s < 4; ++s) ab[s] = wm * 8192 + (unsigned)(r31 * 128 + (((2 * s + hh) ^ ((r31 >> 1) & 7)) * 16));
  const unsigned bb = 16384 + wn * 8192 + lane * 16;
  const unsigned ao = wm * 8192 + lane * 16;

  f32x16 acc[2][2] = {};
  f32x16 acco = {};
  bf16x8 F0[16], F1[16];
#define RD(F, s) do { F[4 * (s) + 0] = *(const bf16x8*)(sl + ab[s]); F[4 * (s) + 1] = *(const bf16x8*)(sl + ab[s] + 4096); \
                      F[4 * (s) + 2] = *(const bf16x8*)(sl + bb + (s) * 1024); F[4 * (s) + 3] = *(const bf16x8*)(sl + bb + (s) * 1024 + 4096); } while (0)
#define RDO(F, s) do { F[4 * (s) + 0] = *(const bf16x8*)(sl + ao + (s) * 2048); F[4 * (s) + 1] = *(const bf16x8*)(sl + bb + (s) * 2048); \
                       F[4 * (s) + 2] = *(const bf16x8*)(sl + ao + (s) * 2048 + 1024); F[4 * (s) + 3] = *(const bf16x8*)(sl + bb + (s) * 2048 + 1024); } while (0)
#define MM0(F, s) acc[0][0] = __builtin_amdgcn_mfma_f32_32x32x16_bf16(F[4 * (s) + 2], F[4 * (s) + 0], acc[0][0], 0, 0, 0)
#define MM1(F, s) acc[0][1] = __builtin_amdgcn_mfma_f32_32x32x16_bf16(F[4 * (s) + 3], F[4 * (s) + 0], acc[0][1], 0, 0, 0)
#define MM2(F, s) acc[1][0] = __builtin_amdgcn_mfma_f32_32x32x16_bf16(F[4 * (s) + 2], F[4 * (s) + 1], acc[1][0], 0, 0, 0)
#define MM3(F, s) acc[1][1] = __builtin_amdgcn_mfma_f32_32x32x16_bf16(F[4 * (s) + 3], F[4 * (s) + 1], acc[1][1], 0, 0, 0)
#define MO(F, s) do { acco = __builtin_amdgcn_mfma_f32_32x32x16_bf16(F[4 * (s) + 1], F[4 * (s) + 0], acco, 0, 0, 0); \
                      acco = __builtin_amdgcn_mfma_f32_32x32x16_bf16(F[4 * (s) + 3], F[4 * (s) + 2], acco, 0, 0, 0); } while (0)
#define PERIOD(u, RDX, FR, FM) do { \
    const char* sl = lds + ((u) & 3) * 32768; \
    BAR(); SB(); \
    MM0(FM, 0); SB(); RDX(FR, 0); SB(); MM1(FM, 0); SB(); MM2(FM, 0); SB(); RDX(FR, 1); SB(); MM3(FM, 0); SB(); \
    MM0(FM, 1); SB(); RDX(FR, 2); SB(); MM1(FM, 1); SB(); MM2(FM, 1); SB(); RDX(FR, 3); SB(); MM3(FM, 1); SB(); \
    MM0(FM, 2); MM1(FM, 2); MM2(FM, 2); MM3(FM, 2); \
    MM0(FM, 3); MM1(FM, 3); MM2(FM, 3); SB(); WAIT_L0(); SB(); MM3(FM, 3); SB(); \
  } while (0)
#define PERIODO(u, FR, FM) do { \
    const char* sl = lds + ((u) & 3) * 32768; \
    BAR(); SB(); \
    RDO(FR, 0); SB(); MO(FM, 0); SB(); RDO(FR, 1); SB(); MO(FM, 1); SB(); RDO(FR, 2); SB(); MO(FM, 2); SB(); RDO(FR, 3); SB(); MO(FM, 3); SB(); \
    WAIT_L0(); SB(); \
  } while (0)
#define K2PF 0
#define K2PF_POS 0
  const unsigned dum = (unsigned)(size_t)(LAS char*)lds + 131072 + wid * 256;
  auto touches = [&]() {
    if (K2PF & 1) {
      const i32x4 rb = mk_rsrc(ws + WS_B + ((size_t)(j * NKK_B + c * 16) << 14));
      touch4(rb, dum, ((lane >> 4) + 4 * wid) * 16384 + ((idx >> 2) * 16 + (lane & 15)) * 128, 0);
    }
    if (K2PF & 2) {
      const i32x4 rc = mk_rsrc((const char*)(c < 2 ? cre : cim) + (size_t)(rt * 128 + j * 32) * 4096 + (c & 1) * 2048);
      const int vo = r31 * 4096 + (2 * wid + hh) * 128;
      touch4_nt(rc, dum, vo, 0); touch4_nt(rc, dum, vo, 1024);
    }
  };
  gu64* fl = (gu64*)(ws + WS_CNT);
  if (wid == 0) {
    gu64* f16 = fl + ((4 * (c >> 1) + j) * 2 + (c & 1)) * 16 + (lane & 15);
    for (unsigned spins = 0;; ++spins) {
      const unsigned long long f = __hip_atomic_load(f16, __ATOMIC_RELAXED, __HIP_MEMORY_SCOPE_AGENT);
      if (__all(f == tag) || spins > (1u << 20)) break;
      __builtin_amdgcn_s_sleep(1);
    }
    asm volatile("" ::: "memory");
    ST(1, 7);
  }
  BAR();
  {
    const char* sl = lds;
    BAR(); SB(); ST(1, 1);
    if (K2PF_POS == 1) touches();
    RD(F0, 0); RD(F0, 1); RD(F0, 2); RD(F0, 3); SB();
    WAIT_L0(); SB();
  }
  for (int u = 1; u < 15; u += 2) {
    if ((K2PF & 4) && u == 7) {
      const i32x4 rh = mk_rsrc(ws + WS_H + ((size_t)((rt * 4 + 2 * (wid & 1) + (c >> 1)) * NSUB) << 10) + j * 22528);
      const int l2 = (wid >> 1) * 88 + lane;
      touch4(rh, dum, l2 * 128, 0);
      touch4(rh, dum, (lane < 24 ? l2 + 64 : l2) * 128, 0);
      const i32x4 rw = mk_rsrc(ws + WS_W2 + ((size_t)((j * 4 + 2 * (wid & 1) + (c & 1)) * NSUB) << 10) + (idx >> 2) * 11264);
      touch4(rw, dum, ((wid >> 1) * 44 + (lane < 44 ? lane : 0)) * 128, 0);
    }
    PERIOD(u, RD, F1, F0);
    PERIOD(u + 1, RD, F0, F1);
    if (u == 11 && wid == 0) {
      for (unsigned spins = 0;; ++spins) {
        const unsigned long long f0 = __hip_atomic_load(fl + lane * 4, __ATOMIC_RELAXED, __HIP_MEMORY_SCOPE_AGENT), f1 = __hip_atomic_load(fl + lane * 4 + 1, __ATOMIC_RELAXED, __HIP_MEMORY_SCOPE_AGENT),
                                 f2 = __hip_atomic_load(fl + lane * 4 + 2, __ATOMIC_RELAXED, __HIP_MEMORY_SCOPE_AGENT), f3 = __hip_atomic_load(fl + lane * 4 + 3, __ATOMIC_RELAXED, __HIP_MEMORY_SCOPE_AGENT);
        if (__all(f0 == tag && f1 == tag && f2 == tag && f3 == tag) || spins > (1u << 20)) break;
        __builtin_amdgcn_s_sleep(1);
      }
      asm volatile("" ::: "memory");
    }
  }
  PERIOD(15, RD, F1, F0);
  PERIOD(16, RDO, F0, F1);
  ST(1, 2);
  const int tile = rt * 4 + j;
  const auto rs = __builtin_amdgcn_make_buffer_rsrc(ws + WS_SLAB + ((size_t)tile << 17), 0, (ABL & 16) ? 0 : (1 << 17), 0x00020000);
  const unsigned lo = wid * 2048 + lane * 16;
#pragma unroll
  for (int q = 0; q < 4; ++q) {
    if (q != c) {
      const f32x16 v = acc[q >> 1][q & 1];
#pragma unroll
      for (int hf = 0; hf < 2; ++hf) {
        const u32x4 w = {pk_bf16(v[hf * 8], v[hf * 8 + 1]), pk_bf16(v[hf * 8 + 2], v[hf * 8 + 3]), pk_bf16(v[hf * 8 + 4], v[hf * 8 + 5]), pk_bf16(v[hf * 8 + 6], v[hf * 8 + 7])};
        __builtin_amdgcn_raw_buffer_store_b128(w, rs, (q * 4 + c) * 8192 + lo + hf * 1024, 0, 16);
      }
    }
  }
  const f32x16 own = c == 0 ? acc[0][0] : (c == 1 ? acc[0][1] : (c == 2 ? acc[1][0] : acc[1][1]));
  const int trow = rt * 128 + wm * 64 + (c >> 1) * 32 + r31;
  const int ecol = j * 128 + wn * 64 + (c & 1) * 32 + 8 * hh;
  u32x4 xv[2], bv[2];
#pragma unroll
  for (int half = 0; half < 2; ++half) { xv[half] = *(const u32x4*)(x + (size_t)trow * D_ + ecol + 16 * half); bv[half] = *(const u32x4*)(b2 + ecol + 16 * half); }
  SB();
  gu64* xfl = (gu64*)(ws + WS_CNT + 4096 + tile * 128);
  u32x4 pv[4][2];
#pragma unroll
  for (int s = 0; s < 4; ++s) { pv[s][0] = (u32x4){}; pv[s][1] = (u32x4){}; }
#pragma unroll 1
  for (int u = 17; u < 27; u += 2) {
    PERIODO(u, F1, F0);
    if (u == 19) { WAIT_V(4); ST(1, 3); }
    if (u == 23) {
      asm volatile("" ::: "memory");
#pragma unroll
      for (int s = 0; s < 4; ++s)
#pragma unroll
        for (int hf = 0; hf < 2; ++hf)
          pv[s][hf] = __builtin_amdgcn_raw_buffer_load_b128(rs, (c * 4 + s) * 8192 + lo + hf * 1024, 0, 16);
      SB();
    }
    PERIODO(u + 1, F0, F1);
    if (u == 19 && tid == 0) __hip_atomic_store(xfl + c, tag, __ATOMIC_RELAXED, __HIP_MEMORY_SCOPE_AGENT);
    if (u == 21) {
      if (wid == 0) {
        for (unsigned spins = 0;; ++spins) {
          const unsigned long long f = __hip_atomic_load(xfl + (lane & 3), __ATOMIC_RELAXED, __HIP_MEMORY_SCOPE_AGENT);
          if (__all(f == tag) || spins > (1u << 20)) break;
          __builtin_amdgcn_s_sleep(1);
        }
      }
      asm volatile("" ::: "memory");
      ST(1, 4);
    }
  }
  MO(F0, 0); MO(F0, 1); MO(F0, 2); MO(F0, 3);
  ST(1, 6);
#pragma unroll
  for (int half = 0; half < 2; ++half) {
    float o[8];
#pragma unroll
    for (int e = 0; e < 8; ++e) {
      const int rg = half * 8 + e;
      float p[4];
#pragma unroll
      for (int s = 0; s < 4; ++s) { const unsigned w = pv[s][half][e >> 1]; p[s] = c == s ? own[rg] : ((e & 1) ? bfhi(w) : bflo(w)); }
      const float s = (p[0] + p[1]) + (p[2] + p[3]);
      const unsigned xw = xv[half][e >> 1], bw = bv[half][e >> 1];
      const float xx = (e & 1) ? bfhi(xw) : bflo(xw), bb2 = (e & 1) ? bfhi(bw) : bflo(bw);
      const float v = acco[rg] + bb2;
      const float g = v * __builtin_amdgcn_rcpf(1.f + __builtin_amdgcn_exp2f(-1.442695041f * v));
      o[e] = xx + s * g;
    }
    u32x4 ov = {pk_bf16(o[0], o[1]), pk_bf16(o[2], o[3]), pk_bf16(o[4], o[5]), pk_bf16(o[6], o[7])};
    *(u32x4*)(out + (size_t)trow * D_ + ecol + 16 * half) = ov;
  }
  ST(1, 5); ST_FLUSH(1);
}

extern "C" __device__ unsigned long long amd_dispatch_id(void) __asm("llvm.amdgcn.dispatch.id");

__global__ __launch_bounds__(512, 2) void fused(const u16* __restrict__ x, const u16* __restrict__ cre, const u16* __restrict__ cim,
                                                const u16* __restrict__ wre, const u16* __restrict__ wim, const u16* __restrict__ w1,
                                                const u16* __restrict__ b1, const u16* __restrict__ w2, const u16* __restrict__ b2,
                                                char* __restrict__ ws, u16* __restrict__ out) {
  extern __shared__ __attribute__((aligned(16))) char lds[];
  const unsigned long long tag = ((amd_dispatch_id() + 1ull) * 0x9E3779B97F4A7C15ull) ^ ((unsigned long long)__builtin_amdgcn_dispatch_ptr() * 0xC2B2AE3D27D4EB4Full) ^ 0x5851F42D4C957F2Dull;
  phase1(x, wre, wim, w1, b1, w2, cre, cim, ws, lds);
  BAR();
  if (threadIdx.x == 0) {
    const int b = blockIdx.x, rt = ((b & 7) >> 1) * 4 + ((b >> 3) & 3), cg = (b & 1) * 8 + (b >> 5);
    __hip_atomic_store((gu64*)(ws + WS_CNT) + ((cg >> 1) * 2 + (rt >> 3)) * 16 + (rt & 7) * 2 + (cg & 1), tag, __ATOMIC_RELAXED, __HIP_MEMORY_SCOPE_AGENT);
  }
  phase2(x, cre, cim, b2, ws, out, lds, tag);
  if (threadIdx.x == 0) ((unsigned long long*)(ws + (60u << 20)))[blockIdx.x] = __builtin_amdgcn_s_memrealtime();
}

__global__ void kq(char* ws) {
  unsigned long long t = ((const unsigned long long*)(ws + (60u << 20)))[threadIdx.x];
  __shared__ unsigned long long sm[256]; sm[threadIdx.x] = t; __syncthreads();
  unsigned long long mx = 0; for (int i = 0; i < 256; ++i) mx = sm[i] > mx ? sm[i] : mx;
  while (__builtin_amdgcn_s_memrealtime() - mx < 0ull) __builtin_amdgcn_s_sleep(1);
}
extern "C" void kernel_launch(void* const* d_in, const int* in_sizes, int n_in, void* d_out, int out_size, void* d_ws, size_t ws_size,
                              hipStream_t stream) {
  (void)hipFuncSetAttribute((const void*)fused, hipFuncAttributeMaxDynamicSharedMemorySize, 163840);
  fused<<<256, 512, 163840, stream>>>((const u16*)d_in[0], (const u16*)d_in[1], (const u16*)d_in[2], (const u16*)d_in[3], (const u16*)d_in[4],
                                      (const u16*)d_in[6], (const u16*)d_in[7], (const u16*)d_in[8], (const u16*)d_in[9], (char*)d_ws, (u16*)d_out);
  kq<<<1, 256, 0, stream>>>((char*)d_ws);
}
```

```cpp
#include <hip/hip_runtime.h>
#include <hip/hip_bf16.h>
#include <stdint.h>
#include <type_traits>

#define ABL 0
typedef unsigned short u16;
typedef short v4s __attribute__((ext_vector_type(4)));
typedef short bf16x8 __attribute__((ext_vector_type(8)));
typedef __bf16 v2bf __attribute__((ext_vector_type(2)));
typedef float f32x4 __attribute__((ext_vector_type(4)));
typedef float f32x16 __attribute__((ext_vector_type(16)));
typedef unsigned u32x4 __attribute__((ext_vector_type(4)));
typedef unsigned u32x2 __attribute__((ext_vector_type(2)));
#define LAS __attribute__((address_space(3)))
#define GAS __attribute__((address_space(1)))

constexpr int S_ = 2048, D_ = 512, F_ = 1376;
constexpr int NKK_B = 64;
constexpr int NSUB = 88;
constexpr size_t WS_B    = 0;
constexpr size_t WS_W2   = 4u << 20;
constexpr size_t WS_H    = 8u << 20;
constexpr size_t WS_SLAB = 16u << 20;
constexpr size_t WS_CNT  = 40u << 20;

__device__ __forceinline__ float bf2f(u16 v) { return __uint_as_float(((unsigned)v) << 16); }
__device__ __forceinline__ float bflo(unsigned v) { return __uint_as_float(v << 16); }
__device__ __forceinline__ float bfhi(unsigned v) { return __uint_as_float(v & 0xffff0000u); }
__device__ __forceinline__ unsigned pk_bf16(float lo, float hi) {
  typedef float f32x2_ __attribute__((ext_vector_type(2)));
  v2bf r = __builtin_convertvector((f32x2_){lo, hi}, v2bf);
  return __builtin_bit_cast(unsigned, r);
}
__device__ __forceinline__ float rbf(float f) { return bflo(pk_bf16(f, 0.f)); }
__device__ __forceinline__ int swap23(int r) { return (r & ~12) | ((r & 4) << 1) | ((r & 8) >> 1); }
__device__ __forceinline__ float dot2(unsigned a, unsigned b, float c) {
  return __builtin_amdgcn_fdot2_f32_bf16(__builtin_bit_cast(v2bf, a), __builtin_bit_cast(v2bf, b), c, false);
}
#define GLDS16(g, l) __builtin_amdgcn_global_load_lds((const GAS void*)(g), (LAS void*)(l), 16, 0, 0)
typedef int i32x4 __attribute__((ext_vector_type(4)));
__device__ __forceinline__ i32x4 mk_rsrc(const void* p) {
  const unsigned long long a = (unsigned long long)p;
  return (i32x4){(int)(unsigned)a, (int)((unsigned)(a >> 32) & 0xffffu), 0x7fffffff, 0x00020000};
}
__device__ __forceinline__ void dma16(i32x4 rsrc, unsigned lds_dst, int voff, int soff) {
  unsigned keep;
  asm volatile("s_mov_b32 %0, m0\n\ts_mov_b32 m0, %1\n\ts_nop 0\n\tbuffer_load_dwordx4 %2, %3, %4 offen lds\n\ts_mov_b32 m0, %0"
               : "=&s"(keep) : "s"(lds_dst), "v"(voff), "s"(rsrc), "s"(soff) : "memory");
}
__device__ __forceinline__ void touch4(i32x4 rsrc, unsigned lds_dst, int voff, int soff) {
  unsigned keep;
  asm volatile("s_mov_b32 %0, m0\n\ts_mov_b32 m0, %1\n\ts_nop 0\n\tbuffer_load_dword %2, %3, %4 offen lds\n\ts_mov_b32 m0, %0"
               : "=&s"(keep) : "s"(lds_dst), "v"(voff), "s"(rsrc), "s"(soff) : "memory");
}
template <int OFF> __device__ __forceinline__ void dma16x(i32x4 rsrc, unsigned lds_dst, int voff, int soff) {
  unsigned keep;
  asm volatile("s_mov_b32 %0, m0\n\ts_mov_b32 m0, %1\n\ts_nop 3\n\tbuffer_load_dwordx4 %2, %3, %4 offen offset:%5 lds\n\ts_mov_b32 m0, %0"
               : "=&s"(keep) : "s"(lds_dst), "v"(voff), "s"(rsrc), "s"(soff), "n"(OFF) : "memory");
}
template <int OFF> __device__ __forceinline__ void dma16x_sc1(i32x4 rsrc, unsigned lds_dst, int voff, int soff) {
  unsigned keep;
  asm volatile("s_mov_b32 %0, m0\n\ts_mov_b32 m0, %1\n\ts_nop 3\n\tbuffer_load_dwordx4 %2, %3, %4 offen offset:%5 sc1 lds\n\ts_mov_b32 m0, %0"
               : "=&s"(keep) : "s"(lds_dst), "v"(voff), "s"(rsrc), "s"(soff), "n"(OFF) : "memory");
}
template <int OFF> __device__ __forceinline__ void dma16x_nt(i32x4 rsrc, unsigned lds_dst, int voff, int soff) {
  unsigned keep;
  asm volatile("s_mov_b32 %0, m0\n\ts_mov_b32 m0, %1\n\ts_nop 3\n\tbuffer_load_dwordx4 %2, %3, %4 offen offset:%5 nt lds\n\ts_mov_b32 m0, %0"
               : "=&s"(keep) : "s"(lds_dst), "v"(voff), "s"(rsrc), "s"(soff), "n"(OFF) : "memory");
}
__device__ __forceinline__ void touch4_nt(i32x4 rsrc, unsigned lds_dst, int voff, int soff) {
  unsigned keep;
  asm volatile("s_mov_b32 %0, m0\n\ts_mov_b32 m0, %1\n\ts_nop 0\n\tbuffer_load_dword %2, %3, %4 offen nt lds\n\ts_mov_b32 m0, %0"
               : "=&s"(keep) : "s"(lds_dst), "v"(voff), "s"(rsrc), "s"(soff) : "memory");
}
#define WAIT_V(n) asm volatile("s_waitcnt vmcnt(%0)" ::"n"(n) : "memory")
#define WAIT_L0() asm volatile("s_waitcnt lgkmcnt(0)" ::: "memory")
#define BAR() __builtin_amdgcn_s_barrier()
#define ST_DECL
#define ST(k, i)
#define ST_FLUSH(k)

#define AUX_PF 2
#define PF_ON 0
#define PF_AT 3
#define AUX_ST 16
#define TOUCH 2
typedef float f32x2 __attribute__((ext_vector_type(2)));
constexpr int SA0 = 0, SA1 = 33280, SH1 = 98304, SLP = 131072, AROW = 1040;
constexpr int SCR = 66560;
constexpr int SC_PART = SCR;
constexpr int SC_HS   = SCR + 2048;
constexpr int SC_RS   = SCR + 3072;
constexpr int SC_MRS  = SCR + 3584;
constexpr int SC_CS   = SCR + 4096;
constexpr int SC_B1   = SCR + 7168;
constexpr int SC_DUM  = SCR + 8192;
#define PIN(x) asm volatile("" : "+v"(x))
#define SBAR() __builtin_amdgcn_sched_barrier(0)

template <int K> __device__ __forceinline__ void eh_op(const f32x16& ap, float (&v)[16], float (&t)[16], unsigned (&ow)[8], float scale, float center) {
  constexpr int st = K / 16, e = K % 16;
  if constexpr (st == 0) { v[e] = fmaf(-center, t[e], v[e]); PIN(v[e]); }
  else if constexpr (st == 1) { v[e] = fmaf(ap[e], scale, v[e]); PIN(v[e]); }
  else if constexpr (st == 2) { t[e] = v[e] * v[e]; PIN(t[e]); }
  else if constexpr (st == 3) { t[e] = fmaf(t[e], -0.1029432397f, -2.302208198f); PIN(t[e]); }
  else if constexpr (st == 4) { t[e] = t[e] * v[e]; PIN(t[e]); }
  else if constexpr (st == 5) { t[e] = __builtin_amdgcn_exp2f(t[e]); PIN(t[e]); }
  else if constexpr (st == 6) { t[e] = 1.f + t[e]; PIN(t[e]); }
  else if constexpr (st == 7) { t[e] = __builtin_amdgcn_rcpf(t[e]); PIN(t[e]); }
  else if constexpr (st == 8) { v[e] = v[e] * t[e]; PIN(v[e]); }
  else if constexpr (K < 152) { ow[e] = pk_bf16(v[2 * e], v[2 * e + 1]); PIN(ow[e]); }
}
template <int K> __device__ __forceinline__ void ea_op(const f32x16& ap, float (&v)[16], float (&t)[16], unsigned (&ow)[8], float cs) {
  constexpr int st = K / 16, e = K % 16;
  if constexpr (st == 0) { t[e] = ap[e] * t[e]; PIN(t[e]); }
  else if constexpr (st == 1) { t[e] = fmaf(-v[e], cs, t[e]); PIN(t[e]); }
  else if constexpr (K < 40) { ow[e] = pk_bf16(t[2 * e], t[2 * e + 1]); PIN(ow[e]); }
}
template <int ET, int K0, int K1> __device__ __forceinline__ void e_ops(const f32x16& ap, float (&v)[16], float (&t)[16], unsigned (&ow)[8], float s0, float s1) {
  if constexpr (K0 < K1) {
    if constexpr (ET == 1) eh_op<K0>(ap, v, t, ow, s0, s1); else if constexpr (ET == 2) ea_op<K0>(ap, v, t, ow, s0);
    e_ops<ET, K0 + 1, K1>(ap, v, t, ow, s0, s1);
  }
}
template <int MT> __device__ __forceinline__ bf16x8 wread(const char* pan, int ks) {
  if constexpr (MT == 1) { return *(const bf16x8*)(pan + ks * 32); }
  else {
    v4s t0 = __builtin_amdgcn_ds_read_tr16_b64_v4i16((LAS v4s*)(pan + ks * 1024));
    v4s t1 = __builtin_amdgcn_ds_read_tr16_b64_v4i16((LAS v4s*)(pan + ks * 1024 + 256));
    return (bf16x8)__builtin_shufflevector(t0, t1, 0, 1, 2, 3, 4, 5, 6, 7);
  }
}
__device__ __forceinline__ void row_stats(float sum, float sq, float& mean, float& inv, float& m_bf, float& scale, float& center) {
  mean = sum * (1.f / 512.f);
  const float var = fmaxf(sq * (1.f / 512.f) - mean * mean, 0.f);
  m_bf = rbf(mean);
  const float sd = rbf(sqrtf(rbf(rbf(var) + 1e-5f)));
  inv = 1.f / sd;
  const float m2 = rbf((mean - m_bf) * inv);
  const float v2 = rbf(var * inv * inv);
  const float sd2 = rbf(sqrtf(rbf(v2 + 1e-5f)));
  scale = inv / sd2; center = (m_bf + m2 * sd) * scale;
}
constexpr int WR = 8, WD = 3;
struct RowStat { float mean, var, m_bf, sd, inv, m2, v2, sd2, scale, center; };
template <int S> __device__ __forceinline__ void rs_stage(RowStat& r) {
  if constexpr (S == 0) { r.mean *= (1.f / 512.f); r.var = fmaxf(r.var * (1.f / 512.f) - r.mean * r.mean, 0.f); r.m_bf = rbf(r.mean); PIN(r.var); PIN(r.m_bf); }
  else if constexpr (S == 1) { r.sd = rbf(rbf(r.var) + 1e-5f); PIN(r.sd); }
  else if constexpr (S == 2) { r.sd = rbf(sqrtf(r.sd)); PIN(r.sd); }
  else if constexpr (S == 3) { r.inv = 1.f / r.sd; PIN(r.inv); }
  else if constexpr (S == 4) { r.m2 = rbf((r.mean - r.m_bf) * r.inv); r.v2 = rbf(rbf(r.var * r.inv * r.inv) + 1e-5f); PIN(r.m2); PIN(r.v2); }
  else if constexpr (S == 5) { r.sd2 = rbf(sqrtf(r.v2)); PIN(r.sd2); }
  else if constexpr (S == 6) { r.scale = r.inv / r.sd2; PIN(r.scale); }
  else if constexpr (S == 7) { r.center = (r.m_bf + r.m2 * r.sd) * r.scale; PIN(r.center); }
}
template <int MT, int ET, int NT, int G0, int KS, class Hook>
__device__ __forceinline__ void unit_steps(const char* pan, const char* pnx, const bf16x8 (&xf)[32], bf16x8 (&wf)[WR], f32x16& aw, const f32x16& ar,
                                           float (&v)[16], float (&t)[16], unsigned (&ow)[8], const float& s0, const float& s1, Hook&& hook) {
  if constexpr (KS < 32) {
    constexpr int NOPS = ET == 1 ? 152 : 40;
    if constexpr (KS == 0) {
      if constexpr (MT == 1) aw = __builtin_amdgcn_mfma_f32_32x32x16_bf16(xf[KS], wf[KS % WR], (f32x16){}, 0, 0, 0);
      else aw = __builtin_amdgcn_mfma_f32_32x32x16_bf16(wf[KS % WR], xf[KS], (f32x16){}, 0, 0, 0);
    } else {
      if constexpr (MT == 1) aw = __builtin_amdgcn_mfma_f32_32x32x16_bf16(xf[KS], wf[KS % WR], aw, 0, 0, 0);
      else aw = __builtin_amdgcn_mfma_f32_32x32x16_bf16(wf[KS % WR], xf[KS], aw, 0, 0, 0);
    }
    PIN(aw);
    SBAR();
    if constexpr (KS + WD < 32) wf[(KS + WD) % WR] = wread<MT>(pan, KS + WD);
    else if constexpr (NT >= 0) wf[(KS + WD) % WR] = wread<NT>(pnx, KS + WD - 32);
    asm volatile("" :: "v"(wf[KS % WR]), "v"(wf[(KS + WR - 1) % WR]));
    SBAR();
    hook(std::integral_constant<int, KS>{});
    constexpr int k0 = KS < G0 ? 0 : (KS - G0) * NOPS / (32 - G0), k1 = KS < G0 ? 0 : (KS + 1 - G0) * NOPS / (32 - G0);
    e_ops<ET, k0, k1>(ar, v, t, ow, s0, s1);
    SBAR();
    unit_steps<MT, ET, NT, G0, KS + 1>(pan, pnx, xf, wf, aw, ar, v, t, ow, s0, s1, hook);
  }
}

typedef __attribute__((address_space(1))) unsigned long long gu64;
__device__ __forceinline__ unsigned ring_off(int t) { return (t & 1) * 32768 + (t & 2) * 49152; }
struct Loader {
  i32x4 ra_s, rb_s, ra_o, rb_o; int aoff[4], voff; unsigned ldsw;
  __device__ __forceinline__ void init(const u16* cre, const u16* cim, char* ws, char* lds, int lw, int lane, int c, int rt, int j) {
#pragma unroll
    for (int i = 0; i < 4; ++i) { const int row = lw * 32 + i * 8 + (lane >> 3); aoff[i] = row * 4096 + (((lane & 7) ^ ((row >> 1) & 7)) * 16) - i * 1024 + 4096; }
    ldsw = (unsigned)(size_t)(LAS char*)lds + lw * 4096;
    ra_s = mk_rsrc((const char*)(c < 2 ? cre : cim) + (size_t)rt * 128 * 4096 + (c & 1) * 2048 - 4096);
    rb_s = mk_rsrc(ws + WS_B + ((size_t)(j * NKK_B + c * 16) << 14) + lw * 4096);
    ra_o = mk_rsrc(ws + WS_H + ((size_t)((rt * 4 + 2 * (lw >> 1) + (c >> 1)) * NSUB) << 10) + (lw & 1) * 4096 - (16 << 13));
    rb_o = mk_rsrc(ws + WS_W2 + ((size_t)((j * 4 + 2 * (lw >> 1) + (c & 1)) * NSUB) << 10) + (lw & 1) * 4096 - (16 << 13));
    voff = lane * 16;
  }
  __device__ __forceinline__ void issue_a(int T) const {
    const unsigned dst = __builtin_amdgcn_readfirstlane(ldsw + ring_off(T));
    if (T < 16) { const int so = __builtin_amdgcn_readfirstlane(T * 128);
                  dma16x_nt<0>(ra_s, dst, aoff[0], so); dma16x_nt<1024>(ra_s, dst, aoff[1], so); dma16x_nt<2048>(ra_s, dst, aoff[2], so); dma16x_nt<3072>(ra_s, dst, aoff[3], so); }
    else { i32x4 r = ra_o; r.z = __builtin_amdgcn_readfirstlane(T < 27 ? 0x7fffffff : 0); const int so = __builtin_amdgcn_readfirstlane(T << 13);
           dma16x_sc1<0>(r, dst, voff, so); dma16x_sc1<1024>(r, dst, voff, so); dma16x_sc1<2048>(r, dst, voff, so); dma16x_sc1<3072>(r, dst, voff, so); }
  }
  __device__ __forceinline__ void issue_b(int T) const {
    const unsigned dst = __builtin_amdgcn_readfirstlane(ldsw + ring_off(T) + 16384);
    i32x4 r = T < 16 ? rb_s : rb_o; r.z = T < 27 ? 0x7fffffff : 0;
    const int so = __builtin_amdgcn_readfirstlane(T < 16 ? T << 14 : T << 13);
    dma16x_sc1<0>(r, dst, voff, so); dma16x_sc1<1024>(r, dst, voff, so); dma16x_sc1<2048>(r, dst, voff, so); dma16x_sc1<3072>(r, dst, voff, so);
  }
};

__device__ __forceinline__ void phase1(const u16* __restrict__ x, const u16* __restrict__ wre, const u16* __restrict__ wim,
                                       const u16* __restrict__ w1, const u16* __restrict__ b1, const u16* __restrict__ w2,
                                       char* __restrict__ ws, char* lds, const Loader& ld, const unsigned long long tag) {
  const int tid = threadIdx.x, lane = tid & 63, wid = __builtin_amdgcn_readfirstlane(tid >> 6);
  const int b = blockIdx.x, xcd = b & 7, idx = b >> 3;
  const int rt = (xcd >> 1) * 4 + (idx & 3);
  const int cg = (xcd & 1) * 8 + (idx >> 2);
  const int h0 = cg < 11 ? cg * 3 : 33 + (cg - 11) * 2;
  const bool h3 = cg < 11;
  const int a0 = cg * 2;
  const int m = wid & 3;
  const bool helper = wid >= 4;
  const int r31 = lane & 31, hh = lane >> 5;
  const unsigned lds0 = (unsigned)(size_t)(LAS char*)lds;
  const int prot = idx & 3;
  ST_DECL;
  ST(0, 0);

  const int hoff = (lane >> 2) * (F_ * 2) + (lane & 3) * 16;
  const i32x4 rw1 = mk_rsrc(w1);
  auto piece_h = [&](int u, int q, int slot_off) { dma16(rw1, lds0 + slot_off + q * 1024, hoff, q * 16 * (F_ * 2) + u * 64); };
  const i32x4 rx = mk_rsrc(x + (size_t)(rt * 128 + m * 32) * D_);
  int xo[4];
#pragma unroll
  for (int i = 0; i < 4; ++i) { const int r = i * 4 + (lane >> 4); xo[i] = r * 1024 + (((lane & 15) ^ r) * 16); }
  auto quarter = [&](int q) {
#pragma unroll
    for (int i = 0; i < 8; ++i) dma16(rx, lds0 + q * 32768 + m * 8192 + i * 1024, xo[i & 3], q * 256 + (i >> 2) * 16384);
    piece_h(h0, q * 8 + m * 2, SLP); piece_h(h0, q * 8 + m * 2 + 1, SLP);
  };
  if (helper) { quarter(0); quarter(1); }

  const auto rws = __builtin_amdgcn_make_buffer_rsrc((void*)ws, 0, 0x7fffffff, 0x00020000);
  const int xrow = m * 32 + swap23(r31);
  const unsigned xbase = xrow * 256 + ((hh ^ (xrow & 15)) * 16);
#define QWAIT(q) WAIT_V(9 + 10 * (3 - (q)))

  if (helper) {
    const auto rw2t = __builtin_amdgcn_make_buffer_rsrc((void*)(ws + WS_W2), 0, 16 * NSUB * 1024, 0x00020000);
    const i32x4 rwa = mk_rsrc((a0 < 16 ? wre : wim) + (size_t)(a0 & 15) * 32 * D_);
    auto panel_h = [&](int u, int slot_off) {
#pragma unroll
      for (int k = 0; k < 8; ++k) piece_h(u, m * 8 + ((k + 2 * prot) & 7), slot_off);
    };
    auto panel_a = [&](int w, int slot_off) {
#pragma unroll
      for (int k = 0; k < 8; ++k) { const int q = m * 8 + ((k + 2 * prot) & 7); dma16(rwa, lds0 + slot_off + q * AROW, lane * 16, (w * 32 + q) * 1024); }
    };
    const v4s ones = {0x3f80, 0x3f80, 0x3f80, 0x3f80};
    const char* pzh = lds + ((lane >> 2) & 15) * 64 + (m & 1) * 32 + (lane & 3) * 8;
    auto cs_h_part = [&](f32x4 (&c)[4], int slot_off, int j0, int n) {
#pragma unroll
      for (int j = 0; j < n; ++j)
        c[j & 3] = __builtin_amdgcn_mfma_f32_4x4x4bf16_1k(ones, __builtin_amdgcn_ds_read_tr16_b64_v4i16((LAS v4s*)(pzh + slot_off + (j0 + j) * 1024)), c[j & 3], 0, 0, 0);
    };
    auto cs_h_fin = [&](f32x4 (&c)[4], int i) {
      float r = (c[0][0] + c[1][0]) + (c[2][0] + c[3][0]);
      r += __shfl_xor(r, 16); r += __shfl_xor(r, 32);
      if (lane < 16) *(float*)(lds + SC_CS + i * 512 + (m & 1) * 64 + lane * 4) = r;
    };
    auto cs_h = [&](int i, int slot_off) {
      if ((m >> 1) == (i & 1)) { f32x4 c[4] = {}; cs_h_part(c, slot_off, 0, 32); cs_h_fin(c, i); }
    };
    auto cs_a = [&](int i, int slot_off) {
      const char* pz = lds + slot_off + swap23(r31) * AROW + hh * 16 + m * 256;
      f32x4 c0 = {}, c1 = {};
#pragma unroll
      for (int j = 0; j < 8; ++j) {
        const bf16x8 w = *(const bf16x8*)(pz + j * 32);
        c0 = __builtin_amdgcn_mfma_f32_4x4x4bf16_1k(ones, __builtin_shufflevector(w, w, 0, 1, 2, 3), c0, 0, 0, 0);
        c1 = __builtin_amdgcn_mfma_f32_4x4x4bf16_1k(ones, __builtin_shufflevector(w, w, 4, 5, 6, 7), c1, 0, 0, 0);
      }
      float r = c0[0] + c1[0]; r += __shfl_xor(r, 32);
      if (lane < 32) *(float*)(lds + SC_CS + i * 512 + m * 128 + lane * 4) = r;
    };
    __builtin_amdgcn_s_setprio(2);
    {
      f32x4 c[4] = {};
      WAIT_V(10); ST(0, 1); BAR();
      quarter(2);
      if (m < 2) cs_h_part(c, SLP, 0, 8);
      WAIT_V(10); BAR();
      quarter(3);
      if (m < 2) cs_h_part(c, SLP, 8, 8);
      WAIT_V(10); BAR();
      panel_a(0, SA0);
      unsigned w2v[2][8], b1v;
      int w2dst[2];
      {
        i32x4 rw2 = mk_rsrc(w2); rw2.z = F_ * D_ * 2;
#pragma unroll
        for (int r = 0; r < 2; ++r) {
          const int hl = r * 256 + (tid - 256), cc = b * 352 + hl;
          const int ln = cc & 63, rest = cc >> 6, ct = rest / NSUB, sub = rest - ct * NSUB;
          const int off = hl < 352 ? ((16 * sub + 4 * (ln >> 5)) * D_ + 32 * ct + swap23(ln & 31)) * 2 : 0x7ff00000;
          w2dst[r] = hl < 352 ? cc * 16 : 0x7ffffff0;
          asm volatile("buffer_load_ushort %0, %8, %10, 0 offen\n\tbuffer_load_ushort %1, %8, %10, 0 offen offset:1024\n\t"
                       "buffer_load_ushort %2, %8, %10, 0 offen offset:2048\n\tbuffer_load_ushort %3, %8, %10, 0 offen offset:3072\n\t"
                       "buffer_load_ushort %4, %9, %10, 0 offen\n\tbuffer_load_ushort %5, %9, %10, 0 offen offset:1024\n\t"
                       "buffer_load_ushort %6, %9, %10, 0 offen offset:2048\n\tbuffer_load_ushort %7, %9, %10, 0 offen offset:3072"
                       : "=&v"(w2v[r][0]), "=&v"(w2v[r][1]), "=&v"(w2v[r][2]), "=&v"(w2v[r][3]), "=&v"(w2v[r][4]), "=&v"(w2v[r][5]), "=&v"(w2v[r][6]), "=&v"(w2v[r][7])
                       : "v"(off), "v"(off + 8192), "s"(rw2) : "memory");
        }
        const int bo = h0 * 32 + lane * 2;
        asm volatile("buffer_load_dword %0, %1, %2, 0 offen" : "=&v"(b1v) : "v"((bo < F_ - 2 ? bo : F_ - 2) * 2), "s"(mk_rsrc(b1)) : "memory");
      }
      if (m < 2) cs_h_part(c, SLP, 16, 8);
      WAIT_V(25); BAR();
      ST(0, 2);
      panel_a(1, SA1);
      if (m < 2) { cs_h_part(c, SLP, 24, 8); cs_h_fin(c, 0); }
      WAIT_V(8);
      asm volatile("" : "+v"(w2v[0][0]), "+v"(w2v[0][1]), "+v"(w2v[0][2]), "+v"(w2v[0][3]), "+v"(w2v[0][4]), "+v"(w2v[0][5]), "+v"(w2v[0][6]), "+v"(w2v[0][7]), "+v"(b1v));
      asm volatile("" : "+v"(w2v[1][0]), "+v"(w2v[1][1]), "+v"(w2v[1][2]), "+v"(w2v[1][3]), "+v"(w2v[1][4]), "+v"(w2v[1][5]), "+v"(w2v[1][6]), "+v"(w2v[1][7]));
      if (m == 3 && lane < 48) *(f32x2*)(lds + SC_B1 + lane * 8) = (f32x2){bflo(b1v), bfhi(b1v)};
#pragma unroll
      for (int r = 0; r < 2; ++r) {
        const u32x4 w = {w2v[r][0] | (w2v[r][1] << 16), w2v[r][2] | (w2v[r][3] << 16), w2v[r][4] | (w2v[r][5] << 16), w2v[r][6] | (w2v[r][7] << 16)};
        __builtin_amdgcn_raw_buffer_store_b128(w, rw2t, w2dst[r], 0, AUX_ST);
      }
    }
    ST(0, 3);
    WAIT_V(2); WAIT_L0(); BAR();
    panel_h(h0 + 1, SH1);
    panel_h(h3 ? h0 + 2 : h0, SLP);
    cs_a(1, SA0);
    {
      float mean, inv, m_bf, sc_, ce_;
      const f32x2 pr = *(const f32x2*)(lds + SC_PART + (m * 32 + r31) * 8);
      row_stats(pr.x, pr.y, mean, inv, m_bf, sc_, ce_);
      const float sg = a0 < 16 ? inv : -inv;
      if (lane < 32) { *(float*)(lds + SC_RS + (m * 32 + lane) * 4) = sg; *(float*)(lds + SC_MRS + (m * 32 + lane) * 4) = m_bf * sg; }
    }
    WAIT_V(8); WAIT_L0(); BAR();
    cs_a(2, SA1);
    WAIT_V(0);
#define CT 1
    {
      const int j2 = idx & 3;
      const int st1 = 3 + 2 * m + hh, st2 = st1 + 8 < 15 ? st1 + 8 : 15;
      const int vb = (j2 * 32 + r31) * 4096 + 4096;
      if (CT == 2) { touch4_nt(ld.ra_s, lds0 + SC_DUM + m * 256, vb + st1 * 128, 0); touch4_nt(ld.ra_s, lds0 + SC_DUM + m * 256, vb + st2 * 128, 0); }
      else { touch4(ld.ra_s, lds0 + SC_DUM + m * 256, vb + st1 * 128, 0); touch4(ld.ra_s, lds0 + SC_DUM + m * 256, vb + st2 * 128, 0); }
    }
    WAIT_L0(); BAR();
    cs_h(3, SH1);
    ld.issue_a(0); ld.issue_a(1);
    WAIT_L0(); BAR();
    cs_h(4, SLP);
    ld.issue_a(2);
    WAIT_L0(); BAR();
    if (tid == 256) __hip_atomic_store((gu64*)(ws + WS_CNT) + ((cg >> 1) * 2 + (rt >> 3)) * 16 + (rt & 7) * 2 + (cg & 1), tag, __ATOMIC_RELAXED, __HIP_MEMORY_SCOPE_AGENT);
    BAR();
    __builtin_amdgcn_s_setprio(0);
    ST(0, 5); ST_FLUSH(0);
    return;
  }

  bf16x8 xf[32];
  unsigned long long clk0 = 0; (void)clk0;
  const int wrow = swap23(r31);
  const unsigned wlA = (unsigned)(wrow * AROW + hh * 16);
  const unsigned wlH = (unsigned)((8 * hh + ((lane & 15) >> 2)) * 64 + ((lane >> 4) & 1) * 32 + (lane & 3) * 8);
  f32x16 acc, ac2;
  bf16x8 wf[WR];
  float v[16], t[16]; unsigned ow[8];
  auto nohook = [](auto) {};
  RowStat rs;
  const int hstore = (swap23(r31) + 32 * hh) * 16;
  const int s0r = rt * 128 + m * 32;

  auto tables_h = [&](int i, int hu) {
#pragma unroll
    for (int g4 = 0; g4 < 4; ++g4) {
      const f32x4 c = *(const f32x4*)(lds + SC_CS + i * 512 + (8 * g4 + 4 * hh) * 4), bq = *(const f32x4*)(lds + SC_B1 + hu * 128 + (8 * g4 + 4 * hh) * 4);
#pragma unroll
      for (int e = 0; e < 4; ++e) { t[g4 * 4 + e] = c[e]; v[g4 * 4 + e] = bq[e]; }
    }
  };
  auto tables_a = [&](int i) -> float {
#pragma unroll
    for (int g4 = 0; g4 < 4; ++g4) {
      const f32x4 c = *(const f32x4*)(lds + SC_RS + (m * 32 + 8 * g4 + 4 * hh) * 4), bq = *(const f32x4*)(lds + SC_MRS + (m * 32 + 8 * g4 + 4 * hh) * 4);
#pragma unroll
      for (int e = 0; e < 4; ++e) { t[g4 * 4 + e] = c[e]; v[g4 * 4 + e] = bq[e]; }
    }
    const char* cq = lds + SC_CS + i * 512 + r31 * 4;
    return (*(const float*)cq + *(const float*)(cq + 128)) + (*(const float*)(cq + 256) + *(const float*)(cq + 384));
  };
  auto tables_a4 = [&](int i, float (&cp)[4]) {
#pragma unroll
    for (int g4 = 0; g4 < 4; ++g4) {
      const f32x4 c = *(const f32x4*)(lds + SC_RS + (m * 32 + 8 * g4 + 4 * hh) * 4), bq = *(const f32x4*)(lds + SC_MRS + (m * 32 + 8 * g4 + 4 * hh) * 4);
#pragma unroll
      for (int e = 0; e < 4; ++e) { t[g4 * 4 + e] = c[e]; v[g4 * 4 + e] = bq[e]; }
      cp[g4] = *(const float*)(lds + SC_CS + i * 512 + g4 * 128 + r31 * 4);
    }
  };
  auto store_h = [&](int hu, const unsigned (&ow)[8]) {
    const int dst = (int)WS_H + (((rt * 4 + m) * NSUB + 2 * (h0 + hu)) << 10);
    if (hu < 2 || h3) {
      __builtin_amdgcn_raw_buffer_store_b128((u32x4){ow[0], ow[1], ow[2], ow[3]}, rws, hstore, dst, AUX_ST);
      __builtin_amdgcn_raw_buffer_store_b128((u32x4){ow[4], ow[5], ow[6], ow[7]}, rws, hstore + 1024, dst, AUX_ST);
    }
    if (h0 + hu == 42 && (hu < 2 || h3)) { u32x4 z = {0u, 0u, 0u, 0u}; __builtin_amdgcn_raw_buffer_store_b128(z, rws, lane * 16 + 2048, dst, AUX_ST); __builtin_amdgcn_raw_buffer_store_b128(z, rws, lane * 16 + 3072, dst, AUX_ST); }
  };
  auto store_a = [&](int w, const unsigned (&ow)[8]) {
    const int u = a0 + w, et = u & 15, j = et >> 2, nt = et & 3;
    const int kk = (u >> 4) * 32 + (s0r >> 6), sub = (s0r & 63) >> 4;
    const int dst = (int)WS_B + (((j * NKK_B + kk) * 16 + nt * 4 + sub) << 10);
    __builtin_amdgcn_raw_buffer_store_b128((u32x4){ow[0], ow[1], ow[2], ow[3]}, rws, lane * 16, dst, AUX_ST);
    __builtin_amdgcn_raw_buffer_store_b128((u32x4){ow[4], ow[5], ow[6], ow[7]}, rws, lane * 16 + 1024, dst, AUX_ST);
  };
  {
    f32x4 ss0 = {}, ss1 = {}, sq0 = {}, sq1 = {};
    const v4s ones4 = {0x3f80, 0x3f80, 0x3f80, 0x3f80};
    acc = (f32x16){};
    const char* pan = lds + SLP + wlH;
#pragma unroll
    for (int q = 0; q < 4; ++q) {
      if (q == 0) ST(0, 1);
      BAR();
      if (q == 3) ST(0, 2);
      bf16x8 w8[8];
#pragma unroll
      for (int k = 0; k < 8; ++k) xf[q * 8 + k] = *(const bf16x8*)(lds + q * 32768 + (xbase ^ (k * 32)));
#pragma unroll
      for (int k = 0; k < 8; ++k) w8[k] = wread<0>(pan, q * 8 + k);
      SBAR();
#pragma unroll
      for (int k = 0; k < 8; ++k) {
        const v4s lo4 = __builtin_shufflevector(xf[q * 8 + k], xf[q * 8 + k], 0, 1, 2, 3), hi4 = __builtin_shufflevector(xf[q * 8 + k], xf[q * 8 + k], 4, 5, 6, 7);
        ss0 = __builtin_amdgcn_mfma_f32_4x4x4bf16_1k(ones4, lo4, ss0, 0, 0, 0); sq0 = __builtin_amdgcn_mfma_f32_4x4x4bf16_1k(lo4, lo4, sq0, 0, 0, 0);
        ss1 = __builtin_amdgcn_mfma_f32_4x4x4bf16_1k(ones4, hi4, ss1, 0, 0, 0); sq1 = __builtin_amdgcn_mfma_f32_4x4x4bf16_1k(hi4, hi4, sq1, 0, 0, 0);
      }
      asm volatile("" : "+v"(ss0), "+v"(ss1), "+v"(sq0), "+v"(sq1));
      SBAR();
#pragma unroll
      for (int k = 0; k < 8; ++k) { acc = __builtin_amdgcn_mfma_f32_32x32x16_bf16(w8[k], xf[q * 8 + k], acc, 0, 0, 0); PIN(acc); }
      WAIT_L0(); SBAR();
    }
    const f32x4 sqv = sq0 + sq1;
    float sum = ss0[0] + ss1[0], sq = (lane & 2) ? ((lane & 1) ? sqv[3] : sqv[2]) : ((lane & 1) ? sqv[1] : sqv[0]);
    sum += __shfl_xor(sum, 32); sq += __shfl_xor(sq, 32);
    if (lane < 32) *(f32x2*)(lds + SC_PART + (m * 32 + lane) * 8) = (f32x2){sum, sq};
    rs.mean = sum; rs.var = sq;
  }
  ST(0, 8);
  WAIT_L0(); BAR();
  ST(0, 4);
  const char* pA0 = lds + SA0 + wlA; const char* pA1 = lds + SA1 + wlA;
  const char* pH1 = lds + SH1 + wlH; const char* pH2 = lds + SLP + wlH;
#pragma unroll
  for (int e = 0; e < WD; ++e) wf[e] = wread<1>(pA0, e);
  wf[WR - 1] = wf[0];
  tables_h(0, 0);
  float csa = 0.f, cp[4];
  auto cs_hook = [&](auto g) { if constexpr (decltype(g)::value == 2) { csa = (cp[0] + cp[1]); PIN(csa); float c2 = cp[2] + cp[3]; PIN(c2); csa += c2; PIN(csa); } };
  unit_steps<1, 1, 1, 8, 0>(pA0, pA1, xf, wf, ac2, acc, v, t, ow, rs.scale, rs.center, [&](auto g) {
    constexpr int G = decltype(g)::value;
    if constexpr (G < 8) rs_stage<G>(rs);
  });
  store_h(0, ow);
  ST(0, 9);
  BAR();
  ST(0, 6);
  tables_a4(1, cp);
  unit_steps<1, 2, 0, 3, 0>(pA1, pH1, xf, wf, acc, ac2, v, t, ow, csa, csa, cs_hook); store_a(0, ow);
  ST(0, 10);
  BAR();
  ST(0, 7);
  tables_a4(2, cp);
  unit_steps<0, 2, 0, 3, 0>(pH1, pH2, xf, wf, ac2, acc, v, t, ow, csa, csa, cs_hook); store_a(1, ow);
  ST(0, 11);
  BAR();
  ST(0, 13);
  tables_h(3, 1);
  unit_steps<0, 1, -1, 3, 0>(pH2, pH2, xf, wf, acc, ac2, v, t, ow, rs.scale, rs.center, [&](auto g) {
    if constexpr (decltype(g)::value == 14) { WAIT_V(0); BAR(); }
  });
  store_h(1, ow);
  ST(0, 12);
  BAR();
  tables_h(4, 2);
  {
#pragma unroll
    for (int e = 0; e < 8; ++e) {
      f32x2 vv = (f32x2){v[2 * e], v[2 * e + 1]} - (f32x2){t[2 * e], t[2 * e + 1]} * rs.center;
      vv = (f32x2){acc[2 * e], acc[2 * e + 1]} * rs.scale + vv;
      const f32x2 tt = ((vv * vv) * (-0.1029432397f) + (-2.302208198f)) * vv;
      const f32x2 d = (f32x2){__builtin_amdgcn_exp2f(tt.x), __builtin_amdgcn_exp2f(tt.y)} + 1.f;
      vv = vv * (f32x2){__builtin_amdgcn_rcpf(d.x), __builtin_amdgcn_rcpf(d.y)};
      ow[e] = pk_bf16(vv.x, vv.y);
    }
  }
  store_h(2, ow);
  WAIT_V(0);
  ST(0, 5); ST_FLUSH(0);
}

#define AUX_C 2
__device__ __forceinline__ void phase2(const u16* __restrict__ x, const u16* __restrict__ cre, const u16* __restrict__ cim,
                                       const u16* __restrict__ b2, char* __restrict__ ws, u16* __restrict__ out, char* lds, const Loader& ld, const unsigned long long tag) {
  const int tid = threadIdx.x, lane = tid & 63, wid = __builtin_amdgcn_readfirstlane(tid >> 6);
  const int b = blockIdx.x, xcd = b & 7, idx = b >> 3;
#define MAP2 0
  const int c = MAP2 ? (xcd >> 2) * 2 + (idx >> 4) : xcd >> 1, rt = MAP2 ? (xcd & 3) * 4 + ((idx >> 2) & 3) : (xcd & 1) * 8 + (idx >> 2), j = idx & 3;
  ST_DECL;
  ST(1, 0);

#define SB() __builtin_amdgcn_sched_barrier(0)

  if (wid >= 4) {
    gu64* f16 = (gu64*)(ws + WS_CNT) + ((4 * (c >> 1) + j) * 2 + (c & 1)) * 16 + (lane & 15);
    for (unsigned spins = 0;; ++spins) {
      const unsigned long long f = __hip_atomic_load(f16, __ATOMIC_RELAXED, __HIP_MEMORY_SCOPE_AGENT);
      if (__all(f == tag) || spins > (1u << 20)) break;
      __builtin_amdgcn_s_sleep(1);
    }
    asm volatile("" ::: "memory");
    ld.issue_b(0); ld.issue_b(1); ld.issue_b(2);
    for (int u = 0; u < 27; ++u) {
      if (u == 0) WAIT_V(8); else if (u == 1) WAIT_V(12); else WAIT_V(16);
      BAR();
      ld.issue_a(u + 3); ld.issue_b(u + 3);
    }
    WAIT_V(0);
    return;
  }

  const int r31 = lane & 31, hh = lane >> 5;
  const int wm = wid >> 1, wn = wid & 1;
  unsigned ab[4];
#pragma unroll
  for (int s = 0; s < 4; ++s) ab[s] = wm * 8192 + (unsigned)(r31 * 128 + (((2 * s + hh) ^ ((r31 >> 1) & 7)) * 16));
  const unsigned bb = 16384 + wn * 8192 + lane * 16;
  const unsigned ao = wm * 8192 + lane * 16;

  f32x16 acc[2][2] = {};
  f32x16 acco = {};
  bf16x8 F0[16], F1[16];
#define RD(F, s) do { F[4 * (s) + 0] = *(const bf16x8*)(sl + ab[s]); F[4 * (s) + 1] = *(const bf16x8*)(sl + ab[s] + 4096); \
                      F[4 * (s) + 2] = *(const bf16x8*)(sl + bb + (s) * 1024); F[4 * (s) + 3] = *(const bf16x8*)(sl + bb + (s) * 1024 + 4096); } while (0)
#define RDO(F, s) do { F[4 * (s) + 0] = *(const bf16x8*)(sl + ao + (s) * 2048); F[4 * (s) + 1] = *(const bf16x8*)(sl + bb + (s) * 2048); \
                       F[4 * (s) + 2] = *(const bf16x8*)(sl + ao + (s) * 2048 + 1024); F[4 * (s) + 3] = *(const bf16x8*)(sl + bb + (s) * 2048 + 1024); } while (0)
#define MM0(F, s) acc[0][0] = __builtin_amdgcn_mfma_f32_32x32x16_bf16(F[4 * (s) + 2], F[4 * (s) + 0], acc[0][0], 0, 0, 0)
#define MM1(F, s) acc[0][1] = __builtin_amdgcn_mfma_f32_32x32x16_bf16(F[4 * (s) + 3], F[4 * (s) + 0], acc[0][1], 0, 0, 0)
#define MM2(F, s) acc[1][0] = __builtin_amdgcn_mfma_f32_32x32x16_bf16(F[4 * (s) + 2], F[4 * (s) + 1], acc[1][0], 0, 0, 0)
#define MM3(F, s) acc[1][1] = __builtin_amdgcn_mfma_f32_32x32x16_bf16(F[4 * (s) + 3], F[4 * (s) + 1], acc[1][1], 0, 0, 0)
#define MO(F, s) do { acco = __builtin_amdgcn_mfma_f32_32x32x16_bf16(F[4 * (s) + 1], F[4 * (s) + 0], acco, 0, 0, 0); \
                      acco = __builtin_amdgcn_mfma_f32_32x32x16_bf16(F[4 * (s) + 3], F[4 * (s) + 2], acco, 0, 0, 0); } while (0)
#define PERIOD(u, RDX, FR, FM) do { \
    const char* sl = lds + ring_off(u); \
    BAR(); SB(); \
    MM0(FM, 0); SB(); RDX(FR, 0); SB(); MM1(FM, 0); SB(); MM2(FM, 0); SB(); RDX(FR, 1); SB(); MM3(FM, 0); SB(); \
    MM0(FM, 1); SB(); RDX(FR, 2); SB(); MM1(FM, 1); SB(); MM2(FM, 1); SB(); RDX(FR, 3); SB(); MM3(FM, 1); SB(); \
    MM0(FM, 2); MM1(FM, 2); MM2(FM, 2); MM3(FM, 2); \
    MM0(FM, 3); MM1(FM, 3); MM2(FM, 3); SB(); WAIT_L0(); SB(); MM3(FM, 3); SB(); \
  } while (0)
#define PERIODO(u, FR, FM) do { \
    const char* sl = lds + ring_off(u); \
    BAR(); SB(); \
    RDO(FR, 0); SB(); MO(FM, 0); SB(); RDO(FR, 1); SB(); MO(FM, 1); SB(); RDO(FR, 2); SB(); MO(FM, 2); SB(); RDO(FR, 3); SB(); MO(FM, 3); SB(); \
    WAIT_L0(); SB(); \
  } while (0)
  gu64* fl = (gu64*)(ws + WS_CNT + 2048);
  {
    const char* sl = lds;
    BAR(); SB(); ST(1, 1);
    if (tid == 0) __hip_atomic_store(fl + blockIdx.x, tag, __ATOMIC_RELAXED, __HIP_MEMORY_SCOPE_AGENT);

    RD(F0, 0); RD(F0, 1); RD(F0, 2); RD(F0, 3); SB();
    WAIT_L0(); SB();
  }
  for (int u = 1; u < 15; u += 2) {
    PERIOD(u, RD, F1, F0);
    PERIOD(u + 1, RD, F0, F1);
    if (u == 11 && wid == 0) {
      for (unsigned spins = 0;; ++spins) {
        const unsigned long long f0 = __hip_atomic_load(fl + lane * 4, __ATOMIC_RELAXED, __HIP_MEMORY_SCOPE_AGENT), f1 = __hip_atomic_load(fl + lane * 4 + 1, __ATOMIC_RELAXED, __HIP_MEMORY_SCOPE_AGENT),
                                 f2 = __hip_atomic_load(fl + lane * 4 + 2, __ATOMIC_RELAXED, __HIP_MEMORY_SCOPE_AGENT), f3 = __hip_atomic_load(fl + lane * 4 + 3, __ATOMIC_RELAXED, __HIP_MEMORY_SCOPE_AGENT);
        if (__all(f0 == tag && f1 == tag && f2 == tag && f3 == tag) || spins > (1u << 20)) break;
        __builtin_amdgcn_s_sleep(1);
      }
      asm volatile("" ::: "memory");
    }
  }
  PERIOD(15, RD, F1, F0);
  PERIOD(16, RDO, F0, F1);
  ST(1, 2);
  const int tile = rt * 4 + j;
  const auto rs = __builtin_amdgcn_make_buffer_rsrc(ws + WS_SLAB + ((size_t)tile << 17), 0, (ABL & 16) ? 0 : (1 << 17), 0x00020000);
  const unsigned lo = wid * 2048 + lane * 16;
#pragma unroll
  for (int q = 0; q < 4; ++q) {
    if (q != c) {
      const f32x16 v = acc[q >> 1][q & 1];
#pragma unroll
      for (int hf = 0; hf < 2; ++hf) {
        const u32x4 w = {pk_bf16(v[hf * 8], v[hf * 8 + 1]), pk_bf16(v[hf * 8 + 2], v[hf * 8 + 3]), pk_bf16(v[hf * 8 + 4], v[hf * 8 + 5]), pk_bf16(v[hf * 8 + 6], v[hf * 8 + 7])};
        __builtin_amdgcn_raw_buffer_store_b128(w, rs, (q * 4 + c) * 8192 + lo + hf * 1024, 0, 16);
      }
    }
  }
  const f32x16 own = c == 0 ? acc[0][0] : (c == 1 ? acc[0][1] : (c == 2 ? acc[1][0] : acc[1][1]));
  const int trow = rt * 128 + wm * 64 + (c >> 1) * 32 + r31;
  const int ecol = j * 128 + wn * 64 + (c & 1) * 32 + 8 * hh;
  u32x4 xv[2], bv[2];
#pragma unroll
  for (int half = 0; half < 2; ++half) { xv[half] = *(const u32x4*)(x + (size_t)trow * D_ + ecol + 16 * half); bv[half] = *(const u32x4*)(b2 + ecol + 16 * half); }
  SB();
  gu64* xfl = (gu64*)(ws + WS_CNT + 4096 + tile * 128);
  u32x4 pv[4][2];
#pragma unroll
  for (int s = 0; s < 4; ++s) { pv[s][0] = (u32x4){}; pv[s][1] = (u32x4){}; }
#pragma unroll 1
  for (int u = 17; u < 27; u += 2) {
    PERIODO(u, F1, F0);
    if (u == 19) { WAIT_V(4); ST(1, 3); }
    if (u == 23) {
      asm volatile("" ::: "memory");
#pragma unroll
      for (int s = 0; s < 4; ++s)
#pragma unroll
        for (int hf = 0; hf < 2; ++hf)
          pv[s][hf] = __builtin_amdgcn_raw_buffer_load_b128(rs, (c * 4 + s) * 8192 + lo + hf * 1024, 0, 16);
      SB();
    }
    PERIODO(u + 1, F0, F1);
    if (u == 19 && tid == 0) __hip_atomic_store(xfl + c, tag, __ATOMIC_RELAXED, __HIP_MEMORY_SCOPE_AGENT);
    if (u == 21) {
      if (wid == 0) {
        for (unsigned spins = 0;; ++spins) {
          const unsigned long long f = __hip_atomic_load(xfl + (lane & 3), __ATOMIC_RELAXED, __HIP_MEMORY_SCOPE_AGENT);
          if (__all(f == tag) || spins > (1u << 20)) break;
          __builtin_amdgcn_s_sleep(1);
        }
      }
      asm volatile("" ::: "memory");
      ST(1, 4);
    }
  }
  MO(F0, 0); MO(F0, 1); MO(F0, 2); MO(F0, 3);
  ST(1, 6);
#pragma unroll
  for (int half = 0; half < 2; ++half) {
    float o[8];
#pragma unroll
    for (int e = 0; e < 8; ++e) {
      const int rg = half * 8 + e;
      float p[4];
#pragma unroll
      for (int s = 0; s < 4; ++s) { const unsigned w = pv[s][half][e >> 1]; p[s] = c == s ? own[rg] : ((e & 1) ? bfhi(w) : bflo(w)); }
      const float s = (p[0] + p[1]) + (p[2] + p[3]);
      const unsigned xw = xv[half][e >> 1], bw = bv[half][e >> 1];
      const float xx = (e & 1) ? bfhi(xw) : bflo(xw), bb2 = (e & 1) ? bfhi(bw) : bflo(bw);
      const float v = acco[rg] + bb2;
      const float g = v * __builtin_amdgcn_rcpf(1.f + __builtin_amdgcn_exp2f(-1.442695041f * v));
      o[e] = xx + s * g;
    }
    u32x4 ov = {pk_bf16(o[0], o[1]), pk_bf16(o[2], o[3]), pk_bf16(o[4], o[5]), pk_bf16(o[6], o[7])};
    *(u32x4*)(out + (size_t)trow * D_ + ecol + 16 * half) = ov;
  }
  ST(1, 5); ST_FLUSH(1);
}

extern "C" __device__ unsigned long long amd_dispatch_id(void) __asm("llvm.amdgcn.dispatch.id");

__global__ __launch_bounds__(512, 2) void fused(const u16* __restrict__ x, const u16* __restrict__ cre, const u16* __restrict__ cim,
                                                const u16* __restrict__ wre, const u16* __restrict__ wim, const u16* __restrict__ w1,
                                                const u16* __restrict__ b1, const u16* __restrict__ w2, const u16* __restrict__ b2,
                                                char* __restrict__ ws, u16* __restrict__ out) {
  extern __shared__ __attribute__((aligned(16))) char lds[];
  const unsigned long long tag = ((amd_dispatch_id() + 1ull) * 0x9E3779B97F4A7C15ull) ^ ((unsigned long long)__builtin_amdgcn_dispatch_ptr() * 0xC2B2AE3D27D4EB4Full) ^ 0x5851F42D4C957F2Dull;
  Loader ld;
  {
    const int b = blockIdx.x, xcd = b & 7, idx = b >> 3;
    ld.init(cre, cim, ws, lds, __builtin_amdgcn_readfirstlane(threadIdx.x >> 6) & 3, threadIdx.x & 63, xcd >> 1, (xcd & 1) * 8 + (idx >> 2), idx & 3);
  }
  phase1(x, wre, wim, w1, b1, w2, ws, lds, ld, tag);
  phase2(x, cre, cim, b2, ws, out, lds, ld, tag);
}

extern "C" void kernel_launch(void* const* d_in, const int* in_sizes, int n_in, void* d_out, int out_size, void* d_ws, size_t ws_size,
                              hipStream_t stream) {
  (void)hipFuncSetAttribute((const void*)fused, hipFuncAttributeMaxDynamicSharedMemorySize, 163840);
  fused<<<256, 512, 163840, stream>>>((const u16*)d_in[0], (const u16*)d_in[1], (const u16*)d_in[2], (const u16*)d_in[3], (const u16*)d_in[4],
                                      (const u16*)d_in[6], (const u16*)d_in[7], (const u16*)d_in[8], (const u16*)d_in[9], (char*)d_ws, (u16*)d_out);
}
```

```cpp
#include <hip/hip_runtime.h>
#include <hip/hip_bf16.h>
#include <stdint.h>
#include <type_traits>

#define ABL 0
typedef unsigned short u16;
typedef short v4s __attribute__((ext_vector_type(4)));
typedef short bf16x8 __attribute__((ext_vector_type(8)));
typedef __bf16 v2bf __attribute__((ext_vector_type(2)));
typedef float f32x4 __attribute__((ext_vector_type(4)));
typedef float f32x16 __attribute__((ext_vector_type(16)));
typedef unsigned u32x4 __attribute__((ext_vector_type(4)));
typedef unsigned u32x2 __attribute__((ext_vector_type(2)));
#define LAS __attribute__((address_space(3)))
#define GAS __attribute__((address_space(1)))

constexpr int S_ = 2048, D_ = 512, F_ = 1376;
constexpr int NKK_B = 64;
constexpr int NSUB = 88;
constexpr size_t WS_B    = 0;
constexpr size_t WS_W2   = 4u << 20;
constexpr size_t WS_H    = 8u << 20;
constexpr size_t WS_SLAB = 16u << 20;
constexpr size_t WS_CNT  = 40u << 20;

__device__ __forceinline__ float bf2f(u16 v) { return __uint_as_float(((unsigned)v) << 16); }
__device__ __forceinline__ float bflo(unsigned v) { return __uint_as_float(v << 16); }
__device__ __forceinline__ float bfhi(unsigned v) { return __uint_as_float(v & 0xffff0000u); }
__device__ __forceinline__ unsigned pk_bf16(float lo, float hi) {
  typedef float f32x2_ __attribute__((ext_vector_type(2)));
  v2bf r = __builtin_convertvector((f32x2_){lo, hi}, v2bf);
  return __builtin_bit_cast(unsigned, r);
}
__device__ __forceinline__ float rbf(float f) { return bflo(pk_bf16(f, 0.f)); }
__device__ __forceinline__ int swap23(int r) { return (r & ~12) | ((r & 4) << 1) | ((r & 8) >> 1); }
__device__ __forceinline__ float dot2(unsigned a, unsigned b, float c) {
  return __builtin_amdgcn_fdot2_f32_bf16(__builtin_bit_cast(v2bf, a), __builtin_bit_cast(v2bf, b), c, false);
}
#define GLDS16(g, l) __builtin_amdgcn_global_load_lds((const GAS void*)(g), (LAS void*)(l), 16, 0, 0)
typedef int i32x4 __attribute__((ext_vector_type(4)));
__device__ __forceinline__ i32x4 mk_rsrc(const void* p) {
  const unsigned long long a = (unsigned long long)p;
  return (i32x4){(int)(unsigned)a, (int)((unsigned)(a >> 32) & 0xffffu), 0x7fffffff, 0x00020000};
}
__device__ __forceinline__ void dma16(i32x4 rsrc, unsigned lds_dst, int voff, int soff) {
  unsigned keep;
  asm volatile("s_mov_b32 %0, m0\n\ts_mov_b32 m0, %1\n\ts_nop 0\n\tbuffer_load_dwordx4 %2, %3, %4 offen lds\n\ts_mov_b32 m0, %0"
               : "=&s"(keep) : "s"(lds_dst), "v"(voff), "s"(rsrc), "s"(soff) : "memory");
}
__device__ __forceinline__ void touch4(i32x4 rsrc, unsigned lds_dst, int voff, int soff) {
  unsigned keep;
  asm volatile("s_mov_b32 %0, m0\n\ts_mov_b32 m0, %1\n\ts_nop 0\n\tbuffer_load_dword %2, %3, %4 offen lds\n\ts_mov_b32 m0, %0"
               : "=&s"(keep) : "s"(lds_dst), "v"(voff), "s"(rsrc), "s"(soff) : "memory");
}
template <int OFF> __device__ __forceinline__ void dma16x(i32x4 rsrc, unsigned lds_dst, int voff, int soff) {
  unsigned keep;
  asm volatile("s_mov_b32 %0, m0\n\ts_mov_b32 m0, %1\n\ts_nop 3\n\tbuffer_load_dwordx4 %2, %3, %4 offen offset:%5 lds\n\ts_mov_b32 m0, %0"
               : "=&s"(keep) : "s"(lds_dst), "v"(voff), "s"(rsrc), "s"(soff), "n"(OFF) : "memory");
}
template <int OFF> __device__ __forceinline__ void dma16x_sc1(i32x4 rsrc, unsigned lds_dst, int voff, int soff) {
  unsigned keep;
  asm volatile("s_mov_b32 %0, m0\n\ts_mov_b32 m0, %1\n\ts_nop 3\n\tbuffer_load_dwordx4 %2, %3, %4 offen offset:%5 sc1 lds\n\ts_mov_b32 m0, %0"
               : "=&s"(keep) : "s"(lds_dst), "v"(voff), "s"(rsrc), "s"(soff), "n"(OFF) : "memory");
}
template <int OFF> __device__ __forceinline__ void dma16x_nt(i32x4 rsrc, unsigned lds_dst, int voff, int soff) {
  unsigned keep;
  asm volatile("s_mov_b32 %0, m0\n\ts_mov_b32 m0, %1\n\ts_nop 3\n\tbuffer_load_dwordx4 %2, %3, %4 offen offset:%5 nt lds\n\ts_mov_b32 m0, %0"
               : "=&s"(keep) : "s"(lds_dst), "v"(voff), "s"(rsrc), "s"(soff), "n"(OFF) : "memory");
}
__device__ __forceinline__ void touch4_nt(i32x4 rsrc, unsigned lds_dst, int voff, int soff) {
  unsigned keep;
  asm volatile("s_mov_b32 %0, m0\n\ts_mov_b32 m0, %1\n\ts_nop 0\n\tbuffer_load_dword %2, %3, %4 offen nt lds\n\ts_mov_b32 m0, %0"
               : "=&s"(keep) : "s"(lds_dst), "v"(voff), "s"(rsrc), "s"(soff) : "memory");
}
#define WAIT_V(n) asm volatile("s_waitcnt vmcnt(%0)" ::"n"(n) : "memory")
#define WAIT_L0() asm volatile("s_waitcnt lgkmcnt(0)" ::: "memory")
#define BAR() __builtin_amdgcn_s_barrier()
#define ST_DECL
#define ST(k, i)
#define ST_FLUSH(k)

#define AUX_PF 2
#define PF_ON 0
#define PF_AT 3
#define AUX_ST 16
#define TOUCH 2
typedef float f32x2 __attribute__((ext_vector_type(2)));
constexpr int SA0 = 0, SA1 = 33280, SH1 = 98304, SLP = 131072, AROW = 1040;
constexpr int SCR = 66560;
constexpr int SC_PART = SCR;
constexpr int SC_HS   = SCR + 2048;
constexpr int SC_RS   = SCR + 3072;
constexpr int SC_MRS  = SCR + 3584;
constexpr int SC_CS   = SCR + 4096;
constexpr int SC_B1   = SCR + 7168;
constexpr int SC_DUM  = SCR + 8192;
#define PIN(x) asm volatile("" : "+v"(x))
#define SBAR() __builtin_amdgcn_sched_barrier(0)

template <int K> __device__ __forceinline__ void eh_op(const f32x16& ap, float (&v)[16], float (&t)[16], unsigned (&ow)[8], float scale, float center) {
  constexpr int st = K / 16, e = K % 16;
  if constexpr (st == 0) { v[e] = fmaf(-center, t[e], v[e]); PIN(v[e]); }
  else if constexpr (st == 1) { v[e] = fmaf(ap[e], scale, v[e]); PIN(v[e]); }
  else if constexpr (st == 2) { t[e] = v[e] * v[e]; PIN(t[e]); }
  else if constexpr (st == 3) { t[e] = fmaf(t[e], -0.1029432397f, -2.302208198f); PIN(t[e]); }
  else if constexpr (st == 4) { t[e] = t[e] * v[e]; PIN(t[e]); }
  else if constexpr (st == 5) { t[e] = __builtin_amdgcn_exp2f(t[e]); PIN(t[e]); }
  else if constexpr (st == 6) { t[e] = 1.f + t[e]; PIN(t[e]); }
  else if constexpr (st == 7) { t[e] = __builtin_amdgcn_rcpf(t[e]); PIN(t[e]); }
  else if constexpr (st == 8) { v[e] = v[e] * t[e]; PIN(v[e]); }
  else if constexpr (K < 152) { ow[e] = pk_bf16(v[2 * e], v[2 * e + 1]); PIN(ow[e]); }
}
template <int K> __device__ __forceinline__ void ea_op(const f32x16& ap, float (&v)[16], float (&t)[16], unsigned (&ow)[8], float cs) {
  constexpr int st = K / 16, e = K % 16;
  if constexpr (st == 0) { t[e] = ap[e] * t[e]; PIN(t[e]); }
  else if constexpr (st == 1) { t[e] = fmaf(-v[e], cs, t[e]); PIN(t[e]); }
  else if constexpr (K < 40) { ow[e] = pk_bf16(t[2 * e], t[2 * e + 1]); PIN(ow[e]); }
}
template <int ET, int K0, int K1> __device__ __forceinline__ void e_ops(const f32x16& ap, float (&v)[16], float (&t)[16], unsigned (&ow)[8], float s0, float s1) {
  if constexpr (K0 < K1) {
    if constexpr (ET == 1) eh_op<K0>(ap, v, t, ow, s0, s1); else if constexpr (ET == 2) ea_op<K0>(ap, v, t, ow, s0);
    e_ops<ET, K0 + 1, K1>(ap, v, t, ow, s0, s1);
  }
}
template <int MT> __device__ __forceinline__ bf16x8 wread(const char* pan, int ks) {
  if constexpr (MT == 1) { return *(const bf16x8*)(pan + ks * 32); }
  else {
    v4s t0 = __builtin_amdgcn_ds_read_tr16_b64_v4i16((LAS v4s*)(pan + ks * 1024));
    v4s t1 = __builtin_amdgcn_ds_read_tr16_b64_v4i16((LAS v4s*)(pan + ks * 1024 + 256));
    return (bf16x8)__builtin_shufflevector(t0, t1, 0, 1, 2, 3, 4, 5, 6, 7);
  }
}
__device__ __forceinline__ void row_stats(float sum, float sq, float& mean, float& inv, float& m_bf, float& scale, float& center) {
  mean = sum * (1.f / 512.f);
  const float var = fmaxf(sq * (1.f / 512.f) - mean * mean, 0.f);
  m_bf = rbf(mean);
  const float sd = rbf(sqrtf(rbf(rbf(var) + 1e-5f)));
  inv = 1.f / sd;
  const float m2 = rbf((mean - m_bf) * inv);
  const float v2 = rbf(var * inv * inv);
  const float sd2 = rbf(sqrtf(rbf(v2 + 1e-5f)));
  scale = inv / sd2; center = (m_bf + m2 * sd) * scale;
}
constexpr int WR = 8, WD = 3;
struct RowStat { float mean, var, m_bf, sd, inv, m2, v2, sd2, scale, center; };
template <int S> __device__ __forceinline__ void rs_stage(RowStat& r) {
  if constexpr (S == 0) { r.mean *= (1.f / 512.f); r.var = fmaxf(r.var * (1.f / 512.f) - r.mean * r.mean, 0.f); r.m_bf = rbf(r.mean); PIN(r.var); PIN(r.m_bf); }
  else if constexpr (S == 1) { r.sd = rbf(rbf(r.var) + 1e-5f); PIN(r.sd); }
  else if constexpr (S == 2) { r.sd = rbf(sqrtf(r.sd)); PIN(r.sd); }
  else if constexpr (S == 3) { r.inv = 1.f / r.sd; PIN(r.inv); }
  else if constexpr (S == 4) { r.m2 = rbf((r.mean - r.m_bf) * r.inv); r.v2 = rbf(rbf(r.var * r.inv * r.inv) + 1e-5f); PIN(r.m2); PIN(r.v2); }
  else if constexpr (S == 5) { r.sd2 = rbf(sqrtf(r.v2)); PIN(r.sd2); }
  else if constexpr (S == 6) { r.scale = r.inv / r.sd2; PIN(r.scale); }
  else if constexpr (S == 7) { r.center = (r.m_bf + r.m2 * r.sd) * r.scale; PIN(r.center); }
}
template <int MT, int ET, int NT, int G0, int KS, class Hook>
__device__ __forceinline__ void unit_steps(const char* pan, const char* pnx, const bf16x8 (&xf)[32], bf16x8 (&wf)[WR], f32x16& aw, const f32x16& ar,
                                           float (&v)[16], float (&t)[16], unsigned (&ow)[8], const float& s0, const float& s1, Hook&& hook) {
  if constexpr (KS < 32) {
    constexpr int NOPS = ET == 1 ? 152 : 40;
    if constexpr (KS == 0) {
      if constexpr (MT == 1) aw = __builtin_amdgcn_mfma_f32_32x32x16_bf16(xf[KS], wf[KS % WR], (f32x16){}, 0, 0, 0);
      else aw = __builtin_amdgcn_mfma_f32_32x32x16_bf16(wf[KS % WR], xf[KS], (f32x16){}, 0, 0, 0);
    } else {
      if constexpr (MT == 1) aw = __builtin_amdgcn_mfma_f32_32x32x16_bf16(xf[KS], wf[KS % WR], aw, 0, 0, 0);
      else aw = __builtin_amdgcn_mfma_f32_32x32x16_bf16(wf[KS % WR], xf[KS], aw, 0, 0, 0);
    }
    PIN(aw);
    SBAR();
    if constexpr (KS + WD < 32) wf[(KS + WD) % WR] = wread<MT>(pan, KS + WD);
    else if constexpr (NT >= 0) wf[(KS + WD) % WR] = wread<NT>(pnx, KS + WD - 32);
    asm volatile("" :: "v"(wf[KS % WR]), "v"(wf[(KS + WR - 1) % WR]));
    SBAR();
    hook(std::integral_constant<int, KS>{});
    constexpr int k0 = KS < G0 ? 0 : (KS - G0) * NOPS / (32 - G0), k1 = KS < G0 ? 0 : (KS + 1 - G0) * NOPS / (32 - G0);
    e_ops<ET, k0, k1>(ar, v, t, ow, s0, s1);
    SBAR();
    unit_steps<MT, ET, NT, G0, KS + 1>(pan, pnx, xf, wf, aw, ar, v, t, ow, s0, s1, hook);
  }
}

typedef __attribute__((address_space(1))) unsigned long long gu64;
__device__ __forceinline__ unsigned ring_off(int t) { return (t & 1) * 32768 + (t & 2) * 49152; }
struct Loader {
  i32x4 ra_s, rb_s, ra_o, rb_o; int aoff[4], voff; unsigned ldsw;
  __device__ __forceinline__ void init(const u16* cre, const u16* cim, char* ws, char* lds, int lw, int lane, int c, int rt, int j) {
#pragma unroll
    for (int i = 0; i < 4; ++i) { const int row = lw * 32 + i * 8 + (lane >> 3); aoff[i] = row * 4096 + (((lane & 7) ^ ((row >> 1) & 7)) * 16) - i * 1024 + 4096; }
    ldsw = (unsigned)(size_t)(LAS char*)lds + lw * 4096;
    ra_s = mk_rsrc((const char*)(c < 2 ? cre : cim) + (size_t)rt * 128 * 4096 + (c & 1) * 2048 - 4096);
    rb_s = mk_rsrc(ws + WS_B + ((size_t)(j * NKK_B + c * 16) << 14) + lw * 4096);
    ra_o = mk_rsrc(ws + WS_H + ((size_t)((rt * 4 + 2 * (lw >> 1) + (c >> 1)) * NSUB) << 10) + (lw & 1) * 4096 - (16 << 13));
    rb_o = mk_rsrc(ws + WS_W2 + ((size_t)((j * 4 + 2 * (lw >> 1) + (c & 1)) * NSUB) << 10) + (lw & 1) * 4096 - (16 << 13));
    voff = lane * 16;
  }
  __device__ __forceinline__ void issue_a(int T) const {
    const unsigned dst = __builtin_amdgcn_readfirstlane(ldsw + ring_off(T));
    if (T < 16) { const int so = __builtin_amdgcn_readfirstlane(T * 128);
                  dma16x_nt<0>(ra_s, dst, aoff[0], so); dma16x_nt<1024>(ra_s, dst, aoff[1], so); dma16x_nt<2048>(ra_s, dst, aoff[2], so); dma16x_nt<3072>(ra_s, dst, aoff[3], so); }
    else { i32x4 r = ra_o; r.z = __builtin_amdgcn_readfirstlane(T < 27 ? 0x7fffffff : 0); const int so = __builtin_amdgcn_readfirstlane(T << 13);
           dma16x_sc1<0>(r, dst, voff, so); dma16x_sc1<1024>(r, dst, voff, so); dma16x_sc1<2048>(r, dst, voff, so); dma16x_sc1<3072>(r, dst, voff, so); }
  }
  __device__ __forceinline__ void issue_b(int T) const {
    const unsigned dst = __builtin_amdgcn_readfirstlane(ldsw + ring_off(T) + 16384);
    i32x4 r = T < 16 ? rb_s : rb_o; r.z = T < 27 ? 0x7fffffff : 0;
    const int so = __builtin_amdgcn_readfirstlane(T < 16 ? T << 14 : T << 13);
    dma16x_sc1<0>(r, dst, voff, so); dma16x_sc1<1024>(r, dst, voff, so); dma16x_sc1<2048>(r, dst, voff, so); dma16x_sc1<3072>(r, dst, voff, so);
  }
};

__device__ __forceinline__ void phase1(const u16* __restrict__ x, const u16* __restrict__ wre, const u16* __restrict__ wim,
                                       const u16* __restrict__ w1, const u16* __restrict__ b1, const u16* __restrict__ w2,
                                       char* __restrict__ ws, char* lds, const Loader& ld, const unsigned long long tag) {
  const int tid = threadIdx.x, lane = tid & 63, wid = __builtin_amdgcn_readfirstlane(tid >> 6);
  const int b = blockIdx.x, xcd = b & 7, idx = b >> 3;
  const int rt = (xcd >> 1) * 4 + (idx & 3);
  const int cg = (xcd & 1) * 8 + (idx >> 2);
  const int h0 = cg < 11 ? cg * 3 : 33 + (cg - 11) * 2;
  const bool h3 = cg < 11;
  const int a0 = cg * 2;
  const int m = wid & 3;
  const bool helper = wid >= 4;
  const int r31 = lane & 31, hh = lane >> 5;
  const unsigned lds0 = (unsigned)(size_t)(LAS char*)lds;
  const int prot = idx & 3;
  ST_DECL;
  ST(0, 0);

  const int hoff = (lane >> 2) * (F_ * 2) + (lane & 3) * 16;
  const i32x4 rw1 = mk_rsrc(w1);
  auto piece_h = [&](int u, int q, int slot_off) { dma16(rw1, lds0 + slot_off + q * 1024, hoff, q * 16 * (F_ * 2) + u * 64); };
  const i32x4 rx = mk_rsrc(x + (size_t)(rt * 128 + m * 32) * D_);
  int xo[4];
#pragma unroll
  for (int i = 0; i < 4; ++i) { const int r = i * 4 + (lane >> 4); xo[i] = r * 1024 + (((lane & 15) ^ r) * 16); }
  auto quarter = [&](int q) {
#pragma unroll
    for (int i = 0; i < 8; ++i) dma16(rx, lds0 + q * 32768 + m * 8192 + i * 1024, xo[i & 3], q * 256 + (i >> 2) * 16384);
    piece_h(h0, q * 8 + m * 2, SLP); piece_h(h0, q * 8 + m * 2 + 1, SLP);
  };
  if (helper) { quarter(0); quarter(1); }

  const auto rws = __builtin_amdgcn_make_buffer_rsrc((void*)ws, 0, 0x7fffffff, 0x00020000);
  const int xrow = m * 32 + swap23(r31);
  const unsigned xbase = xrow * 256 + ((hh ^ (xrow & 15)) * 16);
#define QWAIT(q) WAIT_V(9 + 10 * (3 - (q)))

  if (helper) {
    const auto rw2t = __builtin_amdgcn_make_buffer_rsrc((void*)(ws + WS_W2), 0, 16 * NSUB * 1024, 0x00020000);
    const i32x4 rwa = mk_rsrc((a0 < 16 ? wre : wim) + (size_t)(a0 & 15) * 32 * D_);
    auto panel_h = [&](int u, int slot_off) {
#pragma unroll
      for (int k = 0; k < 8; ++k) piece_h(u, m * 8 + ((k + 2 * prot) & 7), slot_off);
    };
    auto panel_a = [&](int w, int slot_off) {
#pragma unroll
      for (int k = 0; k < 8; ++k) { const int q = m * 8 + ((k + 2 * prot) & 7); dma16(rwa, lds0 + slot_off + q * AROW, lane * 16, (w * 32 + q) * 1024); }
    };
    const v4s ones = {0x3f80, 0x3f80, 0x3f80, 0x3f80};
    const char* pzh = lds + ((lane >> 2) & 15) * 64 + (m & 1) * 32 + (lane & 3) * 8;
    auto cs_h_part = [&](f32x4 (&c)[4], int slot_off, int j0, int n) {
#pragma unroll
      for (int j = 0; j < n; ++j)
        c[j & 3] = __builtin_amdgcn_mfma_f32_4x4x4bf16_1k(ones, __builtin_amdgcn_ds_read_tr16_b64_v4i16((LAS v4s*)(pzh + slot_off + (j0 + j) * 1024)), c[j & 3], 0, 0, 0);
    };
    auto cs_h_fin = [&](f32x4 (&c)[4], int i) {
      float r = (c[0][0] + c[1][0]) + (c[2][0] + c[3][0]);
      r += __shfl_xor(r, 16); r += __shfl_xor(r, 32);
      if (lane < 16) *(float*)(lds + SC_CS + i * 512 + (m & 1) * 64 + lane * 4) = r;
    };
    auto cs_h = [&](int i, int slot_off) {
      if ((m >> 1) == (i & 1)) { f32x4 c[4] = {}; cs_h_part(c, slot_off, 0, 32); cs_h_fin(c, i); }
    };
    auto cs_a = [&](int i, int slot_off) {
      const char* pz = lds + slot_off + swap23(r31) * AROW + hh * 16 + m * 256;
      f32x4 c0 = {}, c1 = {};
#pragma unroll
      for (int j = 0; j < 8; ++j) {
        const bf16x8 w = *(const bf16x8*)(pz + j * 32);
        c0 = __builtin_amdgcn_mfma_f32_4x4x4bf16_1k(ones, __builtin_shufflevector(w, w, 0, 1, 2, 3), c0, 0, 0, 0);
        c1 = __builtin_amdgcn_mfma_f32_4x4x4bf16_1k(ones, __builtin_shufflevector(w, w, 4, 5, 6, 7), c1, 0, 0, 0);
      }
      float r = c0[0] + c1[0]; r += __shfl_xor(r, 32);
      if (lane < 32) *(float*)(lds + SC_CS + i * 512 + m * 128 + lane * 4) = r;
    };
    __builtin_amdgcn_s_setprio(2);
    {
      f32x4 c[4] = {};
      WAIT_V(10); ST(0, 1); BAR();
      quarter(2);
      if (m < 2) cs_h_part(c, SLP, 0, 8);
      WAIT_V(10); BAR();
      quarter(3);
      if (m < 2) cs_h_part(c, SLP, 8, 8);
      WAIT_V(10); BAR();
      panel_a(0, SA0);
      unsigned w2v[2][8], b1v;
      int w2dst[2];
      {
        i32x4 rw2 = mk_rsrc(w2); rw2.z = F_ * D_ * 2;
#pragma unroll
        for (int r = 0; r < 2; ++r) {
          const int hl = r * 256 + (tid - 256), cc = b * 352 + hl;
          const int ln = cc & 63, rest = cc >> 6, ct = rest / NSUB, sub = rest - ct * NSUB;
          const int off = hl < 352 ? ((16 * sub + 4 * (ln >> 5)) * D_ + 32 * ct + swap23(ln & 31)) * 2 : 0x7ff00000;
          w2dst[r] = hl < 352 ? cc * 16 : 0x7ffffff0;
          asm volatile("buffer_load_ushort %0, %8, %10, 0 offen\n\tbuffer_load_ushort %1, %8, %10, 0 offen offset:1024\n\t"
                       "buffer_load_ushort %2, %8, %10, 0 offen offset:2048\n\tbuffer_load_ushort %3, %8, %10, 0 offen offset:3072\n\t"
                       "buffer_load_ushort %4, %9, %10, 0 offen\n\tbuffer_load_ushort %5, %9, %10, 0 offen offset:1024\n\t"
                       "buffer_load_ushort %6, %9, %10, 0 offen offset:2048\n\tbuffer_load_ushort %7, %9, %10, 0 offen offset:3072"
                       : "=&v"(w2v[r][0]), "=&v"(w2v[r][1]), "=&v"(w2v[r][2]), "=&v"(w2v[r][3]), "=&v"(w2v[r][4]), "=&v"(w2v[r][5]), "=&v"(w2v[r][6]), "=&v"(w2v[r][7])
                       : "v"(off), "v"(off + 8192), "s"(rw2) : "memory");
        }
        const int bo = h0 * 32 + lane * 2;
        asm volatile("buffer_load_dword %0, %1, %2, 0 offen" : "=&v"(b1v) : "v"((bo < F_ - 2 ? bo : F_ - 2) * 2), "s"(mk_rsrc(b1)) : "memory");
      }
      if (m < 2) cs_h_part(c, SLP, 16, 8);
      WAIT_V(25); BAR();
      ST(0, 2);
      panel_a(1, SA1);
      if (m < 2) { cs_h_part(c, SLP, 24, 8); cs_h_fin(c, 0); }
      WAIT_V(8);
      asm volatile("" : "+v"(w2v[0][0]), "+v"(w2v[0][1]), "+v"(w2v[0][2]), "+v"(w2v[0][3]), "+v"(w2v[0][4]), "+v"(w2v[0][5]), "+v"(w2v[0][6]), "+v"(w2v[0][7]), "+v"(b1v));
      asm volatile("" : "+v"(w2v[1][0]), "+v"(w2v[1][1]), "+v"(w2v[1][2]), "+v"(w2v[1][3]), "+v"(w2v[1][4]), "+v"(w2v[1][5]), "+v"(w2v[1][6]), "+v"(w2v[1][7]));
      if (m == 3 && lane < 48) *(f32x2*)(lds + SC_B1 + lane * 8) = (f32x2){bflo(b1v), bfhi(b1v)};
#pragma unroll
      for (int r = 0; r < 2; ++r) {
        const u32x4 w = {w2v[r][0] | (w2v[r][1] << 16), w2v[r][2] | (w2v[r][3] << 16), w2v[r][4] | (w2v[r][5] << 16), w2v[r][6] | (w2v[r][7] << 16)};
        __builtin_amdgcn_raw_buffer_store_b128(w, rw2t, w2dst[r], 0, AUX_ST);
      }
    }
    ST(0, 3);
    WAIT_V(2); WAIT_L0(); BAR();
    panel_h(h0 + 1, SH1);
    panel_h(h3 ? h0 + 2 : h0, SLP);
#define CT 1
    {
      const int j2 = idx & 3;
      const int st1 = 3 + 2 * m + hh, st2 = st1 + 8 < 15 ? st1 + 8 : 15;
      const int vb = (j2 * 32 + r31) * 4096 + 4096;
      if (CT == 2) { touch4_nt(ld.ra_s, lds0 + SC_DUM + m * 256, vb + st1 * 128, 0); touch4_nt(ld.ra_s, lds0 + SC_DUM + m * 256, vb + st2 * 128, 0); }
      else { touch4(ld.ra_s, lds0 + SC_DUM + m * 256, vb + st1 * 128, 0); }
    }
    cs_a(1, SA0);
    {
      float mean, inv, m_bf, sc_, ce_;
      const f32x2 pr = *(const f32x2*)(lds + SC_PART + (m * 32 + r31) * 8);
      row_stats(pr.x, pr.y, mean, inv, m_bf, sc_, ce_);
      const float sg = a0 < 16 ? inv : -inv;
      if (lane < 32) { *(float*)(lds + SC_RS + (m * 32 + lane) * 4) = sg; *(float*)(lds + SC_MRS + (m * 32 + lane) * 4) = m_bf * sg; }
    }
    WAIT_V(9); WAIT_L0(); BAR();
    cs_a(2, SA1);
    WAIT_V(1);
    {
      const int j2 = idx & 3;
      const int st1 = 3 + 2 * m + hh, st2 = st1 + 8 < 15 ? st1 + 8 : 15;
      const int vb = (j2 * 32 + r31) * 4096 + 4096;
      if (CT == 2) { touch4_nt(ld.ra_s, lds0 + SC_DUM + m * 256, vb + st1 * 128, 0); touch4_nt(ld.ra_s, lds0 + SC_DUM + m * 256, vb + st2 * 128, 0); }
      else { touch4(ld.ra_s, lds0 + SC_DUM + m * 256, vb + st2 * 128, 0); }
    }
    WAIT_L0(); BAR();
    cs_h(3, SH1);
    ld.issue_a(0); ld.issue_a(1);
    WAIT_L0(); BAR();
    cs_h(4, SLP);
    ld.issue_a(2);
    WAIT_L0(); BAR();
    if (tid == 256) __hip_atomic_store((gu64*)(ws + WS_CNT) + ((cg >> 1) * 2 + (rt >> 3)) * 16 + (rt & 7) * 2 + (cg & 1), tag, __ATOMIC_RELAXED, __HIP_MEMORY_SCOPE_AGENT);
    BAR();
    __builtin_amdgcn_s_setprio(0);
    ST(0, 5); ST_FLUSH(0);
    return;
  }

  bf16x8 xf[32];
  unsigned long long clk0 = 0; (void)clk0;
  const int wrow = swap23(r31);
  const unsigned wlA = (unsigned)(wrow * AROW + hh * 16);
  const unsigned wlH = (unsigned)((8 * hh + ((lane & 15) >> 2)) * 64 + ((lane >> 4) & 1) * 32 + (lane & 3) * 8);
  f32x16 acc, ac2;
  bf16x8 wf[WR];
  float v[16], t[16]; unsigned ow[8];
  auto nohook = [](auto) {};
  RowStat rs;
  const int hstore = (swap23(r31) + 32 * hh) * 16;
  const int s0r = rt * 128 + m * 32;

  auto tables_h = [&](int i, int hu) {
#pragma unroll
    for (int g4 = 0; g4 < 4; ++g4) {
      const f32x4 c = *(const f32x4*)(lds + SC_CS + i * 512 + (8 * g4 + 4 * hh) * 4), bq = *(const f32x4*)(lds + SC_B1 + hu * 128 + (8 * g4 + 4 * hh) * 4);
#pragma unroll
      for (int e = 0; e < 4; ++e) { t[g4 * 4 + e] = c[e]; v[g4 * 4 + e] = bq[e]; }
    }
  };
  auto tables_a = [&](int i) -> float {
#pragma unroll
    for (int g4 = 0; g4 < 4; ++g4) {
      const f32x4 c = *(const f32x4*)(lds + SC_RS + (m * 32 + 8 * g4 + 4 * hh) * 4), bq = *(const f32x4*)(lds + SC_MRS + (m * 32 + 8 * g4 + 4 * hh) * 4);
#pragma unroll
      for (int e = 0; e < 4; ++e) { t[g4 * 4 + e] = c[e]; v[g4 * 4 + e] = bq[e]; }
    }
    const char* cq = lds + SC_CS + i * 512 + r31 * 4;
    return (*(const float*)cq + *(const float*)(cq + 128)) + (*(const float*)(cq + 256) + *(const float*)(cq + 384));
  };
  auto tables_a4 = [&](int i, float (&cp)[4]) {
#pragma unroll
    for (int g4 = 0; g4 < 4; ++g4) {
      const f32x4 c = *(const f32x4*)(lds + SC_RS + (m * 32 + 8 * g4 + 4 * hh) * 4), bq = *(const f32x4*)(lds + SC_MRS + (m * 32 + 8 * g4 + 4 * hh) * 4);
#pragma unroll
      for (int e = 0; e < 4; ++e) { t[g4 * 4 + e] = c[e]; v[g4 * 4 + e] = bq[e]; }
      cp[g4] = *(const float*)(lds + SC_CS + i * 512 + g4 * 128 + r31 * 4);
    }
  };
  auto store_h = [&](int hu, const unsigned (&ow)[8]) {
    const int dst = (int)WS_H + (((rt * 4 + m) * NSUB + 2 * (h0 + hu)) << 10);
    if (hu < 2 || h3) {
      __builtin_amdgcn_raw_buffer_store_b128((u32x4){ow[0], ow[1], ow[2], ow[3]}, rws, hstore, dst, AUX_ST);
      __builtin_amdgcn_raw_buffer_store_b128((u32x4){ow[4], ow[5], ow[6], ow[7]}, rws, hstore + 1024, dst, AUX_ST);
    }
    if (h0 + hu == 42 && (hu < 2 || h3)) { u32x4 z = {0u, 0u, 0u, 0u}; __builtin_amdgcn_raw_buffer_store_b128(z, rws, lane * 16 + 2048, dst, AUX_ST); __builtin_amdgcn_raw_buffer_store_b128(z, rws, lane * 16 + 3072, dst, AUX_ST); }
  };
  auto store_a = [&](int w, const unsigned (&ow)[8]) {
    const int u = a0 + w, et = u & 15, j = et >> 2, nt = et & 3;
    const int kk = (u >> 4) * 32 + (s0r >> 6), sub = (s0r & 63) >> 4;
    const int dst = (int)WS_B + (((j * NKK_B + kk) * 16 + nt * 4 + sub) << 10);
    __builtin_amdgcn_raw_buffer_store_b128((u32x4){ow[0], ow[1], ow[2], ow[3]}, rws, lane * 16, dst, AUX_ST);
    __builtin_amdgcn_raw_buffer_store_b128((u32x4){ow[4], ow[5], ow[6], ow[7]}, rws, lane * 16 + 1024, dst, AUX_ST);
  };
  {
    f32x4 ss0 = {}, ss1 = {}, sq0 = {}, sq1 = {};
    const v4s ones4 = {0x3f80, 0x3f80, 0x3f80, 0x3f80};
    acc = (f32x16){};
    const char* pan = lds + SLP + wlH;
#pragma unroll
    for (int q = 0; q < 4; ++q) {
      if (q == 0) ST(0, 1);
      BAR();
      if (q == 3) ST(0, 2);
      bf16x8 w8[8];
#pragma unroll
      for (int k = 0; k < 8; ++k) xf[q * 8 + k] = *(const bf16x8*)(lds + q * 32768 + (xbase ^ (k * 32)));
#pragma unroll
      for (int k = 0; k < 8; ++k) w8[k] = wread<0>(pan, q * 8 + k);
      SBAR();
#pragma unroll
      for (int k = 0; k < 8; ++k) {
        const v4s lo4 = __builtin_shufflevector(xf[q * 8 + k], xf[q * 8 + k], 0, 1, 2, 3), hi4 = __builtin_shufflevector(xf[q * 8 + k], xf[q * 8 + k], 4, 5, 6, 7);
        ss0 = __builtin_amdgcn_mfma_f32_4x4x4bf16_1k(ones4, lo4, ss0, 0, 0, 0); sq0 = __builtin_amdgcn_mfma_f32_4x4x4bf16_1k(lo4, lo4, sq0, 0, 0, 0);
        ss1 = __builtin_amdgcn_mfma_f32_4x4x4bf16_1k(ones4, hi4, ss1, 0, 0, 0); sq1 = __builtin_amdgcn_mfma_f32_4x4x4bf16_1k(hi4, hi4, sq1, 0, 0, 0);
      }
      asm volatile("" : "+v"(ss0), "+v"(ss1), "+v"(sq0), "+v"(sq1));
      SBAR();
#pragma unroll
      for (int k = 0; k < 8; ++k) { acc = __builtin_amdgcn_mfma_f32_32x32x16_bf16(w8[k], xf[q * 8 + k], acc, 0, 0, 0); PIN(acc); }
      WAIT_L0(); SBAR();
    }
    const f32x4 sqv = sq0 + sq1;
    float sum = ss0[0] + ss1[0], sq = (lane & 2) ? ((lane & 1) ? sqv[3] : sqv[2]) : ((lane & 1) ? sqv[1] : sqv[0]);
    sum += __shfl_xor(sum, 32); sq += __shfl_xor(sq, 32);
    if (lane < 32) *(f32x2*)(lds + SC_PART + (m * 32 + lane) * 8) = (f32x2){sum, sq};
    rs.mean = sum; rs.var = sq;
  }
  ST(0, 8);
  WAIT_L0(); BAR();
  ST(0, 4);
  const char* pA0 = lds + SA0 + wlA; const char* pA1 = lds + SA1 + wlA;
  const char* pH1 = lds + SH1 + wlH; const char* pH2 = lds + SLP + wlH;
#pragma unroll
  for (int e = 0; e < WD; ++e) wf[e] = wread<1>(pA0, e);
  wf[WR - 1] = wf[0];
  tables_h(0, 0);
  float csa = 0.f, cp[4];
  auto cs_hook = [&](auto g) { if constexpr (decltype(g)::value == 2) { csa = (cp[0] + cp[1]); PIN(csa); float c2 = cp[2] + cp[3]; PIN(c2); csa += c2; PIN(csa); } };
  unit_steps<1, 1, 1, 8, 0>(pA0, pA1, xf, wf, ac2, acc, v, t, ow, rs.scale, rs.center, [&](auto g) {
    constexpr int G = decltype(g)::value;
    if constexpr (G < 8) rs_stage<G>(rs);
  });
  store_h(0, ow);
  ST(0, 9);
  BAR();
  ST(0, 6);
  tables_a4(1, cp);
  unit_steps<1, 2, 0, 3, 0>(pA1, pH1, xf, wf, acc, ac2, v, t, ow, csa, csa, cs_hook); store_a(0, ow);
  ST(0, 10);
  BAR();
  ST(0, 7);
  tables_a4(2, cp);
  unit_steps<0, 2, 0, 3, 0>(pH1, pH2, xf, wf, ac2, acc, v, t, ow, csa, csa, cs_hook); store_a(1, ow);
  ST(0, 11);
  BAR();
  ST(0, 13);
  tables_h(3, 1);
  unit_steps<0, 1, -1, 3, 0>(pH2, pH2, xf, wf, acc, ac2, v, t, ow, rs.scale, rs.center, [&](auto g) {
    if constexpr (decltype(g)::value == 14) { WAIT_V(0); BAR(); }
  });
  store_h(1, ow);
  ST(0, 12);
  BAR();
  tables_h(4, 2);
  {
#pragma unroll
    for (int e = 0; e < 8; ++e) {
      f32x2 vv = (f32x2){v[2 * e], v[2 * e + 1]} - (f32x2){t[2 * e], t[2 * e + 1]} * rs.center;
      vv = (f32x2){acc[2 * e], acc[2 * e + 1]} * rs.scale + vv;
      const f32x2 tt = ((vv * vv) * (-0.1029432397f) + (-2.302208198f)) * vv;
      const f32x2 d = (f32x2){__builtin_amdgcn_exp2f(tt.x), __builtin_amdgcn_exp2f(tt.y)} + 1.f;
      vv = vv * (f32x2){__builtin_amdgcn_rcpf(d.x), __builtin_amdgcn_rcpf(d.y)};
      ow[e] = pk_bf16(vv.x, vv.y);
    }
  }
  store_h(2, ow);
  WAIT_V(0);
  ST(0, 5); ST_FLUSH(0);
}

#define AUX_C 2
__device__ __forceinline__ void phase2(const u16* __restrict__ x, const u16* __restrict__ cre, const u16* __restrict__ cim,
                                       const u16* __restrict__ b2, char* __restrict__ ws, u16* __restrict__ out, char* lds, const Loader& ld, const unsigned long long tag) {
  const int tid = threadIdx.x, lane = tid & 63, wid = __builtin_amdgcn_readfirstlane(tid >> 6);
  const int b = blockIdx.x, xcd = b & 7, idx = b >> 3;
#define MAP2 0
  const int c = MAP2 ? (xcd >> 2) * 2 + (idx >> 4) : xcd >> 1, rt = MAP2 ? (xcd & 3) * 4 + ((idx >> 2) & 3) : (xcd & 1) * 8 + (idx >> 2), j = idx & 3;
  ST_DECL;
  ST(1, 0);

#define SB() __builtin_amdgcn_sched_barrier(0)

  if (wid >= 4) {
    gu64* f16 = (gu64*)(ws + WS_CNT) + ((4 * (c >> 1) + j) * 2 + (c & 1)) * 16 + (lane & 15);
    for (unsigned spins = 0;; ++spins) {
      const unsigned long long f = __hip_atomic_load(f16, __ATOMIC_RELAXED, __HIP_MEMORY_SCOPE_AGENT);
      if (__all(f == tag) || spins > (1u << 20)) break;
      __builtin_amdgcn_s_sleep(1);
    }
    asm volatile("" ::: "memory");
    ld.issue_b(0); ld.issue_b(1); ld.issue_b(2);
    for (int u = 0; u < 27; ++u) {
      if (u == 0) WAIT_V(8); else if (u == 1) WAIT_V(12); else WAIT_V(16);
      BAR();
      ld.issue_a(u + 3); ld.issue_b(u + 3);
    }
    WAIT_V(0);
    return;
  }

  const int r31 = lane & 31, hh = lane >> 5;
  const int wm = wid >> 1, wn = wid & 1;
  unsigned ab[4];
#pragma unroll
  for (int s = 0; s < 4; ++s) ab[s] = wm * 8192 + (unsigned)(r31 * 128 + (((2 * s + hh) ^ ((r31 >> 1) & 7)) * 16));
  const unsigned bb = 16384 + wn * 8192 + lane * 16;
  const unsigned ao = wm * 8192 + lane * 16;

  f32x16 acc[2][2] = {};
  f32x16 acco = {};
  bf16x8 F0[16], F1[16];
#define RD(F, s) do { F[4 * (s) + 0] = *(const bf16x8*)(sl + ab[s]); F[4 * (s) + 1] = *(const bf16x8*)(sl + ab[s] + 4096); \
                      F[4 * (s) + 2] = *(const bf16x8*)(sl + bb + (s) * 1024); F[4 * (s) + 3] = *(const bf16x8*)(sl + bb + (s) * 1024 + 4096); } while (0)
#define RDO(F, s) do { F[4 * (s) + 0] = *(const bf16x8*)(sl + ao + (s) * 2048); F[4 * (s) + 1] = *(const bf16x8*)(sl + bb + (s) * 2048); \
                       F[4 * (s) + 2] = *(const bf16x8*)(sl + ao + (s) * 2048 + 1024); F[4 * (s) + 3] = *(const bf16x8*)(sl + bb + (s) * 2048 + 1024); } while (0)
#define MM0(F, s) acc[0][0] = __builtin_amdgcn_mfma_f32_32x32x16_bf16(F[4 * (s) + 2], F[4 * (s) + 0], acc[0][0], 0, 0, 0)
#define MM1(F, s) acc[0][1] = __builtin_amdgcn_mfma_f32_32x32x16_bf16(F[4 * (s) + 3], F[4 * (s) + 0], acc[0][1], 0, 0, 0)
#define MM2(F, s) acc[1][0] = __builtin_amdgcn_mfma_f32_32x32x16_bf16(F[4 * (s) + 2], F[4 * (s) + 1], acc[1][0], 0, 0, 0)
#define MM3(F, s) acc[1][1] = __builtin_amdgcn_mfma_f32_32x32x16_bf16(F[4 * (s) + 3], F[4 * (s) + 1], acc[1][1], 0, 0, 0)
#define MO(F, s) do { acco = __builtin_amdgcn_mfma_f32_32x32x16_bf16(F[4 * (s) + 1], F[4 * (s) + 0], acco, 0, 0, 0); \
                      acco = __builtin_amdgcn_mfma_f32_32x32x16_bf16(F[4 * (s) + 3], F[4 * (s) + 2], acco, 0, 0, 0); } while (0)
#define PERIOD(u, RDX, FR, FM) do { \
    const char* sl = lds + ring_off(u); \
    BAR(); SB(); \
    MM0(FM, 0); SB(); RDX(FR, 0); SB(); MM1(FM, 0); SB(); MM2(FM, 0); SB(); RDX(FR, 1); SB(); MM3(FM, 0); SB(); \
    MM0(FM, 1); SB(); RDX(FR, 2); SB(); MM1(FM, 1); SB(); MM2(FM, 1); SB(); RDX(FR, 3); SB(); MM3(FM, 1); SB(); \
    MM0(FM, 2); MM1(FM, 2); MM2(FM, 2); MM3(FM, 2); \
    MM0(FM, 3); MM1(FM, 3); MM2(FM, 3); SB(); WAIT_L0(); SB(); MM3(FM, 3); SB(); \
  } while (0)
#define PERIODO(u, FR, FM) do { \
    const char* sl = lds + ring_off(u); \
    BAR(); SB(); \
    RDO(FR, 0); SB(); MO(FM, 0); SB(); RDO(FR, 1); SB(); MO(FM, 1); SB(); RDO(FR, 2); SB(); MO(FM, 2); SB(); RDO(FR, 3); SB(); MO(FM, 3); SB(); \
    WAIT_L0(); SB(); \
  } while (0)
  gu64* fl = (gu64*)(ws + WS_CNT + 2048);
  {
    const char* sl = lds;
    BAR(); SB(); ST(1, 1);
    if (tid == 0) __hip_atomic_store(fl + blockIdx.x, tag, __ATOMIC_RELAXED, __HIP_MEMORY_SCOPE_AGENT);

    RD(F0, 0); RD(F0, 1); RD(F0, 2); RD(F0, 3); SB();
    WAIT_L0(); SB();
  }
  for (int u = 1; u < 15; u += 2) {
    PERIOD(u, RD, F1, F0);
    PERIOD(u + 1, RD, F0, F1);
    if (u == 11 && wid == 0) {
      for (unsigned spins = 0;; ++spins) {
        const unsigned long long f0 = __hip_atomic_load(fl + lane * 4, __ATOMIC_RELAXED, __HIP_MEMORY_SCOPE_AGENT), f1 = __hip_atomic_load(fl + lane * 4 + 1, __ATOMIC_RELAXED, __HIP_MEMORY_SCOPE_AGENT),
                                 f2 = __hip_atomic_load(fl + lane * 4 + 2, __ATOMIC_RELAXED, __HIP_MEMORY_SCOPE_AGENT), f3 = __hip_atomic_load(fl + lane * 4 + 3, __ATOMIC_RELAXED, __HIP_MEMORY_SCOPE_AGENT);
        if (__all(f0 == tag && f1 == tag && f2 == tag && f3 == tag) || spins > (1u << 20)) break;
        __builtin_amdgcn_s_sleep(1);
      }
      asm volatile("" ::: "memory");
    }
  }
  PERIOD(15, RD, F1, F0);
  PERIOD(16, RDO, F0, F1);
  ST(1, 2);
  const int tile = rt * 4 + j;
  const auto rs = __builtin_amdgcn_make_buffer_rsrc(ws + WS_SLAB + ((size_t)tile << 17), 0, (ABL & 16) ? 0 : (1 << 17), 0x00020000);
  const unsigned lo = wid * 2048 + lane * 16;
#pragma unroll
  for (int q = 0; q < 4; ++q) {
    if (q != c) {
      const f32x16 v = acc[q >> 1][q & 1];
#pragma unroll
      for (int hf = 0; hf < 2; ++hf) {
        const u32x4 w = {pk_bf16(v[hf * 8], v[hf * 8 + 1]), pk_bf16(v[hf * 8 + 2], v[hf * 8 + 3]), pk_bf16(v[hf * 8 + 4], v[hf * 8 + 5]), pk_bf16(v[hf * 8 + 6], v[hf * 8 + 7])};
        __builtin_amdgcn_raw_buffer_store_b128(w, rs, (q * 4 + c) * 8192 + lo + hf * 1024, 0, 16);
      }
    }
  }
  const f32x16 own = c == 0 ? acc[0][0] : (c == 1 ? acc[0][1] : (c == 2 ? acc[1][0] : acc[1][1]));
  const int trow = rt * 128 + wm * 64 + (c >> 1) * 32 + r31;
  const int ecol = j * 128 + wn * 64 + (c & 1) * 32 + 8 * hh;
  u32x4 xv[2], bv[2];
#pragma unroll
  for (int half = 0; half < 2; ++half) { xv[half] = *(const u32x4*)(x + (size_t)trow * D_ + ecol + 16 * half); bv[half] = *(const u32x4*)(b2 + ecol + 16 * half); }
  SB();
  gu64* xfl = (gu64*)(ws + WS_CNT + 4096 + tile * 128);
  u32x4 pv[4][2];
#pragma unroll
  for (int s = 0; s < 4; ++s) { pv[s][0] = (u32x4){}; pv[s][1] = (u32x4){}; }
#pragma unroll 1
  for (int u = 17; u < 27; u += 2) {
    PERIODO(u, F1, F0);
    if (u == 19) { WAIT_V(4); ST(1, 3); }
    if (u == 23) {
      asm volatile("" ::: "memory");
#pragma unroll
      for (int s = 0; s < 4; ++s)
#pragma unroll
        for (int hf = 0; hf < 2; ++hf)
          pv[s][hf] = __builtin_amdgcn_raw_buffer_load_b128(rs, (c * 4 + s) * 8192 + lo + hf * 1024, 0, 16);
      SB();
    }
    PERIODO(u + 1, F0, F1);
    if (u == 19 && tid == 0) __hip_atomic_store(xfl + c, tag, __ATOMIC_RELAXED, __HIP_MEMORY_SCOPE_AGENT);
    if (u == 21) {
      if (wid == 0) {
        for (unsigned spins = 0;; ++spins) {
          const unsigned long long f = __hip_atomic_load(xfl + (lane & 3), __ATOMIC_RELAXED, __HIP_MEMORY_SCOPE_AGENT);
          if (__all(f == tag) || spins > (1u << 20)) break;
          __builtin_amdgcn_s_sleep(1);
        }
      }
      asm volatile("" ::: "memory");
      ST(1, 4);
    }
  }
  MO(F0, 0); MO(F0, 1); MO(F0, 2); MO(F0, 3);
  ST(1, 6);
#pragma unroll
  for (int half = 0; half < 2; ++half) {
    float o[8];
#pragma unroll
    for (int e = 0; e < 8; ++e) {
      const int rg = half * 8 + e;
      float p[4];
#pragma unroll
      for (int s = 0; s < 4; ++s) { const unsigned w = pv[s][half][e >> 1]; p[s] = c == s ? own[rg] : ((e & 1) ? bfhi(w) : bflo(w)); }
      const float s = (p[0] + p[1]) + (p[2] + p[3]);
      const unsigned xw = xv[half][e >> 1], bw = bv[half][e >> 1];
      const float xx = (e & 1) ? bfhi(xw) : bflo(xw), bb2 = (e & 1) ? bfhi(bw) : bflo(bw);
      const float v = acco[rg] + bb2;
      const float g = v * __builtin_amdgcn_rcpf(1.f + __builtin_amdgcn_exp2f(-1.442695041f * v));
      o[e] = xx + s * g;
    }
    u32x4 ov = {pk_bf16(o[0], o[1]), pk_bf16(o[2], o[3]), pk_bf16(o[4], o[5]), pk_bf16(o[6], o[7])};
    *(u32x4*)(out + (size_t)trow * D_ + ecol + 16 * half) = ov;
  }
  ST(1, 5); ST_FLUSH(1);
}

extern "C" __device__ unsigned long long amd_dispatch_id(void) __asm("llvm.amdgcn.dispatch.id");

__global__ __launch_bounds__(512, 2) void fused(const u16* __restrict__ x, const u16* __restrict__ cre, const u16* __restrict__ cim,
                                                const u16* __restrict__ wre, const u16* __restrict__ wim, const u16* __restrict__ w1,
                                                const u16* __restrict__ b1, const u16* __restrict__ w2, const u16* __restrict__ b2,
                                                char* __restrict__ ws, u16* __restrict__ out) {
  extern __shared__ __attribute__((aligned(16))) char lds[];
  const unsigned long long tag = ((amd_dispatch_id() + 1ull) * 0x9E3779B97F4A7C15ull) ^ ((unsigned long long)__builtin_amdgcn_dispatch_ptr() * 0xC2B2AE3D27D4EB4Full) ^ 0x5851F42D4C957F2Dull;
  Loader ld;
  {
    const int b = blockIdx.x, xcd = b & 7, idx = b >> 3;
    ld.init(cre, cim, ws, lds, __builtin_amdgcn_readfirstlane(threadIdx.x >> 6) & 3, threadIdx.x & 63, xcd >> 1, (xcd & 1) * 8 + (idx >> 2), idx & 3);
  }
  phase1(x, wre, wim, w1, b1, w2, ws, lds, ld, tag);
  phase2(x, cre, cim, b2, ws, out, lds, ld, tag);
}

extern "C" void kernel_launch(void* const* d_in, const int* in_sizes, int n_in, void* d_out, int out_size, void* d_ws, size_t ws_size,
                              hipStream_t stream) {
  (void)hipFuncSetAttribute((const void*)fused, hipFuncAttributeMaxDynamicSharedMemorySize, 163840);
  fused<<<256, 512, 163840, stream>>>((const u16*)d_in[0], (const u16*)d_in[1], (const u16*)d_in[2], (const u16*)d_in[3], (const u16*)d_in[4],
                                      (const u16*)d_in[6], (const u16*)d_in[7], (const u16*)d_in[8], (const u16*)d_in[9], (char*)d_ws, (u16*)d_out);
}
```

```cpp
#include <hip/hip_runtime.h>
#include <hip/hip_bf16.h>
#include <stdint.h>
#include <type_traits>

#define ABL 0
#define XROT 1
typedef unsigned short u16;
typedef short v4s __attribute__((ext_vector_type(4)));
typedef short bf16x8 __attribute__((ext_vector_type(8)));
typedef __bf16 v2bf __attribute__((ext_vector_type(2)));
typedef float f32x4 __attribute__((ext_vector_type(4)));
typedef float f32x16 __attribute__((ext_vector_type(16)));
typedef unsigned u32x4 __attribute__((ext_vector_type(4)));
typedef unsigned u32x2 __attribute__((ext_vector_type(2)));
#define LAS __attribute__((address_space(3)))
#define GAS __attribute__((address_space(1)))

constexpr int S_ = 2048, D_ = 512, F_ = 1376;
constexpr int NKK_B = 64;
constexpr int NSUB = 88;
constexpr size_t WS_B    = 0;
constexpr size_t WS_W2   = 4u << 20;
constexpr size_t WS_H    = 8u << 20;
constexpr size_t WS_SLAB = 16u << 20;
constexpr size_t WS_CNT  = 40u << 20;

__device__ __forceinline__ float bf2f(u16 v) { return __uint_as_float(((unsigned)v) << 16); }
__device__ __forceinline__ float bflo(unsigned v) { return __uint_as_float(v << 16); }
__device__ __forceinline__ float bfhi(unsigned v) { return __uint_as_float(v & 0xffff0000u); }
__device__ __forceinline__ unsigned pk_bf16(float lo, float hi) {
  typedef float f32x2_ __attribute__((ext_vector_type(2)));
  v2bf r = __builtin_convertvector((f32x2_){lo, hi}, v2bf);
  return __builtin_bit_cast(unsigned, r);
}
__device__ __forceinline__ float rbf(float f) { return bflo(pk_bf16(f, 0.f)); }
__device__ __forceinline__ int swap23(int r) { return (r & ~12) | ((r & 4) << 1) | ((r & 8) >> 1); }
__device__ __forceinline__ float dot2(unsigned a, unsigned b, float c) {
  return __builtin_amdgcn_fdot2_f32_bf16(__builtin_bit_cast(v2bf, a), __builtin_bit_cast(v2bf, b), c, false);
}
#define GLDS16(g, l) __builtin_amdgcn_global_load_lds((const GAS void*)(g), (LAS void*)(l), 16, 0, 0)
typedef int i32x4 __attribute__((ext_vector_type(4)));
__device__ __forceinline__ i32x4 mk_rsrc(const void* p) {
  const unsigned long long a = (unsigned long long)p;
  return (i32x4){(int)(unsigned)a, (int)((unsigned)(a >> 32) & 0xffffu), 0x7fffffff, 0x00020000};
}
__device__ __forceinline__ void dma16(i32x4 rsrc, unsigned lds_dst, int voff, int soff) {
  unsigned keep;
  asm volatile("s_mov_b32 %0, m0\n\ts_mov_b32 m0, %1\n\ts_nop 0\n\tbuffer_load_dwordx4 %2, %3, %4 offen lds\n\ts_mov_b32 m0, %0"
               : "=&s"(keep) : "s"(lds_dst), "v"(voff), "s"(rsrc), "s"(soff) : "memory");
}
__device__ __forceinline__ void touch4(i32x4 rsrc, unsigned lds_dst, int voff, int soff) {
  unsigned keep;
  asm volatile("s_mov_b32 %0, m0\n\ts_mov_b32 m0, %1\n\ts_nop 0\n\tbuffer_load_dword %2, %3, %4 offen lds\n\ts_mov_b32 m0, %0"
               : "=&s"(keep) : "s"(lds_dst), "v"(voff), "s"(rsrc), "s"(soff) : "memory");
}
template <int OFF> __device__ __forceinline__ void dma16x(i32x4 rsrc, unsigned lds_dst, int voff, int soff) {
  unsigned keep;
  asm volatile("s_mov_b32 %0, m0\n\ts_mov_b32 m0, %1\n\ts_nop 3\n\tbuffer_load_dwordx4 %2, %3, %4 offen offset:%5 lds\n\ts_mov_b32 m0, %0"
               : "=&s"(keep) : "s"(lds_dst), "v"(voff), "s"(rsrc), "s"(soff), "n"(OFF) : "memory");
}
template <int OFF> __device__ __forceinline__ void dma16x_sc1(i32x4 rsrc, unsigned lds_dst, int voff, int soff) {
  unsigned keep;
  asm volatile("s_mov_b32 %0, m0\n\ts_mov_b32 m0, %1\n\ts_nop 3\n\tbuffer_load_dwordx4 %2, %3, %4 offen offset:%5 sc1 lds\n\ts_mov_b32 m0, %0"
               : "=&s"(keep) : "s"(lds_dst), "v"(voff), "s"(rsrc), "s"(soff), "n"(OFF) : "memory");
}
template <int OFF> __device__ __forceinline__ void dma16x_nt(i32x4 rsrc, unsigned lds_dst, int voff, int soff) {
  unsigned keep;
  asm volatile("s_mov_b32 %0, m0\n\ts_mov_b32 m0, %1\n\ts_nop 3\n\tbuffer_load_dwordx4 %2, %3, %4 offen offset:%5 nt lds\n\ts_mov_b32 m0, %0"
               : "=&s"(keep) : "s"(lds_dst), "v"(voff), "s"(rsrc), "s"(soff), "n"(OFF) : "memory");
}
__device__ __forceinline__ void touch4_nt(i32x4 rsrc, unsigned lds_dst, int voff, int soff) {
  unsigned keep;
  asm volatile("s_mov_b32 %0, m0\n\ts_mov_b32 m0, %1\n\ts_nop 0\n\tbuffer_load_dword %2, %3, %4 offen nt lds\n\ts_mov_b32 m0, %0"
               : "=&s"(keep) : "s"(lds_dst), "v"(voff), "s"(rsrc), "s"(soff) : "memory");
}
#define WAIT_V(n) asm volatile("s_waitcnt vmcnt(%0)" ::"n"(n) : "memory")
#define WAIT_L0() asm volatile("s_waitcnt lgkmcnt(0)" ::: "memory")
#define BAR() __builtin_amdgcn_s_barrier()
#define ST_DECL
#define ST(k, i)
#define ST_FLUSH(k)

#define AUX_PF 2
#define PF_ON 0
#define PF_AT 3
#define AUX_ST 16
#define TOUCH 2
typedef float f32x2 __attribute__((ext_vector_type(2)));
constexpr int SA0 = 0, SA1 = 33280, SH1 = 98304, SLP = 131072, AROW = 1040;
constexpr int SCR = 66560;
constexpr int SC_PART = SCR;
constexpr int SC_HS   = SCR + 2048;
constexpr int SC_RS   = SCR + 3072;
constexpr int SC_MRS  = SCR + 3584;
constexpr int SC_CS   = SCR + 4096;
constexpr int SC_B1   = SCR + 7168;
constexpr int SC_DUM  = SCR + 8192;
#define PIN(x) asm volatile("" : "+v"(x))
#define SBAR() __builtin_amdgcn_sched_barrier(0)

template <int K> __device__ __forceinline__ void eh_op(const f32x16& ap, float (&v)[16], float (&t)[16], unsigned (&ow)[8], float scale, float center) {
  constexpr int st = K / 16, e = K % 16;
  if constexpr (st == 0) { v[e] = fmaf(-center, t[e], v[e]); PIN(v[e]); }
  else if constexpr (st == 1) { v[e] = fmaf(ap[e], scale, v[e]); PIN(v[e]); }
  else if constexpr (st == 2) { t[e] = v[e] * v[e]; PIN(t[e]); }
  else if constexpr (st == 3) { t[e] = fmaf(t[e], -0.1029432397f, -2.302208198f); PIN(t[e]); }
  else if constexpr (st == 4) { t[e] = t[e] * v[e]; PIN(t[e]); }
  else if constexpr (st == 5) { t[e] = __builtin_amdgcn_exp2f(t[e]); PIN(t[e]); }
  else if constexpr (st == 6) { t[e] = 1.f + t[e]; PIN(t[e]); }
  else if constexpr (st == 7) { t[e] = __builtin_amdgcn_rcpf(t[e]); PIN(t[e]); }
  else if constexpr (st == 8) { v[e] = v[e] * t[e]; PIN(v[e]); }
  else if constexpr (K < 152) { ow[e] = pk_bf16(v[2 * e], v[2 * e + 1]); PIN(ow[e]); }
}
template <int K> __device__ __forceinline__ void ea_op(const f32x16& ap, float (&v)[16], float (&t)[16], unsigned (&ow)[8], float cs) {
  constexpr int st = K / 16, e = K % 16;
  if constexpr (st == 0) { t[e] = ap[e] * t[e]; PIN(t[e]); }
  else if constexpr (st == 1) { t[e] = fmaf(-v[e], cs, t[e]); PIN(t[e]); }
  else if constexpr (K < 40) { ow[e] = pk_bf16(t[2 * e], t[2 * e + 1]); PIN(ow[e]); }
}
template <int ET, int K0, int K1> __device__ __forceinline__ void e_ops(const f32x16& ap, float (&v)[16], float (&t)[16], unsigned (&ow)[8], float s0, float s1) {
  if constexpr (K0 < K1) {
    if constexpr (ET == 1) eh_op<K0>(ap, v, t, ow, s0, s1); else if constexpr (ET == 2) ea_op<K0>(ap, v, t, ow, s0);
    e_ops<ET, K0 + 1, K1>(ap, v, t, ow, s0, s1);
  }
}
template <int MT> __device__ __forceinline__ bf16x8 wread(const char* pan, int ks) {
  if constexpr (MT == 1) { return *(const bf16x8*)(pan + ks * 32); }
  else {
    v4s t0 = __builtin_amdgcn_ds_read_tr16_b64_v4i16((LAS v4s*)(pan + ks * 1024));
    v4s t1 = __builtin_amdgcn_ds_read_tr16_b64_v4i16((LAS v4s*)(pan + ks * 1024 + 256));
    return (bf16x8)__builtin_shufflevector(t0, t1, 0, 1, 2, 3, 4, 5, 6, 7);
  }
}
__device__ __forceinline__ void row_stats(float sum, float sq, float& mean, float& inv, float& m_bf, float& scale, float& center) {
  mean = sum * (1.f / 512.f);
  const float var = fmaxf(sq * (1.f / 512.f) - mean * mean, 0.f);
  m_bf = rbf(mean);
  const float sd = rbf(sqrtf(rbf(rbf(var) + 1e-5f)));
  inv = 1.f / sd;
  const float m2 = rbf((mean - m_bf) * inv);
  const float v2 = rbf(var * inv * inv);
  const float sd2 = rbf(sqrtf(rbf(v2 + 1e-5f)));
  scale = inv / sd2; center = (m_bf + m2 * sd) * scale;
}
constexpr int WR = 8, WD = 3;
struct RowStat { float mean, var, m_bf, sd, inv, m2, v2, sd2, scale, center; };
template <int S> __device__ __forceinline__ void rs_stage(RowStat& r) {
  if constexpr (S == 0) { r.mean *= (1.f / 512.f); r.var = fmaxf(r.var * (1.f / 512.f) - r.mean * r.mean, 0.f); r.m_bf = rbf(r.mean); PIN(r.var); PIN(r.m_bf); }
  else if constexpr (S == 1) { r.sd = rbf(rbf(r.var) + 1e-5f); PIN(r.sd); }
  else if constexpr (S == 2) { r.sd = rbf(sqrtf(r.sd)); PIN(r.sd); }
  else if constexpr (S == 3) { r.inv = 1.f / r.sd; PIN(r.inv); }
  else if constexpr (S == 4) { r.m2 = rbf((r.mean - r.m_bf) * r.inv); r.v2 = rbf(rbf(r.var * r.inv * r.inv) + 1e-5f); PIN(r.m2); PIN(r.v2); }
  else if constexpr (S == 5) { r.sd2 = rbf(sqrtf(r.v2)); PIN(r.sd2); }
  else if constexpr (S == 6) { r.scale = r.inv / r.sd2; PIN(r.scale); }
  else if constexpr (S == 7) { r.center = (r.m_bf + r.m2 * r.sd) * r.scale; PIN(r.center); }
}
template <int MT, int ET, int NT, int G0, int KS, class Hook>
__device__ __forceinline__ void unit_steps(const char* pan, const char* pnx, const bf16x8 (&xf)[32], bf16x8 (&wf)[WR], f32x16& aw, const f32x16& ar,
                                           float (&v)[16], float (&t)[16], unsigned (&ow)[8], const float& s0, const float& s1, Hook&& hook) {
  if constexpr (KS < 32) {
    constexpr int NOPS = ET == 1 ? 152 : 40;
    if constexpr (KS == 0) {
      if constexpr (MT == 1) aw = __builtin_amdgcn_mfma_f32_32x32x16_bf16(xf[KS], wf[KS % WR], (f32x16){}, 0, 0, 0);
      else aw = __builtin_amdgcn_mfma_f32_32x32x16_bf16(wf[KS % WR], xf[KS], (f32x16){}, 0, 0, 0);
    } else {
      if constexpr (MT == 1) aw = __builtin_amdgcn_mfma_f32_32x32x16_bf16(xf[KS], wf[KS % WR], aw, 0, 0, 0);
      else aw = __builtin_amdgcn_mfma_f32_32x32x16_bf16(wf[KS % WR], xf[KS], aw, 0, 0, 0);
    }
    PIN(aw);
    SBAR();
    if constexpr (KS + WD < 32) wf[(KS + WD) % WR] = wread<MT>(pan, KS + WD);
    else if constexpr (NT >= 0) wf[(KS + WD) % WR] = wread<NT>(pnx, KS + WD - 32);
    asm volatile("" :: "v"(wf[KS % WR]), "v"(wf[(KS + WR - 1) % WR]));
    SBAR();
    hook(std::integral_constant<int, KS>{});
    constexpr int k0 = KS < G0 ? 0 : (KS - G0) * NOPS / (32 - G0), k1 = KS < G0 ? 0 : (KS + 1 - G0) * NOPS / (32 - G0);
    e_ops<ET, k0, k1>(ar, v, t, ow, s0, s1);
    SBAR();
    unit_steps<MT, ET, NT, G0, KS + 1>(pan, pnx, xf, wf, aw, ar, v, t, ow, s0, s1, hook);
  }
}

typedef __attribute__((address_space(1))) unsigned long long gu64;
__device__ __forceinline__ unsigned ring_off(int t) { return (t & 1) * 32768 + (t & 2) * 49152; }
struct Loader {
  i32x4 ra_s, rb_s, ra_o, rb_o; int aoff[4], voff; unsigned ldsw;
  __device__ __forceinline__ void init(const u16* cre, const u16* cim, char* ws, char* lds, int lw, int lane, int c, int rt, int j) {
#pragma unroll
    for (int i = 0; i < 4; ++i) { const int row = lw * 32 + i * 8 + (lane >> 3); aoff[i] = row * 4096 + (((lane & 7) ^ ((row >> 1) & 7)) * 16) - i * 1024 + 4096; }
    ldsw = (unsigned)(size_t)(LAS char*)lds + lw * 4096;
    ra_s = mk_rsrc((const char*)(c < 2 ? cre : cim) + (size_t)rt * 128 * 4096 + (c & 1) * 2048 - 4096);
    rb_s = mk_rsrc(ws + WS_B + ((size_t)(j * NKK_B + c * 16) << 14) + lw * 4096);
    ra_o = mk_rsrc(ws + WS_H + ((size_t)((rt * 4 + 2 * (lw >> 1) + (c >> 1)) * NSUB) << 10) + (lw & 1) * 4096 - (16 << 13));
    rb_o = mk_rsrc(ws + WS_W2 + ((size_t)((j * 4 + 2 * (lw >> 1) + (c & 1)) * NSUB) << 10) + (lw & 1) * 4096 - (16 << 13));
    voff = lane * 16;
  }
  __device__ __forceinline__ void issue_a(int T) const {
    const unsigned dst = __builtin_amdgcn_readfirstlane(ldsw + ring_off(T));
    if (T < 16) { const int so = __builtin_amdgcn_readfirstlane(T * 128);
                  dma16x_nt<0>(ra_s, dst, aoff[0], so); dma16x_nt<1024>(ra_s, dst, aoff[1], so); dma16x_nt<2048>(ra_s, dst, aoff[2], so); dma16x_nt<3072>(ra_s, dst, aoff[3], so); }
    else { i32x4 r = ra_o; r.z = __builtin_amdgcn_readfirstlane(T < 27 ? 0x7fffffff : 0); const int so = __builtin_amdgcn_readfirstlane(T << 13);
           dma16x_sc1<0>(r, dst, voff, so); dma16x_sc1<1024>(r, dst, voff, so); dma16x_sc1<2048>(r, dst, voff, so); dma16x_sc1<3072>(r, dst, voff, so); }
  }
  __device__ __forceinline__ void issue_b(int T) const {
    const unsigned dst = __builtin_amdgcn_readfirstlane(ldsw + ring_off(T) + 16384);
    i32x4 r = T < 16 ? rb_s : rb_o; r.z = T < 27 ? 0x7fffffff : 0;
    const int so = __builtin_amdgcn_readfirstlane(T < 16 ? T << 14 : T << 13);
    dma16x_sc1<0>(r, dst, voff, so); dma16x_sc1<1024>(r, dst, voff, so); dma16x_sc1<2048>(r, dst, voff, so); dma16x_sc1<3072>(r, dst, voff, so);
  }
};

__device__ __forceinline__ void phase1(const u16* __restrict__ x, const u16* __restrict__ wre, const u16* __restrict__ wim,
                                       const u16* __restrict__ w1, const u16* __restrict__ b1, const u16* __restrict__ w2,
                                       char* __restrict__ ws, char* lds, const Loader& ld, const unsigned long long tag) {
  const int tid = threadIdx.x, lane = tid & 63, wid = __builtin_amdgcn_readfirstlane(tid >> 6);
  const int b = blockIdx.x, xcd = b & 7, idx = b >> 3;
  const int rt = (xcd >> 1) * 4 + (idx & 3);
  const int cg = (xcd & 1) * 8 + (idx >> 2);
  const int h0 = cg < 11 ? cg * 3 : 33 + (cg - 11) * 2;
  const bool h3 = cg < 11;
  const int a0 = cg * 2;
  const int m = wid & 3;
  const bool helper = wid >= 4;
  const int r31 = lane & 31, hh = lane >> 5;
  const unsigned lds0 = (unsigned)(size_t)(LAS char*)lds;
  const int prot = idx & 3;
  ST_DECL;
  ST(0, 0);

  const int hoff = (lane >> 2) * (F_ * 2) + (lane & 3) * 16;
  const i32x4 rw1 = mk_rsrc(w1);
  auto piece_h = [&](int u, int q, int slot_off) { dma16(rw1, lds0 + slot_off + q * 1024, hoff, q * 16 * (F_ * 2) + u * 64); };
  const i32x4 rx = mk_rsrc(x + (size_t)(rt * 128 + m * 32) * D_);
  int xo[8], xs[8];
#pragma unroll
  for (int i = 0; i < 8; ++i) {
    const int ii = (i + XROT * (idx >> 2)) & 7, r = (ii & 3) * 4 + (lane >> 4);
    xo[i] = r * 1024 + (((lane & 15) ^ r) * 16); xs[i] = ii;
  }
  auto quarter = [&](int q) {
#pragma unroll
    for (int i = 0; i < 8; ++i) dma16(rx, lds0 + q * 32768 + m * 8192 + xs[i] * 1024, xo[i], q * 256 + (xs[i] >> 2) * 16384);
    piece_h(h0, q * 8 + ((m * 2 + XROT * 2 * prot) & 7), SLP); piece_h(h0, q * 8 + ((m * 2 + 1 + XROT * 2 * prot) & 7), SLP);
  };
  if (helper) { quarter(0); quarter(1); }

  const auto rws = __builtin_amdgcn_make_buffer_rsrc((void*)ws, 0, 0x7fffffff, 0x00020000);
  const int xrow = m * 32 + swap23(r31);
  const unsigned xbase = xrow * 256 + ((hh ^ (xrow & 15)) * 16);
#define QWAIT(q) WAIT_V(9 + 10 * (3 - (q)))

  if (helper) {
    const auto rw2t = __builtin_amdgcn_make_buffer_rsrc((void*)(ws + WS_W2), 0, 16 * NSUB * 1024, 0x00020000);
    const i32x4 rwa = mk_rsrc((a0 < 16 ? wre : wim) + (size_t)(a0 & 15) * 32 * D_);
    auto panel_h = [&](int u, int slot_off) {
#pragma unroll
      for (int k = 0; k < 8; ++k) piece_h(u, m * 8 + ((k + 2 * prot) & 7), slot_off);
    };
    auto panel_a = [&](int w, int slot_off) {
#pragma unroll
      for (int k = 0; k < 8; ++k) { const int q = m * 8 + ((k + 2 * prot) & 7); dma16(rwa, lds0 + slot_off + q * AROW, lane * 16, (w * 32 + q) * 1024); }
    };
    const v4s ones = {0x3f80, 0x3f80, 0x3f80, 0x3f80};
    const char* pzh = lds + ((lane >> 2) & 15) * 64 + (m & 1) * 32 + (lane & 3) * 8;
    auto cs_h_part = [&](f32x4 (&c)[4], int slot_off, int j0, int n) {
#pragma unroll
      for (int j = 0; j < n; ++j)
        c[j & 3] = __builtin_amdgcn_mfma_f32_4x4x4bf16_1k(ones, __builtin_amdgcn_ds_read_tr16_b64_v4i16((LAS v4s*)(pzh + slot_off + (j0 + j) * 1024)), c[j & 3], 0, 0, 0);
    };
    auto cs_h_fin = [&](f32x4 (&c)[4], int i) {
      float r = (c[0][0] + c[1][0]) + (c[2][0] + c[3][0]);
      r += __shfl_xor(r, 16); r += __shfl_xor(r, 32);
      if (lane < 16) *(float*)(lds + SC_CS + i * 512 + (m & 1) * 64 + lane * 4) = r;
    };
    auto cs_h = [&](int i, int slot_off) {
      if ((m >> 1) == (i & 1)) { f32x4 c[4] = {}; cs_h_part(c, slot_off, 0, 32); cs_h_fin(c, i); }
    };
    auto cs_a = [&](int i, int slot_off) {
      const char* pz = lds + slot_off + swap23(r31) * AROW + hh * 16 + m * 256;
      f32x4 c0 = {}, c1 = {};
#pragma unroll
      for (int j = 0; j < 8; ++j) {
        const bf16x8 w = *(const bf16x8*)(pz + j * 32);
        c0 = __builtin_amdgcn_mfma_f32_4x4x4bf16_1k(ones, __builtin_shufflevector(w, w, 0, 1, 2, 3), c0, 0, 0, 0);
        c1 = __builtin_amdgcn_mfma_f32_4x4x4bf16_1k(ones, __builtin_shufflevector(w, w, 4, 5, 6, 7), c1, 0, 0, 0);
      }
      float r = c0[0] + c1[0]; r += __shfl_xor(r, 32);
      if (lane < 32) *(float*)(lds + SC_CS + i * 512 + m * 128 + lane * 4) = r;
    };
    __builtin_amdgcn_s_setprio(2);
    {
      f32x4 c[4] = {};
      WAIT_V(10); ST(0, 1); BAR();
      quarter(2);
      if (m < 2) cs_h_part(c, SLP, 0, 8);
      WAIT_V(10); BAR();
      quarter(3);
      if (m < 2) cs_h_part(c, SLP, 8, 8);
      WAIT_V(10); BAR();
      panel_a(0, SA0);
      unsigned w2v[2][8], b1v;
      int w2dst[2];
      {
        i32x4 rw2 = mk_rsrc(w2); rw2.z = F_ * D_ * 2;
#pragma unroll
        for (int r = 0; r < 2; ++r) {
          const int hl = r * 256 + (tid - 256), cc = b * 352 + hl;
          const int ln = cc & 63, rest = cc >> 6, ct = rest / NSUB, sub = rest - ct * NSUB;
          const int off = hl < 352 ? ((16 * sub + 4 * (ln >> 5)) * D_ + 32 * ct + swap23(ln & 31)) * 2 : 0x7ff00000;
          w2dst[r] = hl < 352 ? cc * 16 : 0x7ffffff0;
          asm volatile("buffer_load_ushort %0, %8, %10, 0 offen\n\tbuffer_load_ushort %1, %8, %10, 0 offen offset:1024\n\t"
                       "buffer_load_ushort %2, %8, %10, 0 offen offset:2048\n\tbuffer_load_ushort %3, %8, %10, 0 offen offset:3072\n\t"
                       "buffer_load_ushort %4, %9, %10, 0 offen\n\tbuffer_load_ushort %5, %9, %10, 0 offen offset:1024\n\t"
                       "buffer_load_ushort %6, %9, %10, 0 offen offset:2048\n\tbuffer_load_ushort %7, %9, %10, 0 offen offset:3072"
                       : "=&v"(w2v[r][0]), "=&v"(w2v[r][1]), "=&v"(w2v[r][2]), "=&v"(w2v[r][3]), "=&v"(w2v[r][4]), "=&v"(w2v[r][5]), "=&v"(w2v[r][6]), "=&v"(w2v[r][7])
                       : "v"(off), "v"(off + 8192), "s"(rw2) : "memory");
        }
        const int bo = h0 * 32 + lane * 2;
        asm volatile("buffer_load_dword %0, %1, %2, 0 offen" : "=&v"(b1v) : "v"((bo < F_ - 2 ? bo : F_ - 2) * 2), "s"(mk_rsrc(b1)) : "memory");
      }
      if (m < 2) cs_h_part(c, SLP, 16, 8);
      WAIT_V(25); BAR();
      ST(0, 2);
      panel_a(1, SA1);
      if (m < 2) { cs_h_part(c, SLP, 24, 8); cs_h_fin(c, 0); }
      WAIT_V(8);
      asm volatile("" : "+v"(w2v[0][0]), "+v"(w2v[0][1]), "+v"(w2v[0][2]), "+v"(w2v[0][3]), "+v"(w2v[0][4]), "+v"(w2v[0][5]), "+v"(w2v[0][6]), "+v"(w2v[0][7]), "+v"(b1v));
      asm volatile("" : "+v"(w2v[1][0]), "+v"(w2v[1][1]), "+v"(w2v[1][2]), "+v"(w2v[1][3]), "+v"(w2v[1][4]), "+v"(w2v[1][5]), "+v"(w2v[1][6]), "+v"(w2v[1][7]));
      if (m == 3 && lane < 48) *(f32x2*)(lds + SC_B1 + lane * 8) = (f32x2){bflo(b1v), bfhi(b1v)};
#pragma unroll
      for (int r = 0; r < 2; ++r) {
        const u32x4 w = {w2v[r][0] | (w2v[r][1] << 16), w2v[r][2] | (w2v[r][3] << 16), w2v[r][4] | (w2v[r][5] << 16), w2v[r][6] | (w2v[r][7] << 16)};
        __builtin_amdgcn_raw_buffer_store_b128(w, rw2t, w2dst[r], 0, AUX_ST);
      }
    }
    ST(0, 3);
    WAIT_V(2); WAIT_L0(); BAR();
    panel_h(h0 + 1, SH1);
    panel_h(h3 ? h0 + 2 : h0, SLP);
#define CT 1
    {
      const int j2 = idx & 3;
      const int st1 = 3 + 2 * m + hh, st2 = st1 + 8 < 15 ? st1 + 8 : 15;
      const int vb = (j2 * 32 + r31) * 4096 + 4096;
      if (CT == 2) { touch4_nt(ld.ra_s, lds0 + SC_DUM + m * 256, vb + st1 * 128, 0); touch4_nt(ld.ra_s, lds0 + SC_DUM + m * 256, vb + st2 * 128, 0); }
      else { touch4(ld.ra_s, lds0 + SC_DUM + m * 256, vb + st1 * 128, 0); }
    }
    cs_a(1, SA0);
    {
      float mean, inv, m_bf, sc_, ce_;
      const f32x2 pr = *(const f32x2*)(lds + SC_PART + (m * 32 + r31) * 8);
      row_stats(pr.x, pr.y, mean, inv, m_bf, sc_, ce_);
      const float sg = a0 < 16 ? inv : -inv;
      if (lane < 32) { *(float*)(lds + SC_RS + (m * 32 + lane) * 4) = sg; *(float*)(lds + SC_MRS + (m * 32 + lane) * 4) = m_bf * sg; }
    }
    WAIT_V(9); WAIT_L0(); BAR();
    cs_a(2, SA1);
    WAIT_V(1);
    {
      const int j2 = idx & 3;
      const int st1 = 3 + 2 * m + hh, st2 = st1 + 8 < 15 ? st1 + 8 : 15;
      const int vb = (j2 * 32 + r31) * 4096 + 4096;
      if (CT == 2) { touch4_nt(ld.ra_s, lds0 + SC_DUM + m * 256, vb + st1 * 128, 0); touch4_nt(ld.ra_s, lds0 + SC_DUM + m * 256, vb + st2 * 128, 0); }
      else { touch4(ld.ra_s, lds0 + SC_DUM + m * 256, vb + st2 * 128, 0); }
    }
    WAIT_L0(); BAR();
    cs_h(3, SH1);
    ld.issue_a(0); ld.issue_a(1);
    WAIT_L0(); BAR();
    cs_h(4, SLP);
    ld.issue_a(2);
    WAIT_L0(); BAR();
    if (tid == 256) __hip_atomic_store((gu64*)(ws + WS_CNT) + ((cg >> 1) * 2 + (rt >> 3)) * 16 + (rt & 7) * 2 + (cg & 1), tag, __ATOMIC_RELAXED, __HIP_MEMORY_SCOPE_AGENT);
    BAR();
    __builtin_amdgcn_s_setprio(0);
    ST(0, 5); ST_FLUSH(0);
    return;
  }

  bf16x8 xf[32];
  unsigned long long clk0 = 0; (void)clk0;
  const int wrow = swap23(r31);
  const unsigned wlA = (unsigned)(wrow * AROW + hh * 16);
  const unsigned wlH = (unsigned)((8 * hh + ((lane & 15) >> 2)) * 64 + ((lane >> 4) & 1) * 32 + (lane & 3) * 8);
  f32x16 acc, ac2;
  bf16x8 wf[WR];
  float v[16], t[16]; unsigned ow[8];
  auto nohook = [](auto) {};
  RowStat rs;
  const int hstore = (swap23(r31) + 32 * hh) * 16;
  const int s0r = rt * 128 + m * 32;

  auto tables_h = [&](int i, int hu) {
#pragma unroll
    for (int g4 = 0; g4 < 4; ++g4) {
      const f32x4 c = *(const f32x4*)(lds + SC_CS + i * 512 + (8 * g4 + 4 * hh) * 4), bq = *(const f32x4*)(lds + SC_B1 + hu * 128 + (8 * g4 + 4 * hh) * 4);
#pragma unroll
      for (int e = 0; e < 4; ++e) { t[g4 * 4 + e] = c[e]; v[g4 * 4 + e] = bq[e]; }
    }
  };
  auto tables_a = [&](int i) -> float {
#pragma unroll
    for (int g4 = 0; g4 < 4; ++g4) {
      const f32x4 c = *(const f32x4*)(lds + SC_RS + (m * 32 + 8 * g4 + 4 * hh) * 4), bq = *(const f32x4*)(lds + SC_MRS + (m * 32 + 8 * g4 + 4 * hh) * 4);
#pragma unroll
      for (int e = 0; e < 4; ++e) { t[g4 * 4 + e] = c[e]; v[g4 * 4 + e] = bq[e]; }
    }
    const char* cq = lds + SC_CS + i * 512 + r31 * 4;
    return (*(const float*)cq + *(const float*)(cq + 128)) + (*(const float*)(cq + 256) + *(const float*)(cq + 384));
  };
  auto tables_a4 = [&](int i, float (&cp)[4]) {
#pragma unroll
    for (int g4 = 0; g4 < 4; ++g4) {
      const f32x4 c = *(const f32x4*)(lds + SC_RS + (m * 32 + 8 * g4 + 4 * hh) * 4), bq = *(const f32x4*)(lds + SC_MRS + (m * 32 + 8 * g4 + 4 * hh) * 4);
#pragma unroll
      for (int e = 0; e < 4; ++e) { t[g4 * 4 + e] = c[e]; v[g4 * 4 + e] = bq[e]; }
      cp[g4] = *(const float*)(lds + SC_CS + i * 512 + g4 * 128 + r31 * 4);
    }
  };
  auto store_h = [&](int hu, const unsigned (&ow)[8]) {
    const int dst = (int)WS_H + (((rt * 4 + m) * NSUB + 2 * (h0 + hu)) << 10);
    if (hu < 2 || h3) {
      __builtin_amdgcn_raw_buffer_store_b128((u32x4){ow[0], ow[1], ow[2], ow[3]}, rws, hstore, dst, AUX_ST);
      __builtin_amdgcn_raw_buffer_store_b128((u32x4){ow[4], ow[5], ow[6], ow[7]}, rws, hstore + 1024, dst, AUX_ST);
    }
    if (h0 + hu == 42 && (hu < 2 || h3)) { u32x4 z = {0u, 0u, 0u, 0u}; __builtin_amdgcn_raw_buffer_store_b128(z, rws, lane * 16 + 2048, dst, AUX_ST); __builtin_amdgcn_raw_buffer_store_b128(z, rws, lane * 16 + 3072, dst, AUX_ST); }
  };
  auto store_a = [&](int w, const unsigned (&ow)[8]) {
    const int u = a0 + w, et = u & 15, j = et >> 2, nt = et & 3;
    const int kk = (u >> 4) * 32 + (s0r >> 6), sub = (s0r & 63) >> 4;
    const int dst = (int)WS_B + (((j * NKK_B + kk) * 16 + nt * 4 + sub) << 10);
    __builtin_amdgcn_raw_buffer_store_b128((u32x4){ow[0], ow[1], ow[2], ow[3]}, rws, lane * 16, dst, AUX_ST);
    __builtin_amdgcn_raw_buffer_store_b128((u32x4){ow[4], ow[5], ow[6], ow[7]}, rws, lane * 16 + 1024, dst, AUX_ST);
  };
  {
    f32x4 ss0 = {}, ss1 = {}, sq0 = {}, sq1 = {};
    const v4s ones4 = {0x3f80, 0x3f80, 0x3f80, 0x3f80};
    acc = (f32x16){};
    const char* pan = lds + SLP + wlH;
#pragma unroll
    for (int q = 0; q < 4; ++q) {
      if (q == 0) ST(0, 1);
      BAR();
      if (q == 3) ST(0, 2);
      bf16x8 w8[8];
#pragma unroll
      for (int k = 0; k < 8; ++k) xf[q * 8 + k] = *(const bf16x8*)(lds + q * 32768 + (xbase ^ (k * 32)));
#pragma unroll
      for (int k = 0; k < 8; ++k) w8[k] = wread<0>(pan, q * 8 + k);
      SBAR();
#pragma unroll
      for (int k = 0; k < 8; ++k) {
        const v4s lo4 = __builtin_shufflevector(xf[q * 8 + k], xf[q * 8 + k], 0, 1, 2, 3), hi4 = __builtin_shufflevector(xf[q * 8 + k], xf[q * 8 + k], 4, 5, 6, 7);
        ss0 = __builtin_amdgcn_mfma_f32_4x4x4bf16_1k(ones4, lo4, ss0, 0, 0, 0); sq0 = __builtin_amdgcn_mfma_f32_4x4x4bf16_1k(lo4, lo4, sq0, 0, 0, 0);
        ss1 = __builtin_amdgcn_mfma_f32_4x4x4bf16_1k(ones4, hi4, ss1, 0, 0, 0); sq1 = __builtin_amdgcn_mfma_f32_4x4x4bf16_1k(hi4, hi4, sq1, 0, 0, 0);
      }
      asm volatile("" : "+v"(ss0), "+v"(ss1), "+v"(sq0), "+v"(sq1));
      SBAR();
#pragma unroll
      for (int k = 0; k < 8; ++k) { acc = __builtin_amdgcn_mfma_f32_32x32x16_bf16(w8[k], xf[q * 8 + k], acc, 0, 0, 0); PIN(acc); }
      WAIT_L0(); SBAR();
    }
    const f32x4 sqv = sq0 + sq1;
    float sum = ss0[0] + ss1[0], sq = (lane & 2) ? ((lane & 1) ? sqv[3] : sqv[2]) : ((lane & 1) ? sqv[1] : sqv[0]);
    sum += __shfl_xor(sum, 32); sq += __shfl_xor(sq, 32);
    if (lane < 32) *(f32x2*)(lds + SC_PART + (m * 32 + lane) * 8) = (f32x2){sum, sq};
    rs.mean = sum; rs.var = sq;
  }
  ST(0, 8);
  WAIT_L0(); BAR();
  ST(0, 4);
  const char* pA0 = lds + SA0 + wlA; const char* pA1 = lds + SA1 + wlA;
  const char* pH1 = lds + SH1 + wlH; const char* pH2 = lds + SLP + wlH;
#pragma unroll
  for (int e = 0; e < WD; ++e) wf[e] = wread<1>(pA0, e);
  wf[WR - 1] = wf[0];
  tables_h(0, 0);
  float csa = 0.f, cp[4];
  auto cs_hook = [&](auto g) { if constexpr (decltype(g)::value == 2) { csa = (cp[0] + cp[1]); PIN(csa); float c2 = cp[2] + cp[3]; PIN(c2); csa += c2; PIN(csa); } };
  unit_steps<1, 1, 1, 8, 0>(pA0, pA1, xf, wf, ac2, acc, v, t, ow, rs.scale, rs.center, [&](auto g) {
    constexpr int G = decltype(g)::value;
    if constexpr (G < 8) rs_stage<G>(rs);
  });
  store_h(0, ow);
  ST(0, 9);
  BAR();
  ST(0, 6);
  tables_a4(1, cp);
  unit_steps<1, 2, 0, 3, 0>(pA1, pH1, xf, wf, acc, ac2, v, t, ow, csa, csa, cs_hook); store_a(0, ow);
  ST(0, 10);
  BAR();
  ST(0, 7);
  tables_a4(2, cp);
  unit_steps<0, 2, 0, 3, 0>(pH1, pH2, xf, wf, ac2, acc, v, t, ow, csa, csa, cs_hook); store_a(1, ow);
  ST(0, 11);
  BAR();
  ST(0, 13);
  tables_h(3, 1);
  unit_steps<0, 1, -1, 3, 0>(pH2, pH2, xf, wf, acc, ac2, v, t, ow, rs.scale, rs.center, [&](auto g) {
    if constexpr (decltype(g)::value == 14) { WAIT_V(0); BAR(); }
  });
  store_h(1, ow);
  ST(0, 12);
  BAR();
  tables_h(4, 2);
  {
#pragma unroll
    for (int e = 0; e < 8; ++e) {
      f32x2 vv = (f32x2){v[2 * e], v[2 * e + 1]} - (f32x2){t[2 * e], t[2 * e + 1]} * rs.center;
      vv = (f32x2){acc[2 * e], acc[2 * e + 1]} * rs.scale + vv;
      const f32x2 tt = ((vv * vv) * (-0.1029432397f) + (-2.302208198f)) * vv;
      const f32x2 d = (f32x2){__builtin_amdgcn_exp2f(tt.x), __builtin_amdgcn_exp2f(tt.y)} + 1.f;
      vv = vv * (f32x2){__builtin_amdgcn_rcpf(d.x), __builtin_amdgcn_rcpf(d.y)};
      ow[e] = pk_bf16(vv.x, vv.y);
    }
  }
  store_h(2, ow);
  WAIT_V(0);
  ST(0, 5); ST_FLUSH(0);
}

#define AUX_C 2
__device__ __forceinline__ void phase2(const u16* __restrict__ x, const u16* __restrict__ cre, const u16* __restrict__ cim,
                                       const u16* __restrict__ b2, char* __restrict__ ws, u16* __restrict__ out, char* lds, const Loader& ld, const unsigned long long tag) {
  const int tid = threadIdx.x, lane = tid & 63, wid = __builtin_amdgcn_readfirstlane(tid >> 6);
  const int b = blockIdx.x, xcd = b & 7, idx = b >> 3;
#define MAP2 0
  const int c = MAP2 ? (xcd >> 2) * 2 + (idx >> 4) : xcd >> 1, rt = MAP2 ? (xcd & 3) * 4 + ((idx >> 2) & 3) : (xcd & 1) * 8 + (idx >> 2), j = idx & 3;
  ST_DECL;
  ST(1, 0);

#define SB() __builtin_amdgcn_sched_barrier(0)

  if (wid >= 4) {
    gu64* f16 = (gu64*)(ws + WS_CNT) + ((4 * (c >> 1) + j) * 2 + (c & 1)) * 16 + (lane & 15);
    for (unsigned spins = 0;; ++spins) {
      const unsigned long long f = __hip_atomic_load(f16, __ATOMIC_RELAXED, __HIP_MEMORY_SCOPE_AGENT);
      if (__all(f == tag) || spins > (1u << 20)) break;
      __builtin_amdgcn_s_sleep(1);
    }
    asm volatile("" ::: "memory");
    ld.issue_b(0); ld.issue_b(1); ld.issue_b(2);
    for (int u = 0; u < 27; ++u) {
      if (u == 0) WAIT_V(8); else if (u == 1) WAIT_V(12); else WAIT_V(16);
      BAR();
      ld.issue_a(u + 3); ld.issue_b(u + 3);
    }
    WAIT_V(0);
    return;
  }

  const int r31 = lane & 31, hh = lane >> 5;
  const int wm = wid >> 1, wn = wid & 1;
  unsigned ab[4];
#pragma unroll
  for (int s = 0; s < 4; ++s) ab[s] = wm * 8192 + (unsigned)(r31 * 128 + (((2 * s + hh) ^ ((r31 >> 1) & 7)) * 16));
  const unsigned bb = 16384 + wn * 8192 + lane * 16;
  const unsigned ao = wm * 8192 + lane * 16;

  f32x16 acc[2][2] = {};
  f32x16 acco = {};
  bf16x8 F0[16], F1[16];
#define RD(F, s) do { F[4 * (s) + 0] = *(const bf16x8*)(sl + ab[s]); F[4 * (s) + 1] = *(const bf16x8*)(sl + ab[s] + 4096); \
                      F[4 * (s) + 2] = *(const bf16x8*)(sl + bb + (s) * 1024); F[4 * (s) + 3] = *(const bf16x8*)(sl + bb + (s) * 1024 + 4096); } while (0)
#define RDO(F, s) do { F[4 * (s) + 0] = *(const bf16x8*)(sl + ao + (s) * 2048); F[4 * (s) + 1] = *(const bf16x8*)(sl + bb + (s) * 2048); \
                       F[4 * (s) + 2] = *(const bf16x8*)(sl + ao + (s) * 2048 + 1024); F[4 * (s) + 3] = *(const bf16x8*)(sl + bb + (s) * 2048 + 1024); } while (0)
#define MM0(F, s) acc[0][0] = __builtin_amdgcn_mfma_f32_32x32x16_bf16(F[4 * (s) + 2], F[4 * (s) + 0], acc[0][0], 0, 0, 0)
#define MM1(F, s) acc[0][1] = __builtin_amdgcn_mfma_f32_32x32x16_bf16(F[4 * (s) + 3], F[4 * (s) + 0], acc[0][1], 0, 0, 0)
#define MM2(F, s) acc[1][0] = __builtin_amdgcn_mfma_f32_32x32x16_bf16(F[4 * (s) + 2], F[4 * (s) + 1], acc[1][0], 0, 0, 0)
#define MM3(F, s) acc[1][1] = __builtin_amdgcn_mfma_f32_32x32x16_bf16(F[4 * (s) + 3], F[4 * (s) + 1], acc[1][1], 0, 0, 0)
#define MO(F, s) do { acco = __builtin_amdgcn_mfma_f32_32x32x16_bf16(F[4 * (s) + 1], F[4 * (s) + 0], acco, 0, 0, 0); \
                      acco = __builtin_amdgcn_mfma_f32_32x32x16_bf16(F[4 * (s) + 3], F[4 * (s) + 2], acco, 0, 0, 0); } while (0)
#define PERIOD(u, RDX, FR, FM) do { \
    const char* sl = lds + ring_off(u); \
    BAR(); SB(); \
    MM0(FM, 0); SB(); RDX(FR, 0); SB(); MM1(FM, 0); SB(); MM2(FM, 0); SB(); RDX(FR, 1); SB(); MM3(FM, 0); SB(); \
    MM0(FM, 1); SB(); RDX(FR, 2); SB(); MM1(FM, 1); SB(); MM2(FM, 1); SB(); RDX(FR, 3); SB(); MM3(FM, 1); SB(); \
    MM0(FM, 2); MM1(FM, 2); MM2(FM, 2); MM3(FM, 2); \
    MM0(FM, 3); MM1(FM, 3); MM2(FM, 3); SB(); WAIT_L0(); SB(); MM3(FM, 3); SB(); \
  } while (0)
#define PERIODO(u, FR, FM) do { \
    const char* sl = lds + ring_off(u); \
    BAR(); SB(); \
    RDO(FR, 0); SB(); MO(FM, 0); SB(); RDO(FR, 1); SB(); MO(FM, 1); SB(); RDO(FR, 2); SB(); MO(FM, 2); SB(); RDO(FR, 3); SB(); MO(FM, 3); SB(); \
    WAIT_L0(); SB(); \
  } while (0)
  gu64* fl = (gu64*)(ws + WS_CNT + 2048);
  {
    const char* sl = lds;
    BAR(); SB(); ST(1, 1);
    if (tid == 0) __hip_atomic_store(fl + blockIdx.x, tag, __ATOMIC_RELAXED, __HIP_MEMORY_SCOPE_AGENT);

    RD(F0, 0); RD(F0, 1); RD(F0, 2); RD(F0, 3); SB();
    WAIT_L0(); SB();
  }
  for (int u = 1; u < 15; u += 2) {
    PERIOD(u, RD, F1, F0);
    PERIOD(u + 1, RD, F0, F1);
    if (u == 11 && wid == 0) {
      for (unsigned spins = 0;; ++spins) {
        const unsigned long long f0 = __hip_atomic_load(fl + lane * 4, __ATOMIC_RELAXED, __HIP_MEMORY_SCOPE_AGENT), f1 = __hip_atomic_load(fl + lane * 4 + 1, __ATOMIC_RELAXED, __HIP_MEMORY_SCOPE_AGENT),
                                 f2 = __hip_atomic_load(fl + lane * 4 + 2, __ATOMIC_RELAXED, __HIP_MEMORY_SCOPE_AGENT), f3 = __hip_atomic_load(fl + lane * 4 + 3, __ATOMIC_RELAXED, __HIP_MEMORY_SCOPE_AGENT);
        if (__all(f0 == tag && f1 == tag && f2 == tag && f3 == tag) || spins > (1u << 20)) break;
        __builtin_amdgcn_s_sleep(1);
      }
      asm volatile("" ::: "memory");
    }
  }
  PERIOD(15, RD, F1, F0);
  PERIOD(16, RDO, F0, F1);
  ST(1, 2);
  const int tile = rt * 4 + j;
  const auto rs = __builtin_amdgcn_make_buffer_rsrc(ws + WS_SLAB + ((size_t)tile << 17), 0, (ABL & 16) ? 0 : (1 << 17), 0x00020000);
  const unsigned lo = wid * 2048 + lane * 16;
#pragma unroll
  for (int q = 0; q < 4; ++q) {
    if (q != c) {
      const f32x16 v = acc[q >> 1][q & 1];
#pragma unroll
      for (int hf = 0; hf < 2; ++hf) {
        const u32x4 w = {pk_bf16(v[hf * 8], v[hf * 8 + 1]), pk_bf16(v[hf * 8 + 2], v[hf * 8 + 3]), pk_bf16(v[hf * 8 + 4], v[hf * 8 + 5]), pk_bf16(v[hf * 8 + 6], v[hf * 8 + 7])};
        __builtin_amdgcn_raw_buffer_store_b128(w, rs, (q * 4 + c) * 8192 + lo + hf * 1024, 0, 16);
      }
    }
  }
  const f32x16 own = c == 0 ? acc[0][0] : (c == 1 ? acc[0][1] : (c == 2 ? acc[1][0] : acc[1][1]));
  const int trow = rt * 128 + wm * 64 + (c >> 1) * 32 + r31;
  const int ecol = j * 128 + wn * 64 + (c & 1) * 32 + 8 * hh;
  u32x4 xv[2], bv[2];
#pragma unroll
  for (int half = 0; half < 2; ++half) { xv[half] = *(const u32x4*)(x + (size_t)trow * D_ + ecol + 16 * half); bv[half] = *(const u32x4*)(b2 + ecol + 16 * half); }
  SB();
  gu64* xfl = (gu64*)(ws + WS_CNT + 4096 + tile * 128);
  u32x4 pv[4][2];
#pragma unroll
  for (int s = 0; s < 4; ++s) { pv[s][0] = (u32x4){}; pv[s][1] = (u32x4){}; }
#pragma unroll 1
  for (int u = 17; u < 27; u += 2) {
    PERIODO(u, F1, F0);
    if (u == 19) { WAIT_V(4); ST(1, 3); }
    if (u == 23) {
      asm volatile("" ::: "memory");
#pragma unroll
      for (int s = 0; s < 4; ++s)
#pragma unroll
        for (int hf = 0; hf < 2; ++hf)
          pv[s][hf] = __builtin_amdgcn_raw_buffer_load_b128(rs, (c * 4 + s) * 8192 + lo + hf * 1024, 0, 16);
      SB();
    }
    PERIODO(u + 1, F0, F1);
    if (u == 19 && tid == 0) __hip_atomic_store(xfl + c, tag, __ATOMIC_RELAXED, __HIP_MEMORY_SCOPE_AGENT);
    if (u == 21) {
      if (wid == 0) {
        for (unsigned spins = 0;; ++spins) {
          const unsigned long long f = __hip_atomic_load(xfl + (lane & 3), __ATOMIC_RELAXED, __HIP_MEMORY_SCOPE_AGENT);
          if (__all(f == tag) || spins > (1u << 20)) break;
          __builtin_amdgcn_s_sleep(1);
        }
      }
      asm volatile("" ::: "memory");
      ST(1, 4);
    }
  }
  MO(F0, 0); MO(F0, 1); MO(F0, 2); MO(F0, 3);
  ST(1, 6);
#pragma unroll
  for (int half = 0; half < 2; ++half) {
    float o[8];
#pragma unroll
    for (int e = 0; e < 8; ++e) {
      const int rg = half * 8 + e;
      float p[4];
#pragma unroll
      for (int s = 0; s < 4; ++s) { const unsigned w = pv[s][half][e >> 1]; p[s] = c == s ? own[rg] : ((e & 1) ? bfhi(w) : bflo(w)); }
      const float s = (p[0] + p[1]) + (p[2] + p[3]);
      const unsigned xw = xv[half][e >> 1], bw = bv[half][e >> 1];
      const float xx = (e & 1) ? bfhi(xw) : bflo(xw), bb2 = (e & 1) ? bfhi(bw) : bflo(bw);
      const float v = acco[rg] + bb2;
      const float g = v * __builtin_amdgcn_rcpf(1.f + __builtin_amdgcn_exp2f(-1.442695041f * v));
      o[e] = xx + s * g;
    }
    u32x4 ov = {pk_bf16(o[0], o[1]), pk_bf16(o[2], o[3]), pk_bf16(o[4], o[5]), pk_bf16(o[6], o[7])};
    *(u32x4*)(out + (size_t)trow * D_ + ecol + 16 * half) = ov;
  }
  ST(1, 5); ST_FLUSH(1);
}

extern "C" __device__ unsigned long long amd_dispatch_id(void) __asm("llvm.amdgcn.dispatch.id");

__global__ __launch_bounds__(512, 2) void fused(const u16* __restrict__ x, const u16* __restrict__ cre, const u16* __restrict__ cim,
                                                const u16* __restrict__ wre, const u16* __restrict__ wim, const u16* __restrict__ w1,
                                                const u16* __restrict__ b1, const u16* __restrict__ w2, const u16* __restrict__ b2,
                                                char* __restrict__ ws, u16* __restrict__ out) {
  extern __shared__ __attribute__((aligned(16))) char lds[];
  const unsigned long long tag = ((amd_dispatch_id() + 1ull) * 0x9E3779B97F4A7C15ull) ^ ((unsigned long long)__builtin_amdgcn_dispatch_ptr() * 0xC2B2AE3D27D4EB4Full) ^ 0x5851F42D4C957F2Dull;
  Loader ld;
  {
    const int b = blockIdx.x, xcd = b & 7, idx = b >> 3;
    ld.init(cre, cim, ws, lds, __builtin_amdgcn_readfirstlane(threadIdx.x >> 6) & 3, threadIdx.x & 63, xcd >> 1, (xcd & 1) * 8 + (idx >> 2), idx & 3);
  }
  phase1(x, wre, wim, w1, b1, w2, ws, lds, ld, tag);
  phase2(x, cre, cim, b2, ws, out, lds, ld, tag);
}

extern "C" void kernel_launch(void* const* d_in, const int* in_sizes, int n_in, void* d_out, int out_size, void* d_ws, size_t ws_size,
                              hipStream_t stream) {
  (void)hipFuncSetAttribute((const void*)fused, hipFuncAttributeMaxDynamicSharedMemorySize, 163840);
  fused<<<256, 512, 163840, stream>>>((const u16*)d_in[0], (const u16*)d_in[1], (const u16*)d_in[2], (const u16*)d_in[3], (const u16*)d_in[4],
                                      (const u16*)d_in[6], (const u16*)d_in[7], (const u16*)d_in[8], (const u16*)d_in[9], (char*)d_ws, (u16*)d_out);
}
```

```cpp
#include <hip/hip_runtime.h>
#include <hip/hip_bf16.h>
#include <stdint.h>
#include <type_traits>

typedef unsigned short u16;
typedef short v4s __attribute__((ext_vector_type(4)));
typedef short bf16x8 __attribute__((ext_vector_type(8)));
typedef __bf16 v2bf __attribute__((ext_vector_type(2)));
typedef float f32x4 __attribute__((ext_vector_type(4)));
typedef float f32x16 __attribute__((ext_vector_type(16)));
typedef unsigned u32x4 __attribute__((ext_vector_type(4)));
typedef unsigned u32x2 __attribute__((ext_vector_type(2)));
#define LAS __attribute__((address_space(3)))

constexpr int S_ = 2048, D_ = 512, F_ = 1376;
constexpr int NKK_B = 64;
constexpr int NSUB = 88;
constexpr size_t WS_B    = 0;
constexpr size_t WS_W2   = 4u << 20;
constexpr size_t WS_H    = 8u << 20;
constexpr size_t WS_SLAB = 16u << 20;
constexpr size_t WS_CNT  = 40u << 20;

__device__ __forceinline__ float bflo(unsigned v) { return __uint_as_float(v << 16); }
__device__ __forceinline__ float bfhi(unsigned v) { return __uint_as_float(v & 0xffff0000u); }
__device__ __forceinline__ unsigned pk_bf16(float lo, float hi) {
  typedef float f32x2_ __attribute__((ext_vector_type(2)));
  v2bf r = __builtin_convertvector((f32x2_){lo, hi}, v2bf);
  return __builtin_bit_cast(unsigned, r);
}
__device__ __forceinline__ float rbf(float f) { return bflo(pk_bf16(f, 0.f)); }
__device__ __forceinline__ int swap23(int r) { return (r & ~12) | ((r & 4) << 1) | ((r & 8) >> 1); }
typedef int i32x4 __attribute__((ext_vector_type(4)));
__device__ __forceinline__ i32x4 mk_rsrc(const void* p) {
  const unsigned long long a = (unsigned long long)p;
  return (i32x4){(int)(unsigned)a, (int)((unsigned)(a >> 32) & 0xffffu), 0x7fffffff, 0x00020000};
}
__device__ __forceinline__ void dma16(i32x4 rsrc, unsigned lds_dst, int voff, int soff) {
  unsigned keep;
  asm volatile("s_mov_b32 %0, m0\n\ts_mov_b32 m0, %1\n\ts_nop 3\n\tbuffer_load_dwordx4 %2, %3, %4 offen lds\n\ts_mov_b32 m0, %0"
               : "=&s"(keep) : "s"(lds_dst), "v"(voff), "s"(rsrc), "s"(soff) : "memory");
}
__device__ __forceinline__ void touch4(i32x4 rsrc, unsigned lds_dst, int voff, int soff) {
  unsigned keep;
  asm volatile("s_mov_b32 %0, m0\n\ts_mov_b32 m0, %1\n\ts_nop 3\n\tbuffer_load_dword %2, %3, %4 offen lds\n\ts_mov_b32 m0, %0"
               : "=&s"(keep) : "s"(lds_dst), "v"(voff), "s"(rsrc), "s"(soff) : "memory");
}
template <int OFF> __device__ __forceinline__ void dma16x_sc1(i32x4 rsrc, unsigned lds_dst, int voff, int soff) {
  unsigned keep;
  asm volatile("s_mov_b32 %0, m0\n\ts_mov_b32 m0, %1\n\ts_nop 3\n\tbuffer_load_dwordx4 %2, %3, %4 offen offset:%5 sc1 lds\n\ts_mov_b32 m0, %0"
               : "=&s"(keep) : "s"(lds_dst), "v"(voff), "s"(rsrc), "s"(soff), "n"(OFF) : "memory");
}
template <int OFF> __device__ __forceinline__ void dma16x_nt(i32x4 rsrc, unsigned lds_dst, int voff, int soff) {
  unsigned keep;
  asm volatile("s_mov_b32 %0, m0\n\ts_mov_b32 m0, %1\n\ts_nop 3\n\tbuffer_load_dwordx4 %2, %3, %4 offen offset:%5 nt lds\n\ts_mov_b32 m0, %0"
               : "=&s"(keep) : "s"(lds_dst), "v"(voff), "s"(rsrc), "s"(soff), "n"(OFF) : "memory");
}
#define WAIT_V(n) asm volatile("s_waitcnt vmcnt(%0)" ::"n"(n) : "memory")
#define WAIT_L0() asm volatile("s_waitcnt lgkmcnt(0)" ::: "memory")
#define BAR() asm volatile("s_barrier" ::: "memory")
constexpr int AUX_ST = 16;

typedef float f32x2 __attribute__((ext_vector_type(2)));
constexpr int SA0 = 0, SA1 = 33280, SH1 = 98304, SLP = 131072, AROW = 1040;
constexpr int SCR = 66560;
constexpr int SC_PART = SCR;
constexpr int SC_RS   = SCR + 3072;
constexpr int SC_MRS  = SCR + 3584;
constexpr int SC_CS   = SCR + 4096;
constexpr int SC_B1   = SCR + 7168;
constexpr int SC_DUM  = SCR + 8192;
#define PIN(x) asm volatile("" : "+v"(x))
#define SBAR() __builtin_amdgcn_sched_barrier(0)

template <int K> __device__ __forceinline__ void eh_op(const f32x16& ap, float (&v)[16], float (&t)[16], unsigned (&ow)[8], float scale, float center) {
  constexpr int st = K / 16, e = K % 16;
  if constexpr (st == 0) { v[e] = fmaf(-center, t[e], v[e]); PIN(v[e]); }
  else if constexpr (st == 1) { v[e] = fmaf(ap[e], scale, v[e]); PIN(v[e]); }
  else if constexpr (st == 2) { t[e] = v[e] * v[e]; PIN(t[e]); }
  else if constexpr (st == 3) { t[e] = fmaf(t[e], -0.1029432397f, -2.302208198f); PIN(t[e]); }
  else if constexpr (st == 4) { t[e] = t[e] * v[e]; PIN(t[e]); }
  else if constexpr (st == 5) { t[e] = __builtin_amdgcn_exp2f(t[e]); PIN(t[e]); }
  else if constexpr (st == 6) { t[e] = 1.f + t[e]; PIN(t[e]); }
  else if constexpr (st == 7) { t[e] = __builtin_amdgcn_rcpf(t[e]); PIN(t[e]); }
  else if constexpr (st == 8) { v[e] = v[e] * t[e]; PIN(v[e]); }
  else if constexpr (K < 152) { ow[e] = pk_bf16(v[2 * e], v[2 * e + 1]); PIN(ow[e]); }
}
template <int K> __device__ __forceinline__ void ea_op(const f32x16& ap, float (&v)[16], float (&t)[16], unsigned (&ow)[8], float cs) {
  constexpr int st = K / 16, e = K % 16;
  if constexpr (st == 0) { t[e] = ap[e] * t[e]; PIN(t[e]); }
  else if constexpr (st == 1) { t[e] = fmaf(-v[e], cs, t[e]); PIN(t[e]); }
  else if constexpr (K < 40) { ow[e] = pk_bf16(t[2 * e], t[2 * e + 1]); PIN(ow[e]); }
}
template <int ET, int K0, int K1> __device__ __forceinline__ void e_ops(const f32x16& ap, float (&v)[16], float (&t)[16], unsigned (&ow)[8], float s0, float s1) {
  if constexpr (K0 < K1) {
    if constexpr (ET == 1) eh_op<K0>(ap, v, t, ow, s0, s1); else if constexpr (ET == 2) ea_op<K0>(ap, v, t, ow, s0);
    e_ops<ET, K0 + 1, K1>(ap, v, t, ow, s0, s1);
  }
}
template <int MT> __device__ __forceinline__ bf16x8 wread(const char* pan, int ks) {
  if constexpr (MT == 1) { return *(const bf16x8*)(pan + ks * 32); }
  else {
    v4s t0 = __builtin_amdgcn_ds_read_tr16_b64_v4i16((LAS v4s*)(pan + ks * 1024));
    v4s t1 = __builtin_amdgcn_ds_read_tr16_b64_v4i16((LAS v4s*)(pan + ks * 1024 + 256));
    return (bf16x8)__builtin_shufflevector(t0, t1, 0, 1, 2, 3, 4, 5, 6, 7);
  }
}
__device__ __forceinline__ void row_stats(float sum, float sq, float& mean, float& inv, float& m_bf, float& scale, float& center) {
  mean = sum * (1.f / 512.f);
  const float var = fmaxf(sq * (1.f / 512.f) - mean * mean, 0.f);
  m_bf = rbf(mean);
  const float sd = rbf(sqrtf(rbf(rbf(var) + 1e-5f)));
  inv = 1.f / sd;
  const float m2 = rbf((mean - m_bf) * inv);
  const float v2 = rbf(var * inv * inv);
  const float sd2 = rbf(sqrtf(rbf(v2 + 1e-5f)));
  scale = inv / sd2; center = (m_bf + m2 * sd) * scale;
}
constexpr int WR = 8, WD = 3;
struct RowStat { float mean, var, m_bf, sd, inv, m2, v2, sd2, scale, center; };
template <int S> __device__ __forceinline__ void rs_stage(RowStat& r) {
  if constexpr (S == 0) { r.mean *= (1.f / 512.f); r.var = fmaxf(r.var * (1.f / 512.f) - r.mean * r.mean, 0.f); r.m_bf = rbf(r.mean); PIN(r.var); PIN(r.m_bf); }
  else if constexpr (S == 1) { r.sd = rbf(rbf(r.var) + 1e-5f); PIN(r.sd); }
  else if constexpr (S == 2) { r.sd = rbf(sqrtf(r.sd)); PIN(r.sd); }
  else if constexpr (S == 3) { r.inv = 1.f / r.sd; PIN(r.inv); }
  else if constexpr (S == 4) { r.m2 = rbf((r.mean - r.m_bf) * r.inv); r.v2 = rbf(rbf(r.var * r.inv * r.inv) + 1e-5f); PIN(r.m2); PIN(r.v2); }
  else if constexpr (S == 5) { r.sd2 = rbf(sqrtf(r.v2)); PIN(r.sd2); }
  else if constexpr (S == 6) { r.scale = r.inv / r.sd2; PIN(r.scale); }
  else if constexpr (S == 7) { r.center = (r.m_bf + r.m2 * r.sd) * r.scale; PIN(r.center); }
}
template <int MT, int ET, int NT, int G0, int KS, class Hook>
__device__ __forceinline__ void unit_steps(const char* pan, const char* pnx, const bf16x8 (&xf)[32], bf16x8 (&wf)[WR], f32x16& aw, const f32x16& ar,
                                           float (&v)[16], float (&t)[16], unsigned (&ow)[8], const float& s0, const float& s1, Hook&& hook) {
  if constexpr (KS < 32) {
    constexpr int NOPS = ET == 1 ? 152 : 40;
    if constexpr (KS == 0) {
      if constexpr (MT == 1) aw = __builtin_amdgcn_mfma_f32_32x32x16_bf16(xf[KS], wf[KS % WR], (f32x16){}, 0, 0, 0);
      else aw = __builtin_amdgcn_mfma_f32_32x32x16_bf16(wf[KS % WR], xf[KS], (f32x16){}, 0, 0, 0);
    } else {
      if constexpr (MT == 1) aw = __builtin_amdgcn_mfma_f32_32x32x16_bf16(xf[KS], wf[KS % WR], aw, 0, 0, 0);
      else aw = __builtin_amdgcn_mfma_f32_32x32x16_bf16(wf[KS % WR], xf[KS], aw, 0, 0, 0);
    }
    PIN(aw);
    SBAR();
    if constexpr (KS + WD < 32) wf[(KS + WD) % WR] = wread<MT>(pan, KS + WD);
    else if constexpr (NT >= 0) wf[(KS + WD) % WR] = wread<NT>(pnx, KS + WD - 32);
    asm volatile("" :: "v"(wf[KS % WR]), "v"(wf[(KS + WR - 1) % WR]));
    SBAR();
    hook(std::integral_constant<int, KS>{});
    constexpr int k0 = KS < G0 ? 0 : (KS - G0) * NOPS / (32 - G0), k1 = KS < G0 ? 0 : (KS + 1 - G0) * NOPS / (32 - G0);
    e_ops<ET, k0, k1>(ar, v, t, ow, s0, s1);
    SBAR();
    unit_steps<MT, ET, NT, G0, KS + 1>(pan, pnx, xf, wf, aw, ar, v, t, ow, s0, s1, hook);
  }
}

typedef __attribute__((address_space(1))) unsigned long long gu64;
__device__ __forceinline__ unsigned ring_off(int t) { return (t & 1) * 32768 + (t & 2) * 49152; }
struct Loader {
  i32x4 ra_s, rb_s, ra_o, rb_o; int aoff[4], voff; unsigned ldsw;
  __device__ __forceinline__ void init(const u16* cre, const u16* cim, char* ws, char* lds, int lw, int lane, int c, int rt, int j) {
#pragma unroll
    for (int i = 0; i < 4; ++i) { const int row = lw * 32 + i * 8 + (lane >> 3); aoff[i] = row * 4096 + (((lane & 7) ^ ((row >> 1) & 7)) * 16) - i * 1024 + 4096; }
    ldsw = (unsigned)(size_t)(LAS char*)lds + lw * 4096;
    ra_s = mk_rsrc((const char*)(c < 2 ? cre : cim) + (size_t)rt * 128 * 4096 + (c & 1) * 2048 - 4096);
    rb_s = mk_rsrc(ws + WS_B + ((size_t)(j * NKK_B + c * 16) << 14) + lw * 4096);
    ra_o = mk_rsrc(ws + WS_H + ((size_t)((rt * 4 + 2 * (lw >> 1) + (c >> 1)) * NSUB) << 10) + (lw & 1) * 4096 - (16 << 13));
    rb_o = mk_rsrc(ws + WS_W2 + ((size_t)((j * 4 + 2 * (lw >> 1) + (c & 1)) * NSUB) << 10) + (lw & 1) * 4096 - (16 << 13));
    voff = lane * 16;
  }
  __device__ __forceinline__ void issue_a(int T) const {
    const unsigned dst = __builtin_amdgcn_readfirstlane(ldsw + ring_off(T));
    if (T < 16) { const int so = __builtin_amdgcn_readfirstlane(T * 128);
                  dma16x_nt<0>(ra_s, dst, aoff[0], so); dma16x_nt<1024>(ra_s, dst, aoff[1], so); dma16x_nt<2048>(ra_s, dst, aoff[2], so); dma16x_nt<3072>(ra_s, dst, aoff[3], so); }
    else { i32x4 r = ra_o; r.z = __builtin_amdgcn_readfirstlane(T < 27 ? 0x7fffffff : 0); const int so = __builtin_amdgcn_readfirstlane(T << 13);
           dma16x_sc1<0>(r, dst, voff, so); dma16x_sc1<1024>(r, dst, voff, so); dma16x_sc1<2048>(r, dst, voff, so); dma16x_sc1<3072>(r, dst, voff, so); }
  }
  __device__ __forceinline__ void issue_b(int T) const {
    const unsigned dst = __builtin_amdgcn_readfirstlane(ldsw + ring_off(T) + 16384);
    i32x4 r = T < 16 ? rb_s : rb_o; r.z = T < 27 ? 0x7fffffff : 0;
    const int so = __builtin_amdgcn_readfirstlane(T < 16 ? T << 14 : T << 13);
    dma16x_sc1<0>(r, dst, voff, so); dma16x_sc1<1024>(r, dst, voff, so); dma16x_sc1<2048>(r, dst, voff, so); dma16x_sc1<3072>(r, dst, voff, so);
  }
};

__device__ __forceinline__ void phase1(const u16* __restrict__ x, const u16* __restrict__ wre, const u16* __restrict__ wim,
                                       const u16* __restrict__ w1, const u16* __restrict__ b1, const u16* __restrict__ w2,
                                       char* __restrict__ ws, char* lds, const Loader& ld, const unsigned long long tag) {
  const int tid = threadIdx.x, lane = tid & 63, wid = __builtin_amdgcn_readfirstlane(tid >> 6);
  const int b = blockIdx.x, xcd = b & 7, idx = b >> 3;
  const int rt = (xcd >> 1) * 4 + (idx & 3);
  const int cg = (xcd & 1) * 8 + (idx >> 2);
  const int h0 = cg < 11 ? cg * 3 : 33 + (cg - 11) * 2;
  const bool h3 = cg < 11;
  const int a0 = cg * 2;
  const int m = wid & 3;
  const bool helper = wid >= 4;
  const int r31 = lane & 31, hh = lane >> 5;
  const unsigned lds0 = (unsigned)(size_t)(LAS char*)lds;
  const int prot = idx & 3;

  const int hoff = (lane >> 2) * (F_ * 2) + (lane & 3) * 16;
  const i32x4 rw1 = mk_rsrc(w1);
  auto piece_h = [&](int u, int q, int slot_off) { dma16(rw1, lds0 + slot_off + q * 1024, hoff, q * 16 * (F_ * 2) + u * 64); };
  const i32x4 rx = mk_rsrc(x + (size_t)(rt * 128 + m * 32) * D_);
  int xo[8], xs[8];
#pragma unroll
  for (int i = 0; i < 8; ++i) {
    const int ii = (i + (idx >> 2)) & 7, r = (ii & 3) * 4 + (lane >> 4);
    xo[i] = r * 1024 + (((lane & 15) ^ r) * 16); xs[i] = ii;
  }
  auto quarter = [&](int q) {
#pragma unroll
    for (int i = 0; i < 8; ++i) dma16(rx, lds0 + q * 32768 + m * 8192 + xs[i] * 1024, xo[i], q * 256 + (xs[i] >> 2) * 16384);
    piece_h(h0, q * 8 + ((m * 2 + 2 * prot) & 7), SLP); piece_h(h0, q * 8 + ((m * 2 + 1 + 2 * prot) & 7), SLP);
  };
  if (helper) { quarter(0); quarter(1); }

  const auto rws = __builtin_amdgcn_make_buffer_rsrc((void*)ws, 0, 0x7fffffff, 0x00020000);
  const int xrow = m * 32 + swap23(r31);
  const unsigned xbase = xrow * 256 + ((hh ^ (xrow & 15)) * 16);

  if (helper) {
    const auto rw2t = __builtin_amdgcn_make_buffer_rsrc((void*)(ws + WS_W2), 0, 16 * NSUB * 1024, 0x00020000);
    const i32x4 rwa = mk_rsrc((a0 < 16 ? wre : wim) + (size_t)(a0 & 15) * 32 * D_);
    auto panel_h = [&](int u, int slot_off) {
#pragma unroll
      for (int k = 0; k < 8; ++k) piece_h(u, m * 8 + ((k + 2 * prot) & 7), slot_off);
    };
    auto panel_a = [&](int w, int slot_off) {
#pragma unroll
      for (int k = 0; k < 8; ++k) { const int q = m * 8 + ((k + 2 * prot) & 7); dma16(rwa, lds0 + slot_off + q * AROW, lane * 16, (w * 32 + q) * 1024); }
    };
    const v4s ones = {0x3f80, 0x3f80, 0x3f80, 0x3f80};
    const char* pzh = lds + ((lane >> 2) & 15) * 64 + (m & 1) * 32 + (lane & 3) * 8;
    auto cs_h_part = [&](f32x4 (&c)[4], int slot_off, int j0, int n) {
#pragma unroll
      for (int j = 0; j < n; ++j)
        c[j & 3] = __builtin_amdgcn_mfma_f32_4x4x4bf16_1k(ones, __builtin_amdgcn_ds_read_tr16_b64_v4i16((LAS v4s*)(pzh + slot_off + (j0 + j) * 1024)), c[j & 3], 0, 0, 0);
    };
    auto cs_h_fin = [&](f32x4 (&c)[4], int i) {
      float r = (c[0][0] + c[1][0]) + (c[2][0] + c[3][0]);
      r += __shfl_xor(r, 16); r += __shfl_xor(r, 32);
      if (lane < 16) *(float*)(lds + SC_CS + i * 512 + (m & 1) * 64 + lane * 4) = r;
    };
    auto cs_h = [&](int i, int slot_off) {
      if ((m >> 1) == (i & 1)) { f32x4 c[4] = {}; cs_h_part(c, slot_off, 0, 32); cs_h_fin(c, i); }
    };
    auto cs_a = [&](int i, int slot_off) {
      const char* pz = lds + slot_off + swap23(r31) * AROW + hh * 16 + m * 256;
      f32x4 c0 = {}, c1 = {};
#pragma unroll
      for (int j = 0; j < 8; ++j) {
        const bf16x8 w = *(const bf16x8*)(pz + j * 32);
        c0 = __builtin_amdgcn_mfma_f32_4x4x4bf16_1k(ones, __builtin_shufflevector(w, w, 0, 1, 2, 3), c0, 0, 0, 0);
        c1 = __builtin_amdgcn_mfma_f32_4x4x4bf16_1k(ones, __builtin_shufflevector(w, w, 4, 5, 6, 7), c1, 0, 0, 0);
      }
      float r = c0[0] + c1[0]; r += __shfl_xor(r, 32);
      if (lane < 32) *(float*)(lds + SC_CS + i * 512 + m * 128 + lane * 4) = r;
    };
    __builtin_amdgcn_s_setprio(2);
    {
      f32x4 c[4] = {};
      WAIT_V(10); BAR();
      quarter(2);
      if (m < 2) cs_h_part(c, SLP, 0, 8);
      WAIT_V(10); BAR();
      quarter(3);
      if (m < 2) cs_h_part(c, SLP, 8, 8);
      WAIT_V(10); BAR();
      panel_a(0, SA0);
      unsigned w2v[2][8], b1v;
      int w2dst[2];
      {
        i32x4 rw2 = mk_rsrc(w2); rw2.z = F_ * D_ * 2;
#pragma unroll
        for (int r = 0; r < 2; ++r) {
          const int hl = r * 256 + (tid - 256), cc = b * 352 + hl;
          const int ln = cc & 63, rest = cc >> 6, ct = rest / NSUB, sub = rest - ct * NSUB;
          const int off = hl < 352 ? ((16 * sub + 4 * (ln >> 5)) * D_ + 32 * ct + swap23(ln & 31)) * 2 : 0x7ff00000;
          w2dst[r] = hl < 352 ? cc * 16 : 0x7ffffff0;
          asm volatile("buffer_load_ushort %0, %8, %10, 0 offen\n\tbuffer_load_ushort %1, %8, %10, 0 offen offset:1024\n\t"
                       "buffer_load_ushort %2, %8, %10, 0 offen offset:2048\n\tbuffer_load_ushort %3, %8, %10, 0 offen offset:3072\n\t"
                       "buffer_load_ushort %4, %9, %10, 0 offen\n\tbuffer_load_ushort %5, %9, %10, 0 offen offset:1024\n\t"
                       "buffer_load_ushort %6, %9, %10, 0 offen offset:2048\n\tbuffer_load_ushort %7, %9, %10, 0 offen offset:3072"
                       : "=&v"(w2v[r][0]), "=&v"(w2v[r][1]), "=&v"(w2v[r][2]), "=&v"(w2v[r][3]), "=&v"(w2v[r][4]), "=&v"(w2v[r][5]), "=&v"(w2v[r][6]), "=&v"(w2v[r][7])
                       : "v"(off), "v"(off + 8192), "s"(rw2) : "memory");
        }
        const int bo = h0 * 32 + lane * 2;
        asm volatile("buffer_load_dword %0, %1, %2, 0 offen" : "=&v"(b1v) : "v"((bo < F_ - 2 ? bo : F_ - 2) * 2), "s"(mk_rsrc(b1)) : "memory");
      }
      if (m < 2) cs_h_part(c, SLP, 16, 8);
      WAIT_V(25); BAR();
      panel_a(1, SA1);
      if (m < 2) { cs_h_part(c, SLP, 24, 8); cs_h_fin(c, 0); }
      WAIT_V(8);
      asm volatile("" : "+v"(w2v[0][0]), "+v"(w2v[0][1]), "+v"(w2v[0][2]), "+v"(w2v[0][3]), "+v"(w2v[0][4]), "+v"(w2v[0][5]), "+v"(w2v[0][6]), "+v"(w2v[0][7]), "+v"(b1v));
      asm volatile("" : "+v"(w2v[1][0]), "+v"(w2v[1][1]), "+v"(w2v[1][2]), "+v"(w2v[1][3]), "+v"(w2v[1][4]), "+v"(w2v[1][5]), "+v"(w2v[1][6]), "+v"(w2v[1][7]));
      if (m == 3 && lane < 48) *(f32x2*)(lds + SC_B1 + lane * 8) = (f32x2){bflo(b1v), bfhi(b1v)};
#pragma unroll
      for (int r = 0; r < 2; ++r) {
        const u32x4 w = {w2v[r][0] | (w2v[r][1] << 16), w2v[r][2] | (w2v[r][3] << 16), w2v[r][4] | (w2v[r][5] << 16), w2v[r][6] | (w2v[r][7] << 16)};
        __builtin_amdgcn_raw_buffer_store_b128(w, rw2t, w2dst[r], 0, AUX_ST);
      }
    }
    WAIT_V(2); WAIT_L0(); BAR();
    panel_h(h0 + 1, SH1);
    panel_h(h3 ? h0 + 2 : h0, SLP);
    {
      const int j2 = idx & 3;
      const int st1 = 3 + 2 * m + hh, st2 = st1 + 8 < 15 ? st1 + 8 : 15;
      const int vb = (j2 * 32 + r31) * 4096 + 4096;
      touch4(ld.ra_s, lds0 + SC_DUM + m * 256, vb + st1 * 128, 0);
    }
    cs_a(1, SA0);
    {
      float mean, inv, m_bf, sc_, ce_;
      const f32x2 pr = *(const f32x2*)(lds + SC_PART + (m * 32 + r31) * 8);
      row_stats(pr.x, pr.y, mean, inv, m_bf, sc_, ce_);
      const float sg = a0 < 16 ? inv : -inv;
      if (lane < 32) { *(float*)(lds + SC_RS + (m * 32 + lane) * 4) = sg; *(float*)(lds + SC_MRS + (m * 32 + lane) * 4) = m_bf * sg; }
    }
    WAIT_V(9); WAIT_L0(); BAR();
    cs_a(2, SA1);
    WAIT_V(1);
    {
      const int j2 = idx & 3;
      const int st1 = 3 + 2 * m + hh, st2 = st1 + 8 < 15 ? st1 + 8 : 15;
      const int vb = (j2 * 32 + r31) * 4096 + 4096;
      touch4(ld.ra_s, lds0 + SC_DUM + m * 256, vb + st2 * 128, 0);
    }
    WAIT_L0(); BAR();
    cs_h(3, SH1);
    ld.issue_a(0); ld.issue_a(1);
    WAIT_L0(); BAR();
    cs_h(4, SLP);
    ld.issue_a(2);
    WAIT_L0(); BAR();
    if (tid == 256) __hip_atomic_store((gu64*)(ws + WS_CNT) + ((cg >> 1) * 2 + (rt >> 3)) * 16 + (rt & 7) * 2 + (cg & 1), tag, __ATOMIC_RELAXED, __HIP_MEMORY_SCOPE_AGENT);
    BAR();
    __builtin_amdgcn_s_setprio(0);
    return;
  }

  bf16x8 xf[32];
  const int wrow = swap23(r31);
  const unsigned wlA = (unsigned)(wrow * AROW + hh * 16);
  const unsigned wlH = (unsigned)((8 * hh + ((lane & 15) >> 2)) * 64 + ((lane >> 4) & 1) * 32 + (lane & 3) * 8);
  f32x16 acc, ac2;
  bf16x8 wf[WR];
  float v[16], t[16]; unsigned ow[8];
  auto nohook = [](auto) {};
  RowStat rs;
  const int hstore = (swap23(r31) + 32 * hh) * 16;
  const int s0r = rt * 128 + m * 32;

  auto tables_h = [&](int i, int hu) {
#pragma unroll
    for (int g4 = 0; g4 < 4; ++g4) {
      const f32x4 c = *(const f32x4*)(lds + SC_CS + i * 512 + (8 * g4 + 4 * hh) * 4), bq = *(const f32x4*)(lds + SC_B1 + hu * 128 + (8 * g4 + 4 * hh) * 4);
#pragma unroll
      for (int e = 0; e < 4; ++e) { t[g4 * 4 + e] = c[e]; v[g4 * 4 + e] = bq[e]; }
    }
  };
  auto tables_a = [&](int i) -> float {
#pragma unroll
    for (int g4 = 0; g4 < 4; ++g4) {
      const f32x4 c = *(const f32x4*)(lds + SC_RS + (m * 32 + 8 * g4 + 4 * hh) * 4), bq = *(const f32x4*)(lds + SC_MRS + (m * 32 + 8 * g4 + 4 * hh) * 4);
#pragma unroll
      for (int e = 0; e < 4; ++e) { t[g4 * 4 + e] = c[e]; v[g4 * 4 + e] = bq[e]; }
    }
    const char* cq = lds + SC_CS + i * 512 + r31 * 4;
    return (*(const float*)cq + *(const float*)(cq + 128)) + (*(const float*)(cq + 256) + *(const float*)(cq + 384));
  };
  auto tables_a4 = [&](int i, float (&cp)[4]) {
#pragma unroll
    for (int g4 = 0; g4 < 4; ++g4) {
      const f32x4 c = *(const f32x4*)(lds + SC_RS + (m * 32 + 8 * g4 + 4 * hh) * 4), bq = *(const f32x4*)(lds + SC_MRS + (m * 32 + 8 * g4 + 4 * hh) * 4);
#pragma unroll
      for (int e = 0; e < 4; ++e) { t[g4 * 4 + e] = c[e]; v[g4 * 4 + e] = bq[e]; }
      cp[g4] = *(const float*)(lds + SC_CS + i * 512 + g4 * 128 + r31 * 4);
    }
  };
  auto store_h = [&](int hu, const unsigned (&ow)[8]) {
    const int dst = (int)WS_H + (((rt * 4 + m) * NSUB + 2 * (h0 + hu)) << 10);
    if (hu < 2 || h3) {
      __builtin_amdgcn_raw_buffer_store_b128((u32x4){ow[0], ow[1], ow[2], ow[3]}, rws, hstore, dst, AUX_ST);
      __builtin_amdgcn_raw_buffer_store_b128((u32x4){ow[4], ow[5], ow[6], ow[7]}, rws, hstore + 1024, dst, AUX_ST);
    }
    if (h0 + hu == 42 && (hu < 2 || h3)) { u32x4 z = {0u, 0u, 0u, 0u}; __builtin_amdgcn_raw_buffer_store_b128(z, rws, lane * 16 + 2048, dst, AUX_ST); __builtin_amdgcn_raw_buffer_store_b128(z, rws, lane * 16 + 3072, dst, AUX_ST); }
  };
  auto store_a = [&](int w, const unsigned (&ow)[8]) {
    const int u = a0 + w, et = u & 15, j = et >> 2, nt = et & 3;
    const int kk = (u >> 4) * 32 + (s0r >> 6), sub = (s0r & 63) >> 4;
    const int dst = (int)WS_B + (((j * NKK_B + kk) * 16 + nt * 4 + sub) << 10);
    __builtin_amdgcn_raw_buffer_store_b128((u32x4){ow[0], ow[1], ow[2], ow[3]}, rws, lane * 16, dst, AUX_ST);
    __builtin_amdgcn_raw_buffer_store_b128((u32x4){ow[4], ow[5], ow[6], ow[7]}, rws, lane * 16 + 1024, dst, AUX_ST);
  };
  {
    f32x4 ss0 = {}, ss1 = {}, sq0 = {}, sq1 = {};
    const v4s ones4 = {0x3f80, 0x3f80, 0x3f80, 0x3f80};
    acc = (f32x16){};
    const char* pan = lds + SLP + wlH;
#pragma unroll
    for (int q = 0; q < 4; ++q) {
      BAR();
      bf16x8 w8[8];
#pragma unroll
      for (int k = 0; k < 8; ++k) xf[q * 8 + k] = *(const bf16x8*)(lds + q * 32768 + (xbase ^ (k * 32)));
#pragma unroll
      for (int k = 0; k < 8; ++k) w8[k] = wread<0>(pan, q * 8 + k);
      SBAR();
#pragma unroll
      for (int k = 0; k < 8; ++k) {
        const v4s lo4 = __builtin_shufflevector(xf[q * 8 + k], xf[q * 8 + k], 0, 1, 2, 3), hi4 = __builtin_shufflevector(xf[q * 8 + k], xf[q * 8 + k], 4, 5, 6, 7);
        ss0 = __builtin_amdgcn_mfma_f32_4x4x4bf16_1k(ones4, lo4, ss0, 0, 0, 0); sq0 = __builtin_amdgcn_mfma_f32_4x4x4bf16_1k(lo4, lo4, sq0, 0, 0, 0);
        ss1 = __builtin_amdgcn_mfma_f32_4x4x4bf16_1k(ones4, hi4, ss1, 0, 0, 0); sq1 = __builtin_amdgcn_mfma_f32_4x4x4bf16_1k(hi4, hi4, sq1, 0, 0, 0);
      }
      asm volatile("" : "+v"(ss0), "+v"(ss1), "+v"(sq0), "+v"(sq1));
      SBAR();
#pragma unroll
      for (int k = 0; k < 8; ++k) { acc = __builtin_amdgcn_mfma_f32_32x32x16_bf16(w8[k], xf[q * 8 + k], acc, 0, 0, 0); PIN(acc); }
      WAIT_L0(); SBAR();
    }
    const f32x4 sqv = sq0 + sq1;
    float sum = ss0[0] + ss1[0], sq = (lane & 2) ? ((lane & 1) ? sqv[3] : sqv[2]) : ((lane & 1) ? sqv[1] : sqv[0]);
    sum += __shfl_xor(sum, 32); sq += __shfl_xor(sq, 32);
    if (lane < 32) *(f32x2*)(lds + SC_PART + (m * 32 + lane) * 8) = (f32x2){sum, sq};
    rs.mean = sum; rs.var = sq;
  }
  WAIT_L0(); BAR();
  const char* pA0 = lds + SA0 + wlA; const char* pA1 = lds + SA1 + wlA;
  const char* pH1 = lds + SH1 + wlH; const char* pH2 = lds + SLP + wlH;
#pragma unroll
  for (int e = 0; e < WD; ++e) wf[e] = wread<1>(pA0, e);
  wf[WR - 1] = wf[0];
  tables_h(0, 0);
  float csa = 0.f, cp[4];
  auto cs_hook = [&](auto g) { if constexpr (decltype(g)::value == 2) { csa = (cp[0] + cp[1]); PIN(csa); float c2 = cp[2] + cp[3]; PIN(c2); csa += c2; PIN(csa); } };
  unit_steps<1, 1, 1, 8, 0>(pA0, pA1, xf, wf, ac2, acc, v, t, ow, rs.scale, rs.center, [&](auto g) {
    constexpr int G = decltype(g)::value;
    if constexpr (G < 8) rs_stage<G>(rs);
  });
  store_h(0, ow);
  BAR();
  tables_a4(1, cp);
  unit_steps<1, 2, 0, 3, 0>(pA1, pH1, xf, wf, acc, ac2, v, t, ow, csa, csa, cs_hook); store_a(0, ow);
  BAR();
  tables_a4(2, cp);
  unit_steps<0, 2, 0, 3, 0>(pH1, pH2, xf, wf, ac2, acc, v, t, ow, csa, csa, cs_hook); store_a(1, ow);
  BAR();
  tables_h(3, 1);
  unit_steps<0, 1, -1, 3, 0>(pH2, pH2, xf, wf, acc, ac2, v, t, ow, rs.scale, rs.center, [&](auto g) {
    if constexpr (decltype(g)::value == 14) { WAIT_V(0); BAR(); }
  });
  store_h(1, ow);
  BAR();
  tables_h(4, 2);
  {
#pragma unroll
    for (int e = 0; e < 8; ++e) {
      f32x2 vv = (f32x2){v[2 * e], v[2 * e + 1]} - (f32x2){t[2 * e], t[2 * e + 1]} * rs.center;
      vv = (f32x2){acc[2 * e], acc[2 * e + 1]} * rs.scale + vv;
      const f32x2 tt = ((vv * vv) * (-0.1029432397f) + (-2.302208198f)) * vv;
      const f32x2 d = (f32x2){__builtin_amdgcn_exp2f(tt.x), __builtin_amdgcn_exp2f(tt.y)} + 1.f;
      vv = vv * (f32x2){__builtin_amdgcn_rcpf(d.x), __builtin_amdgcn_rcpf(d.y)};
      ow[e] = pk_bf16(vv.x, vv.y);
    }
  }
  store_h(2, ow);
  WAIT_V(0);
}

__device__ __forceinline__ void phase2(const u16* __restrict__ x, const u16* __restrict__ cre, const u16* __restrict__ cim,
                                       const u16* __restrict__ b2, char* __restrict__ ws, u16* __restrict__ out, char* lds, const Loader& ld, const unsigned long long tag) {
  const int tid = threadIdx.x, lane = tid & 63, wid = __builtin_amdgcn_readfirstlane(tid >> 6);
  const int b = blockIdx.x, xcd = b & 7, idx = b >> 3;
  const int c = xcd >> 1, rt = (xcd & 1) * 8 + (idx >> 2), j = idx & 3;

#define SB() __builtin_amdgcn_sched_barrier(0)

  if (wid >= 4) {
    gu64* f16 = (gu64*)(ws + WS_CNT) + ((4 * (c >> 1) + j) * 2 + (c & 1)) * 16 + (lane & 15);
    for (unsigned spins = 0;; ++spins) {
      const unsigned long long f = __hip_atomic_load(f16, __ATOMIC_RELAXED, __HIP_MEMORY_SCOPE_AGENT);
      if (__all(f == tag) || spins > (1u << 20)) break;
      __builtin_amdgcn_s_sleep(1);
    }
    asm volatile("" ::: "memory");
    ld.issue_b(0); ld.issue_b(1); ld.issue_b(2);
    for (int u = 0; u < 27; ++u) {
      if (u == 0) WAIT_V(8); else if (u == 1) WAIT_V(12); else WAIT_V(16);
      BAR();
      ld.issue_a(u + 3); ld.issue_b(u + 3);
    }
    WAIT_V(0);
    return;
  }

  const int r31 = lane & 31, hh = lane >> 5;
  const int wm = wid >> 1, wn = wid & 1;
  unsigned ab[4];
#pragma unroll
  for (int s = 0; s < 4; ++s) ab[s] = wm * 8192 + (unsigned)(r31 * 128 + (((2 * s + hh) ^ ((r31 >> 1) & 7)) * 16));
  const unsigned bb = 16384 + wn * 8192 + lane * 16;
  const unsigned ao = wm * 8192 + lane * 16;

  f32x16 acc[2][2] = {};
  f32x16 acco = {};
  bf16x8 F0[16], F1[16];
#define RD(F, s) do { F[4 * (s) + 0] = *(const bf16x8*)(sl + ab[s]); F[4 * (s) + 1] = *(const bf16x8*)(sl + ab[s] + 4096); \
                      F[4 * (s) + 2] = *(const bf16x8*)(sl + bb + (s) * 1024); F[4 * (s) + 3] = *(const bf16x8*)(sl + bb + (s) * 1024 + 4096); } while (0)
#define RDO(F, s) do { F[4 * (s) + 0] = *(const bf16x8*)(sl + ao + (s) * 2048); F[4 * (s) + 1] = *(const bf16x8*)(sl + bb + (s) * 2048); \
                       F[4 * (s) + 2] = *(const bf16x8*)(sl + ao + (s) * 2048 + 1024); F[4 * (s) + 3] = *(const bf16x8*)(sl + bb + (s) * 2048 + 1024); } while (0)
#define MM0(F, s) acc[0][0] = __builtin_amdgcn_mfma_f32_32x32x16_bf16(F[4 * (s) + 2], F[4 * (s) + 0], acc[0][0], 0, 0, 0)
#define MM1(F, s) acc[0][1] = __builtin_amdgcn_mfma_f32_32x32x16_bf16(F[4 * (s) + 3], F[4 * (s) + 0], acc[0][1], 0, 0, 0)
#define MM2(F, s) acc[1][0] = __builtin_amdgcn_mfma_f32_32x32x16_bf16(F[4 * (s) + 2], F[4 * (s) + 1], acc[1][0], 0, 0, 0)
#define MM3(F, s) acc[1][1] = __builtin_amdgcn_mfma_f32_32x32x16_bf16(F[4 * (s) + 3], F[4 * (s) + 1], acc[1][1], 0, 0, 0)
#define MO(F, s) do { acco = __builtin_amdgcn_mfma_f32_32x32x16_bf16(F[4 * (s) + 1], F[4 * (s) + 0], acco, 0, 0, 0); \
                      acco = __builtin_amdgcn_mfma_f32_32x32x16_bf16(F[4 * (s) + 3], F[4 * (s) + 2], acco, 0, 0, 0); } while (0)
#define PERIOD(u, RDX, FR, FM) do { \
    const char* sl = lds + ring_off(u); \
    BAR(); SB(); \
    MM0(FM, 0); SB(); RDX(FR, 0); SB(); MM1(FM, 0); SB(); MM2(FM, 0); SB(); RDX(FR, 1); SB(); MM3(FM, 0); SB(); \
    MM0(FM, 1); SB(); RDX(FR, 2); SB(); MM1(FM, 1); SB(); MM2(FM, 1); SB(); RDX(FR, 3); SB(); MM3(FM, 1); SB(); \
    MM0(FM, 2); MM1(FM, 2); MM2(FM, 2); MM3(FM, 2); \
    MM0(FM, 3); MM1(FM, 3); MM2(FM, 3); SB(); WAIT_L0(); SB(); MM3(FM, 3); SB(); \
  } while (0)
#define PERIODO(u, FR, FM) do { \
    const char* sl = lds + ring_off(u); \
    BAR(); SB(); \
    RDO(FR, 0); SB(); MO(FM, 0); SB(); RDO(FR, 1); SB(); MO(FM, 1); SB(); RDO(FR, 2); SB(); MO(FM, 2); SB(); RDO(FR, 3); SB(); MO(FM, 3); SB(); \
    WAIT_L0(); SB(); \
  } while (0)
  gu64* fl = (gu64*)(ws + WS_CNT + 2048);
  {
    const char* sl = lds;
    BAR(); SB();
    if (tid == 0) __hip_atomic_store(fl + blockIdx.x, tag, __ATOMIC_RELAXED, __HIP_MEMORY_SCOPE_AGENT);

    RD(F0, 0); RD(F0, 1); RD(F0, 2); RD(F0, 3); SB();
    WAIT_L0(); SB();
  }
  for (int u = 1; u < 15; u += 2) {
    PERIOD(u, RD, F1, F0);
    PERIOD(u + 1, RD, F0, F1);
    if (u == 11 && wid == 0) {
      for (unsigned spins = 0;; ++spins) {
        const unsigned long long f0 = __hip_atomic_load(fl + lane * 4, __ATOMIC_RELAXED, __HIP_MEMORY_SCOPE_AGENT), f1 = __hip_atomic_load(fl + lane * 4 + 1, __ATOMIC_RELAXED, __HIP_MEMORY_SCOPE_AGENT),
                                 f2 = __hip_atomic_load(fl + lane * 4 + 2, __ATOMIC_RELAXED, __HIP_MEMORY_SCOPE_AGENT), f3 = __hip_atomic_load(fl + lane * 4 + 3, __ATOMIC_RELAXED, __HIP_MEMORY_SCOPE_AGENT);
        if (__all(f0 == tag && f1 == tag && f2 == tag && f3 == tag) || spins > (1u << 20)) break;
        __builtin_amdgcn_s_sleep(1);
      }
      asm volatile("" ::: "memory");
    }
  }
  PERIOD(15, RD, F1, F0);
  PERIOD(16, RDO, F0, F1);
  const int tile = rt * 4 + j;
  const auto rs = __builtin_amdgcn_make_buffer_rsrc(ws + WS_SLAB + ((size_t)tile << 17), 0, 1 << 17, 0x00020000);
  const unsigned lo = wid * 2048 + lane * 16;
#pragma unroll
  for (int q = 0; q < 4; ++q) {
    if (q != c) {
      const f32x16 v = acc[q >> 1][q & 1];
#pragma unroll
      for (int hf = 0; hf < 2; ++hf) {
        const u32x4 w = {pk_bf16(v[hf * 8], v[hf * 8 + 1]), pk_bf16(v[hf * 8 + 2], v[hf * 8 + 3]), pk_bf16(v[hf * 8 + 4], v[hf * 8 + 5]), pk_bf16(v[hf * 8 + 6], v[hf * 8 + 7])};
        __builtin_amdgcn_raw_buffer_store_b128(w, rs, (q * 4 + c) * 8192 + lo + hf * 1024, 0, 16);
      }
    }
  }
  const f32x16 own = c == 0 ? acc[0][0] : (c == 1 ? acc[0][1] : (c == 2 ? acc[1][0] : acc[1][1]));
  const int trow = rt * 128 + wm * 64 + (c >> 1) * 32 + r31;
  const int ecol = j * 128 + wn * 64 + (c & 1) * 32 + 8 * hh;
  u32x4 xv[2], bv[2];
#pragma unroll
  for (int half = 0; half < 2; ++half) { xv[half] = *(const u32x4*)(x + (size_t)trow * D_ + ecol + 16 * half); bv[half] = *(const u32x4*)(b2 + ecol + 16 * half); }
  SB();
  gu64* xfl = (gu64*)(ws + WS_CNT + 4096 + tile * 128);
  u32x4 pv[4][2];
#pragma unroll
  for (int s = 0; s < 4; ++s) { pv[s][0] = (u32x4){}; pv[s][1] = (u32x4){}; }
#pragma unroll 1
  for (int u = 17; u < 27; u += 2) {
    PERIODO(u, F1, F0);
    if (u == 19) { WAIT_V(4); }
    if (u == 23) {
      asm volatile("" ::: "memory");
#pragma unroll
      for (int s = 0; s < 4; ++s)
#pragma unroll
        for (int hf = 0; hf < 2; ++hf)
          pv[s][hf] = __builtin_amdgcn_raw_buffer_load_b128(rs, (c * 4 + s) * 8192 + lo + hf * 1024, 0, 16);
      SB();
    }
    PERIODO(u + 1, F0, F1);
    if (u == 19 && tid == 0) __hip_atomic_store(xfl + c, tag, __ATOMIC_RELAXED, __HIP_MEMORY_SCOPE_AGENT);
    if (u == 21) {
      if (wid == 0) {
        for (unsigned spins = 0;; ++spins) {
          const unsigned long long f = __hip_atomic_load(xfl + (lane & 3), __ATOMIC_RELAXED, __HIP_MEMORY_SCOPE_AGENT);
          if (__all(f == tag) || spins > (1u << 20)) break;
          __builtin_amdgcn_s_sleep(1);
        }
      }
      asm volatile("" ::: "memory");
    }
  }
  MO(F0, 0); MO(F0, 1); MO(F0, 2); MO(F0, 3);
#pragma unroll
  for (int half = 0; half < 2; ++half) {
    float o[8];
#pragma unroll
    for (int e = 0; e < 8; ++e) {
      const int rg = half * 8 + e;
      float p[4];
#pragma unroll
      for (int s = 0; s < 4; ++s) { const unsigned w = pv[s][half][e >> 1]; p[s] = c == s ? own[rg] : ((e & 1) ? bfhi(w) : bflo(w)); }
      const float s = (p[0] + p[1]) + (p[2] + p[3]);
      const unsigned xw = xv[half][e >> 1], bw = bv[half][e >> 1];
      const float xx = (e & 1) ? bfhi(xw) : bflo(xw), bb2 = (e & 1) ? bfhi(bw) : bflo(bw);
      const float v = acco[rg] + bb2;
      const float g = v * __builtin_amdgcn_rcpf(1.f + __builtin_amdgcn_exp2f(-1.442695041f * v));
      o[e] = xx + s * g;
    }
    u32x4 ov = {pk_bf16(o[0], o[1]), pk_bf16(o[2], o[3]), pk_bf16(o[4], o[5]), pk_bf16(o[6], o[7])};
    *(u32x4*)(out + (size_t)trow * D_ + ecol + 16 * half) = ov;
  }
}

extern "C" __device__ unsigned long long amd_dispatch_id(void) __asm("llvm.amdgcn.dispatch.id");

__global__ __launch_bounds__(512, 2) void fused(const u16* __restrict__ x, const u16* __restrict__ cre, const u16* __restrict__ cim,
                                                const u16* __restrict__ wre, const u16* __restrict__ wim, const u16* __restrict__ w1,
                                                const u16* __restrict__ b1, const u16* __restrict__ w2, const u16* __restrict__ b2,
                                                char* __restrict__ ws, u16* __restrict__ out) {
  extern __shared__ __attribute__((aligned(16))) char lds[];
  const unsigned long long tag = ((amd_dispatch_id() + 1ull) * 0x9E3779B97F4A7C15ull) ^ ((unsigned long long)__builtin_amdgcn_dispatch_ptr() * 0xC2B2AE3D27D4EB4Full) ^ 0x5851F42D4C957F2Dull;
  Loader ld;
  {
    const int b = blockIdx.x, xcd = b & 7, idx = b >> 3;
    ld.init(cre, cim, ws, lds, __builtin_amdgcn_readfirstlane(threadIdx.x >> 6) & 3, threadIdx.x & 63, xcd >> 1, (xcd & 1) * 8 + (idx >> 2), idx & 3);
  }
  phase1(x, wre, wim, w1, b1, w2, ws, lds, ld, tag);
  phase2(x, cre, cim, b2, ws, out, lds, ld, tag);
}

extern "C" void kernel_launch(void* const* d_in, const int* in_sizes, int n_in, void* d_out, int out_size, void* d_ws, size_t ws_size,
                              hipStream_t stream) {
  (void)hipFuncSetAttribute((const void*)fused, hipFuncAttributeMaxDynamicSharedMemorySize, 163840);
  fused<<<256, 512, 163840, stream>>>((const u16*)d_in[0], (const u16*)d_in[1], (const u16*)d_in[2], (const u16*)d_in[3], (const u16*)d_in[4],
                                      (const u16*)d_in[6], (const u16*)d_in[7], (const u16*)d_in[8], (const u16*)d_in[9], (char*)d_ws, (u16*)d_out);
}
```

```cpp
#include <hip/hip_runtime.h>
#include <hip/hip_bf16.h>
#include <stdint.h>
#include <type_traits>

typedef unsigned short u16;
typedef short v4s __attribute__((ext_vector_type(4)));
typedef short bf16x8 __attribute__((ext_vector_type(8)));
typedef __bf16 v2bf __attribute__((ext_vector_type(2)));
typedef float f32x4 __attribute__((ext_vector_type(4)));
typedef float f32x16 __attribute__((ext_vector_type(16)));
typedef unsigned u32x4 __attribute__((ext_vector_type(4)));
typedef unsigned u32x2 __attribute__((ext_vector_type(2)));
#define LAS __attribute__((address_space(3)))

constexpr int S_ = 2048, D_ = 512, F_ = 1376;
constexpr int NKK_B = 64;
constexpr int NSUB = 88;
constexpr size_t WS_B    = 0;
constexpr size_t WS_W2   = 4u << 20;
constexpr size_t WS_H    = 8u << 20;
constexpr size_t WS_SLAB = 16u << 20;
constexpr size_t WS_CNT  = 40u << 20;

__device__ __forceinline__ float bflo(unsigned v) { return __uint_as_float(v << 16); }
__device__ __forceinline__ float bfhi(unsigned v) { return __uint_as_float(v & 0xffff0000u); }
__device__ __forceinline__ unsigned pk_bf16(float lo, float hi) {
  typedef float f32x2_ __attribute__((ext_vector_type(2)));
  v2bf r = __builtin_convertvector((f32x2_){lo, hi}, v2bf);
  return __builtin_bit_cast(unsigned, r);
}
__device__ __forceinline__ float rbf(float f) { return bflo(pk_bf16(f, 0.f)); }
__device__ __forceinline__ int swap23(int r) { return (r & ~12) | ((r & 4) << 1) | ((r & 8) >> 1); }
typedef int i32x4 __attribute__((ext_vector_type(4)));
__device__ __forceinline__ i32x4 mk_rsrc(const void* p) {
  const unsigned long long a = (unsigned long long)p;
  return (i32x4){(int)(unsigned)a, (int)((unsigned)(a >> 32) & 0xffffu), 0x7fffffff, 0x00020000};
}
__device__ __forceinline__ void dma16(i32x4 rsrc, unsigned lds_dst, int voff, int soff) {
  unsigned keep;
  asm volatile("s_mov_b32 %0, m0\n\ts_mov_b32 m0, %1\n\ts_nop 3\n\tbuffer_load_dwordx4 %2, %3, %4 offen lds\n\ts_mov_b32 m0, %0"
               : "=&s"(keep) : "s"(lds_dst), "v"(voff), "s"(rsrc), "s"(soff) : "memory");
}
__device__ __forceinline__ void touch4(i32x4 rsrc, unsigned lds_dst, int voff, int soff) {
  unsigned keep;
  asm volatile("s_mov_b32 %0, m0\n\ts_mov_b32 m0, %1\n\ts_nop 3\n\tbuffer_load_dword %2, %3, %4 offen lds\n\ts_mov_b32 m0, %0"
               : "=&s"(keep) : "s"(lds_dst), "v"(voff), "s"(rsrc), "s"(soff) : "memory");
}
template <int OFF> __device__ __forceinline__ void dma16x_sc1(i32x4 rsrc, unsigned lds_dst, int voff, int soff) {
  unsigned keep;
  asm volatile("s_mov_b32 %0, m0\n\ts_mov_b32 m0, %1\n\ts_nop 3\n\tbuffer_load_dwordx4 %2, %3, %4 offen offset:%5 sc1 lds\n\ts_mov_b32 m0, %0"
               : "=&s"(keep) : "s"(lds_dst), "v"(voff), "s"(rsrc), "s"(soff), "n"(OFF) : "memory");
}
template <int OFF> __device__ __forceinline__ void dma16x_nt(i32x4 rsrc, unsigned lds_dst, int voff, int soff) {
  unsigned keep;
  asm volatile("s_mov_b32 %0, m0\n\ts_mov_b32 m0, %1\n\ts_nop 3\n\tbuffer_load_dwordx4 %2, %3, %4 offen offset:%5 nt lds\n\ts_mov_b32 m0, %0"
               : "=&s"(keep) : "s"(lds_dst), "v"(voff), "s"(rsrc), "s"(soff), "n"(OFF) : "memory");
}
#define WAIT_V(n) asm volatile("s_waitcnt vmcnt(%0)" ::"n"(n) : "memory")
#define WAIT_L0() asm volatile("s_waitcnt lgkmcnt(0)" ::: "memory")
#define BAR() asm volatile("s_barrier" ::: "memory")
constexpr int AUX_ST = 16;

typedef float f32x2 __attribute__((ext_vector_type(2)));
constexpr int SA0 = 0, SA1 = 33280, SH1 = 98304, SLP = 131072, AROW = 1040;
constexpr int SCR = 66560;
constexpr int SC_PART = SCR;
constexpr int SC_RS   = SCR + 3072;
constexpr int SC_MRS  = SCR + 3584;
constexpr int SC_CS   = SCR + 4096;
constexpr int SC_B1   = SCR + 7168;
constexpr int SC_DUM  = SCR + 8192;
#define PIN(x) asm volatile("" : "+v"(x))
#define SBAR() __builtin_amdgcn_sched_barrier(0)

template <int K> __device__ __forceinline__ void eh_op(const f32x16& ap, float (&v)[16], float (&t)[16], unsigned (&ow)[8], float scale, float center) {
  constexpr int st = K / 16, e = K % 16;
  if constexpr (st == 0) { v[e] = fmaf(-center, t[e], v[e]); PIN(v[e]); }
  else if constexpr (st == 1) { v[e] = fmaf(ap[e], scale, v[e]); PIN(v[e]); }
  else if constexpr (st == 2) { t[e] = v[e] * v[e]; PIN(t[e]); }
  else if constexpr (st == 3) { t[e] = fmaf(t[e], -0.1029432397f, -2.302208198f); PIN(t[e]); }
  else if constexpr (st == 4) { t[e] = t[e] * v[e]; PIN(t[e]); }
  else if constexpr (st == 5) { t[e] = __builtin_amdgcn_exp2f(t[e]); PIN(t[e]); }
  else if constexpr (st == 6) { t[e] = 1.f + t[e]; PIN(t[e]); }
  else if constexpr (st == 7) { t[e] = __builtin_amdgcn_rcpf(t[e]); PIN(t[e]); }
  else if constexpr (st == 8) { v[e] = v[e] * t[e]; PIN(v[e]); }
  else if constexpr (K < 152) { ow[e] = pk_bf16(v[2 * e], v[2 * e + 1]); PIN(ow[e]); }
}
template <int K> __device__ __forceinline__ void ea_op(const f32x16& ap, float (&v)[16], float (&t)[16], unsigned (&ow)[8], float cs) {
  constexpr int st = K / 16, e = K % 16;
  if constexpr (st == 0) { t[e] = ap[e] * t[e]; PIN(t[e]); }
  else if constexpr (st == 1) { t[e] = fmaf(-v[e], cs, t[e]); PIN(t[e]); }
  else if constexpr (K < 40) { ow[e] = pk_bf16(t[2 * e], t[2 * e + 1]); PIN(ow[e]); }
}
template <int ET, int K0, int K1> __device__ __forceinline__ void e_ops(const f32x16& ap, float (&v)[16], float (&t)[16], unsigned (&ow)[8], float s0, float s1) {
  if constexpr (K0 < K1) {
    if constexpr (ET == 1) eh_op<K0>(ap, v, t, ow, s0, s1); else if constexpr (ET == 2) ea_op<K0>(ap, v, t, ow, s0);
    e_ops<ET, K0 + 1, K1>(ap, v, t, ow, s0, s1);
  }
}
template <int MT> __device__ __forceinline__ bf16x8 wread(const char* pan, int ks) {
  if constexpr (MT == 1) { return *(const bf16x8*)(pan + ks * 32); }
  else {
    v4s t0 = __builtin_amdgcn_ds_read_tr16_b64_v4i16((LAS v4s*)(pan + ks * 1024));
    v4s t1 = __builtin_amdgcn_ds_read_tr16_b64_v4i16((LAS v4s*)(pan + ks * 1024 + 256));
    return (bf16x8)__builtin_shufflevector(t0, t1, 0, 1, 2, 3, 4, 5, 6, 7);
  }
}
__device__ __forceinline__ void row_stats(float sum, float sq, float& mean, float& inv, float& m_bf, float& scale, float& center) {
  mean = sum * (1.f / 512.f);
  const float var = fmaxf(sq * (1.f / 512.f) - mean * mean, 0.f);
  m_bf = rbf(mean);
  const float sd = rbf(sqrtf(rbf(rbf(var) + 1e-5f)));
  inv = 1.f / sd;
  const float m2 = rbf((mean - m_bf) * inv);
  const float v2 = rbf(var * inv * inv);
  const float sd2 = rbf(sqrtf(rbf(v2 + 1e-5f)));
  scale = inv / sd2; center = (m_bf + m2 * sd) * scale;
}
constexpr int WR = 8, WD = 3;
struct RowStat { float mean, var, m_bf, sd, inv, m2, v2, sd2, scale, center; };
template <int S> __device__ __forceinline__ void rs_stage(RowStat& r) {
  if constexpr (S == 0) { r.mean *= (1.f / 512.f); r.var = fmaxf(r.var * (1.f / 512.f) - r.mean * r.mean, 0.f); r.m_bf = rbf(r.mean); PIN(r.var); PIN(r.m_bf); }
  else if constexpr (S == 1) { r.sd = rbf(rbf(r.var) + 1e-5f); PIN(r.sd); }
  else if constexpr (S == 2) { r.sd = rbf(sqrtf(r.sd)); PIN(r.sd); }
  else if constexpr (S == 3) { r.inv = 1.f / r.sd; PIN(r.inv); }
  else if constexpr (S == 4) { r.m2 = rbf((r.mean - r.m_bf) * r.inv); r.v2 = rbf(rbf(r.var * r.inv * r.inv) + 1e-5f); PIN(r.m2); PIN(r.v2); }
  else if constexpr (S == 5) { r.sd2 = rbf(sqrtf(r.v2)); PIN(r.sd2); }
  else if constexpr (S == 6) { r.scale = r.inv / r.sd2; PIN(r.scale); }
  else if constexpr (S == 7) { r.center = (r.m_bf + r.m2 * r.sd) * r.scale; PIN(r.center); }
}
template <int MT, int ET, int NT, int G0, int KS, class Hook>
__device__ __forceinline__ void unit_steps(const char* pan, const char* pnx, const bf16x8 (&xf)[32], bf16x8 (&wf)[WR], f32x16& aw, const f32x16& ar,
                                           float (&v)[16], float (&t)[16], unsigned (&ow)[8], const float& s0, const float& s1, Hook&& hook) {
  if constexpr (KS < 32) {
    constexpr int NOPS = ET == 1 ? 152 : 40;
    if constexpr (KS == 0) {
      if constexpr (MT == 1) aw = __builtin_amdgcn_mfma_f32_32x32x16_bf16(xf[KS], wf[KS % WR], (f32x16){}, 0, 0, 0);
      else aw = __builtin_amdgcn_mfma_f32_32x32x16_bf16(wf[KS % WR], xf[KS], (f32x16){}, 0, 0, 0);
    } else {
      if constexpr (MT == 1) aw = __builtin_amdgcn_mfma_f32_32x32x16_bf16(xf[KS], wf[KS % WR], aw, 0, 0, 0);
      else aw = __builtin_amdgcn_mfma_f32_32x32x16_bf16(wf[KS % WR], xf[KS], aw, 0, 0, 0);
    }
    PIN(aw);
    SBAR();
    if constexpr (KS + WD < 32) wf[(KS + WD) % WR] = wread<MT>(pan, KS + WD);
    else if constexpr (NT >= 0) wf[(KS + WD) % WR] = wread<NT>(pnx, KS + WD - 32);
    asm volatile("" :: "v"(wf[KS % WR]), "v"(wf[(KS + WR - 1) % WR]));
    SBAR();
    hook(std::integral_constant<int, KS>{});
    constexpr int k0 = KS < G0 ? 0 : (KS - G0) * NOPS / (32 - G0), k1 = KS < G0 ? 0 : (KS + 1 - G0) * NOPS / (32 - G0);
    e_ops<ET, k0, k1>(ar, v, t, ow, s0, s1);
    SBAR();
    unit_steps<MT, ET, NT, G0, KS + 1>(pan, pnx, xf, wf, aw, ar, v, t, ow, s0, s1, hook);
  }
}

typedef __attribute__((address_space(1))) unsigned long long gu64;
__device__ __forceinline__ unsigned ring_off(int t) { return (t & 1) * 32768 + (t & 2) * 49152; }
struct Loader {
  i32x4 ra_s, rb_s, ra_o, rb_o; int aoff[4], voff; unsigned ldsw;
  __device__ __forceinline__ void init(const u16* cre, const u16* cim, char* ws, char* lds, int lw, int lane, int c, int rt, int j) {
#pragma unroll
    for (int i = 0; i < 4; ++i) { const int row = lw * 32 + i * 8 + (lane >> 3); aoff[i] = row * 4096 + (((lane & 7) ^ ((row >> 1) & 7)) * 16) - i * 1024 + 4096; }
    ldsw = (unsigned)(size_t)(LAS char*)lds + lw * 4096;
    ra_s = mk_rsrc((const char*)(c < 2 ? cre : cim) + (size_t)rt * 128 * 4096 + (c & 1) * 2048 - 4096);
    rb_s = mk_rsrc(ws + WS_B + ((size_t)(j * NKK_B + c * 16) << 14) + lw * 4096);
    ra_o = mk_rsrc(ws + WS_H + ((size_t)((rt * 4 + 2 * (lw >> 1) + (c >> 1)) * NSUB) << 10) + (lw & 1) * 4096 - (16 << 13));
    rb_o = mk_rsrc(ws + WS_W2 + ((size_t)((j * 4 + 2 * (lw >> 1) + (c & 1)) * NSUB) << 10) + (lw & 1) * 4096 - (16 << 13));
    voff = lane * 16;
  }
  __device__ __forceinline__ void issue_a(int T) const {
    const unsigned dst = __builtin_amdgcn_readfirstlane(ldsw + ring_off(T));
    if (T < 16) { const int so = __builtin_amdgcn_readfirstlane(T * 128);
                  dma16x_nt<0>(ra_s, dst, aoff[0], so); dma16x_nt<1024>(ra_s, dst, aoff[1], so); dma16x_nt<2048>(ra_s, dst, aoff[2], so); dma16x_nt<3072>(ra_s, dst, aoff[3], so); }
    else { i32x4 r = ra_o; r.z = __builtin_amdgcn_readfirstlane(T < 27 ? 0x7fffffff : 0); const int so = __builtin_amdgcn_readfirstlane(T << 13);
           dma16x_sc1<0>(r, dst, voff, so); dma16x_sc1<1024>(r, dst, voff, so); dma16x_sc1<2048>(r, dst, voff, so); dma16x_sc1<3072>(r, dst, voff, so); }
  }
  __device__ __forceinline__ void issue_b(int T) const {
    const unsigned dst = __builtin_amdgcn_readfirstlane(ldsw + ring_off(T) + 16384);
    i32x4 r = T < 16 ? rb_s : rb_o; r.z = T < 27 ? 0x7fffffff : 0;
    const int so = __builtin_amdgcn_readfirstlane(T < 16 ? T << 14 : T << 13);
    dma16x_sc1<0>(r, dst, voff, so); dma16x_sc1<1024>(r, dst, voff, so); dma16x_sc1<2048>(r, dst, voff, so); dma16x_sc1<3072>(r, dst, voff, so);
  }
};

__device__ __forceinline__ void phase1(const u16* __restrict__ x, const u16* __restrict__ wre, const u16* __restrict__ wim,
                                       const u16* __restrict__ w1, const u16* __restrict__ b1, const u16* __restrict__ w2,
                                       char* __restrict__ ws, char* lds, const Loader& ld, const unsigned long long tag) {
  const int tid = threadIdx.x, lane = tid & 63, wid = __builtin_amdgcn_readfirstlane(tid >> 6);
  const int b = blockIdx.x, xcd = b & 7, idx = b >> 3;
  const int rt = (xcd >> 1) * 4 + (idx & 3);
  const int cg = (xcd & 1) * 8 + (idx >> 2);
  const int h0 = cg < 11 ? cg * 3 : 33 + (cg - 11) * 2;
  const bool h3 = cg < 11;
  const int a0 = cg * 2;
  const int m = wid & 3;
  const bool helper = wid >= 4;
  const int r31 = lane & 31, hh = lane >> 5;
  const unsigned lds0 = (unsigned)(size_t)(LAS char*)lds;
  const int prot = idx & 3;

  const int hoff = (lane >> 2) * (F_ * 2) + (lane & 3) * 16;
  const i32x4 rw1 = mk_rsrc(w1);
  auto piece_h = [&](int u, int q, int slot_off) { dma16(rw1, lds0 + slot_off + q * 1024, hoff, q * 16 * (F_ * 2) + u * 64); };
  const i32x4 rx = mk_rsrc(x + (size_t)(rt * 128 + m * 32) * D_);
  int xo[8], xs[8];
#pragma unroll
  for (int i = 0; i < 8; ++i) {
    const int ii = (i + (idx >> 2)) & 7, r = (ii & 3) * 4 + (lane >> 4);
    xo[i] = r * 1024 + (((lane & 15) ^ r) * 16); xs[i] = ii;
  }
  auto quarter = [&](int q) {
#pragma unroll
    for (int i = 0; i < 8; ++i) dma16(rx, lds0 + q * 32768 + m * 8192 + xs[i] * 1024, xo[i], q * 256 + (xs[i] >> 2) * 16384);
    piece_h(h0, q * 8 + ((m * 2 + 2 * prot) & 7), SLP); piece_h(h0, q * 8 + ((m * 2 + 1 + 2 * prot) & 7), SLP);
  };
  if (helper) { quarter(0); quarter(1); }

  const auto rws = __builtin_amdgcn_make_buffer_rsrc((void*)ws, 0, 0x7fffffff, 0x00020000);
  const int xrow = m * 32 + swap23(r31);
  const unsigned xbase = xrow * 256 + ((hh ^ (xrow & 15)) * 16);

  if (helper) {
    const auto rw2t = __builtin_amdgcn_make_buffer_rsrc((void*)(ws + WS_W2), 0, 16 * NSUB * 1024, 0x00020000);
    const i32x4 rwa = mk_rsrc((a0 < 16 ? wre : wim) + (size_t)(a0 & 15) * 32 * D_);
    auto panel_h = [&](int u, int slot_off) {
#pragma unroll
      for (int k = 0; k < 8; ++k) piece_h(u, m * 8 + ((k + 2 * prot) & 7), slot_off);
    };
    auto panel_a = [&](int w, int slot_off) {
#pragma unroll
      for (int k = 0; k < 8; ++k) { const int q = m * 8 + ((k + 2 * prot) & 7); dma16(rwa, lds0 + slot_off + q * AROW, lane * 16, (w * 32 + q) * 1024); }
    };
    const v4s ones = {0x3f80, 0x3f80, 0x3f80, 0x3f80};
    const char* pzh = lds + ((lane >> 2) & 15) * 64 + (m & 1) * 32 + (lane & 3) * 8;
    auto cs_h_part = [&](f32x4 (&c)[4], int slot_off, int j0, int n) {
#pragma unroll
      for (int j = 0; j < n; ++j)
        c[j & 3] = __builtin_amdgcn_mfma_f32_4x4x4bf16_1k(ones, __builtin_amdgcn_ds_read_tr16_b64_v4i16((LAS v4s*)(pzh + slot_off + (j0 + j) * 1024)), c[j & 3], 0, 0, 0);
    };
    auto cs_h_fin = [&](f32x4 (&c)[4], int i) {
      float r = (c[0][0] + c[1][0]) + (c[2][0] + c[3][0]);
      r += __shfl_xor(r, 16); r += __shfl_xor(r, 32);
      if (lane < 16) *(float*)(lds + SC_CS + i * 512 + (m & 1) * 64 + lane * 4) = r;
    };
    auto cs_h = [&](int i, int slot_off) {
      if ((m >> 1) == (i & 1)) { f32x4 c[4] = {}; cs_h_part(c, slot_off, 0, 32); cs_h_fin(c, i); }
    };
    auto cs_a = [&](int i, int slot_off) {
      const char* pz = lds + slot_off + swap23(r31) * AROW + hh * 16 + m * 256;
      f32x4 c0 = {}, c1 = {};
#pragma unroll
      for (int j = 0; j < 8; ++j) {
        const bf16x8 w = *(const bf16x8*)(pz + j * 32);
        c0 = __builtin_amdgcn_mfma_f32_4x4x4bf16_1k(ones, __builtin_shufflevector(w, w, 0, 1, 2, 3), c0, 0, 0, 0);
        c1 = __builtin_amdgcn_mfma_f32_4x4x4bf16_1k(ones, __builtin_shufflevector(w, w, 4, 5, 6, 7), c1, 0, 0, 0);
      }
      float r = c0[0] + c1[0]; r += __shfl_xor(r, 32);
      if (lane < 32) *(float*)(lds + SC_CS + i * 512 + m * 128 + lane * 4) = r;
    };
    __builtin_amdgcn_s_setprio(2);
    {
      f32x4 c[4] = {};
      WAIT_V(10); BAR();
      quarter(2);
      if (m < 2) cs_h_part(c, SLP, 0, 8);
      WAIT_V(10); BAR();
      quarter(3);
      if (m < 2) cs_h_part(c, SLP, 8, 8);
      WAIT_V(10); BAR();
      panel_a(0, SA0);
      unsigned w2v[2][8], b1v;
      int w2dst[2];
      {
        i32x4 rw2 = mk_rsrc(w2); rw2.z = F_ * D_ * 2;
#pragma unroll
        for (int r = 0; r < 2; ++r) {
          const int hl = r * 256 + (tid - 256), cc = b * 352 + hl;
          const int ln = cc & 63, rest = cc >> 6, ct = rest / NSUB, sub = rest - ct * NSUB;
          const int off = hl < 352 ? ((16 * sub + 4 * (ln >> 5)) * D_ + 32 * ct + swap23(ln & 31)) * 2 : 0x7ff00000;
          w2dst[r] = hl < 352 ? cc * 16 : 0x7ffffff0;
          asm volatile("buffer_load_ushort %0, %8, %10, 0 offen\n\tbuffer_load_ushort %1, %8, %10, 0 offen offset:1024\n\t"
                       "buffer_load_ushort %2, %8, %10, 0 offen offset:2048\n\tbuffer_load_ushort %3, %8, %10, 0 offen offset:3072\n\t"
                       "buffer_load_ushort %4, %9, %10, 0 offen\n\tbuffer_load_ushort %5, %9, %10, 0 offen offset:1024\n\t"
                       "buffer_load_ushort %6, %9, %10, 0 offen offset:2048\n\tbuffer_load_ushort %7, %9, %10, 0 offen offset:3072"
                       : "=&v"(w2v[r][0]), "=&v"(w2v[r][1]), "=&v"(w2v[r][2]), "=&v"(w2v[r][3]), "=&v"(w2v[r][4]), "=&v"(w2v[r][5]), "=&v"(w2v[r][6]), "=&v"(w2v[r][7])
                       : "v"(off), "v"(off + 8192), "s"(rw2) : "memory");
        }
        const int bo = h0 * 32 + lane * 2;
        asm volatile("buffer_load_dword %0, %1, %2, 0 offen" : "=&v"(b1v) : "v"((bo < F_ - 2 ? bo : F_ - 2) * 2), "s"(mk_rsrc(b1)) : "memory");
      }
      if (m < 2) cs_h_part(c, SLP, 16, 8);
      WAIT_V(25); BAR();
      panel_a(1, SA1);
      if (m < 2) { cs_h_part(c, SLP, 24, 8); cs_h_fin(c, 0); }
      WAIT_V(8);
      asm volatile("" : "+v"(w2v[0][0]), "+v"(w2v[0][1]), "+v"(w2v[0][2]), "+v"(w2v[0][3]), "+v"(w2v[0][4]), "+v"(w2v[0][5]), "+v"(w2v[0][6]), "+v"(w2v[0][7]), "+v"(b1v));
      asm volatile("" : "+v"(w2v[1][0]), "+v"(w2v[1][1]), "+v"(w2v[1][2]), "+v"(w2v[1][3]), "+v"(w2v[1][4]), "+v"(w2v[1][5]), "+v"(w2v[1][6]), "+v"(w2v[1][7]));
      if (m == 3 && lane < 48) *(f32x2*)(lds + SC_B1 + lane * 8) = (f32x2){bflo(b1v), bfhi(b1v)};
#pragma unroll
      for (int r = 0; r < 2; ++r) {
        const u32x4 w = {w2v[r][0] | (w2v[r][1] << 16), w2v[r][2] | (w2v[r][3] << 16), w2v[r][4] | (w2v[r][5] << 16), w2v[r][6] | (w2v[r][7] << 16)};
        __builtin_amdgcn_raw_buffer_store_b128(w, rw2t, w2dst[r], 0, AUX_ST);
      }
    }
    WAIT_V(2); WAIT_L0(); BAR();
    panel_h(h0 + 1, SH1);
    panel_h(h3 ? h0 + 2 : h0, SLP);
    {
      const int j2 = idx & 3;
      const int st1 = 3 + 2 * m + hh, st2 = st1 + 8 < 15 ? st1 + 8 : 15;
      const int vb = (j2 * 32 + r31) * 4096 + 4096;
      touch4(ld.ra_s, lds0 + SC_DUM + m * 256, vb + st1 * 128, 0);
    }
    cs_a(1, SA0);
    {
      float mean, inv, m_bf, sc_, ce_;
      const f32x2 pr = *(const f32x2*)(lds + SC_PART + (m * 32 + r31) * 8);
      row_stats(pr.x, pr.y, mean, inv, m_bf, sc_, ce_);
      const float sg = a0 < 16 ? inv : -inv;
      if (lane < 32) { *(float*)(lds + SC_RS + (m * 32 + lane) * 4) = sg; *(float*)(lds + SC_MRS + (m * 32 + lane) * 4) = m_bf * sg; }
    }
    WAIT_V(9); WAIT_L0(); BAR();
    cs_a(2, SA1);
    WAIT_V(1);
    {
      const int j2 = idx & 3;
      const int st1 = 3 + 2 * m + hh, st2 = st1 + 8 < 15 ? st1 + 8 : 15;
      const int vb = (j2 * 32 + r31) * 4096 + 4096;
      touch4(ld.ra_s, lds0 + SC_DUM + m * 256, vb + st2 * 128, 0);
    }
    WAIT_L0(); BAR();
    cs_h(3, SH1);
    ld.issue_a(0); ld.issue_a(1);
    WAIT_L0(); BAR();
    cs_h(4, SLP);
    ld.issue_a(2);
    WAIT_L0(); BAR();
    if (tid == 256) __hip_atomic_store((gu64*)(ws + WS_CNT) + ((cg >> 1) * 2 + (rt >> 3)) * 16 + (rt & 7) * 2 + (cg & 1), tag, __ATOMIC_RELAXED, __HIP_MEMORY_SCOPE_AGENT);
    BAR();
    __builtin_amdgcn_s_setprio(0);
    return;
  }

  bf16x8 xf[32];
  const int wrow = swap23(r31);
  const unsigned wlA = (unsigned)(wrow * AROW + hh * 16);
  const unsigned wlH = (unsigned)((8 * hh + ((lane & 15) >> 2)) * 64 + ((lane >> 4) & 1) * 32 + (lane & 3) * 8);
  f32x16 acc, ac2;
  bf16x8 wf[WR];
  float v[16], t[16]; unsigned ow[8];
  auto nohook = [](auto) {};
  RowStat rs;
  const int hstore = (swap23(r31) + 32 * hh) * 16;
  const int s0r = rt * 128 + m * 32;

  auto tables_h = [&](int i, int hu) {
#pragma unroll
    for (int g4 = 0; g4 < 4; ++g4) {
      const f32x4 c = *(const f32x4*)(lds + SC_CS + i * 512 + (8 * g4 + 4 * hh) * 4), bq = *(const f32x4*)(lds + SC_B1 + hu * 128 + (8 * g4 + 4 * hh) * 4);
#pragma unroll
      for (int e = 0; e < 4; ++e) { t[g4 * 4 + e] = c[e]; v[g4 * 4 + e] = bq[e]; }
    }
  };
  auto tables_a = [&](int i) -> float {
#pragma unroll
    for (int g4 = 0; g4 < 4; ++g4) {
      const f32x4 c = *(const f32x4*)(lds + SC_RS + (m * 32 + 8 * g4 + 4 * hh) * 4), bq = *(const f32x4*)(lds + SC_MRS + (m * 32 + 8 * g4 + 4 * hh) * 4);
#pragma unroll
      for (int e = 0; e < 4; ++e) { t[g4 * 4 + e] = c[e]; v[g4 * 4 + e] = bq[e]; }
    }
    const char* cq = lds + SC_CS + i * 512 + r31 * 4;
    return (*(const float*)cq + *(const float*)(cq + 128)) + (*(const float*)(cq + 256) + *(const float*)(cq + 384));
  };
  auto tables_a4 = [&](int i, float (&cp)[4]) {
#pragma unroll
    for (int g4 = 0; g4 < 4; ++g4) {
      const f32x4 c = *(const f32x4*)(lds + SC_RS + (m * 32 + 8 * g4 + 4 * hh) * 4), bq = *(const f32x4*)(lds + SC_MRS + (m * 32 + 8 * g4 + 4 * hh) * 4);
#pragma unroll
      for (int e = 0; e < 4; ++e) { t[g4 * 4 + e] = c[e]; v[g4 * 4 + e] = bq[e]; }
      cp[g4] = *(const float*)(lds + SC_CS + i * 512 + g4 * 128 + r31 * 4);
    }
  };
  auto store_h = [&](int hu, const unsigned (&ow)[8]) {
    const int dst = (int)WS_H + (((rt * 4 + m) * NSUB + 2 * (h0 + hu)) << 10);
    if (hu < 2 || h3) {
      __builtin_amdgcn_raw_buffer_store_b128((u32x4){ow[0], ow[1], ow[2], ow[3]}, rws, hstore, dst, AUX_ST);
      __builtin_amdgcn_raw_buffer_store_b128((u32x4){ow[4], ow[5], ow[6], ow[7]}, rws, hstore + 1024, dst, AUX_ST);
    }
    if (h0 + hu == 42 && (hu < 2 || h3)) { u32x4 z = {0u, 0u, 0u, 0u}; __builtin_amdgcn_raw_buffer_store_b128(z, rws, lane * 16 + 2048, dst, AUX_ST); __builtin_amdgcn_raw_buffer_store_b128(z, rws, lane * 16 + 3072, dst, AUX_ST); }
  };
  auto store_a = [&](int w, const unsigned (&ow)[8]) {
    const int u = a0 + w, et = u & 15, j = et >> 2, nt = et & 3;
    const int kk = (u >> 4) * 32 + (s0r >> 6), sub = (s0r & 63) >> 4;
    const int dst = (int)WS_B + (((j * NKK_B + kk) * 16 + nt * 4 + sub) << 10);
    __builtin_amdgcn_raw_buffer_store_b128((u32x4){ow[0], ow[1], ow[2], ow[3]}, rws, lane * 16, dst, AUX_ST);
    __builtin_amdgcn_raw_buffer_store_b128((u32x4){ow[4], ow[5], ow[6], ow[7]}, rws, lane * 16 + 1024, dst, AUX_ST);
  };
  {
    f32x4 ss0 = {}, ss1 = {}, sq0 = {}, sq1 = {};
    const v4s ones4 = {0x3f80, 0x3f80, 0x3f80, 0x3f80};
    acc = (f32x16){};
    const char* pan = lds + SLP + wlH;
#pragma unroll
    for (int q = 0; q < 4; ++q) {
      BAR();
      bf16x8 w8[8];
#pragma unroll
      for (int k = 0; k < 8; ++k) xf[q * 8 + k] = *(const bf16x8*)(lds + q * 32768 + (xbase ^ (k * 32)));
#pragma unroll
      for (int k = 0; k < 8; ++k) w8[k] = wread<0>(pan, q * 8 + k);
      SBAR();
#pragma unroll
      for (int k = 0; k < 8; ++k) {
        const v4s lo4 = __builtin_shufflevector(xf[q * 8 + k], xf[q * 8 + k], 0, 1, 2, 3), hi4 = __builtin_shufflevector(xf[q * 8 + k], xf[q * 8 + k], 4, 5, 6, 7);
        ss0 = __builtin_amdgcn_mfma_f32_4x4x4bf16_1k(ones4, lo4, ss0, 0, 0, 0); sq0 = __builtin_amdgcn_mfma_f32_4x4x4bf16_1k(lo4, lo4, sq0, 0, 0, 0);
        ss1 = __builtin_amdgcn_mfma_f32_4x4x4bf16_1k(ones4, hi4, ss1, 0, 0, 0); sq1 = __builtin_amdgcn_mfma_f32_4x4x4bf16_1k(hi4, hi4, sq1, 0, 0, 0);
      }
      asm volatile("" : "+v"(ss0), "+v"(ss1), "+v"(sq0), "+v"(sq1));
      SBAR();
#pragma unroll
      for (int k = 0; k < 8; ++k) { acc = __builtin_amdgcn_mfma_f32_32x32x16_bf16(w8[k], xf[q * 8 + k], acc, 0, 0, 0); PIN(acc); }
      WAIT_L0(); SBAR();
    }
    const f32x4 sqv = sq0 + sq1;
    float sum = ss0[0] + ss1[0], sq = (lane & 2) ? ((lane & 1) ? sqv[3] : sqv[2]) : ((lane & 1) ? sqv[1] : sqv[0]);
    sum += __shfl_xor(sum, 32); sq += __shfl_xor(sq, 32);
    if (lane < 32) *(f32x2*)(lds + SC_PART + (m * 32 + lane) * 8) = (f32x2){sum, sq};
    rs.mean = sum; rs.var = sq;
  }
  WAIT_L0(); BAR();
  const char* pA0 = lds + SA0 + wlA; const char* pA1 = lds + SA1 + wlA;
  const char* pH1 = lds + SH1 + wlH; const char* pH2 = lds + SLP + wlH;
#pragma unroll
  for (int e = 0; e < WD; ++e) wf[e] = wread<1>(pA0, e);
  wf[WR - 1] = wf[0];
  tables_h(0, 0);
  float csa = 0.f, cp[4];
  auto cs_hook = [&](auto g) { if constexpr (decltype(g)::value == 2) { csa = (cp[0] + cp[1]); PIN(csa); float c2 = cp[2] + cp[3]; PIN(c2); csa += c2; PIN(csa); } };
  unit_steps<1, 1, 1, 8, 0>(pA0, pA1, xf, wf, ac2, acc, v, t, ow, rs.scale, rs.center, [&](auto g) {
    constexpr int G = decltype(g)::value;
    if constexpr (G < 8) rs_stage<G>(rs);
  });
  store_h(0, ow);
  BAR();
  tables_a4(1, cp);
  unit_steps<1, 2, 0, 3, 0>(pA1, pH1, xf, wf, acc, ac2, v, t, ow, csa, csa, cs_hook); store_a(0, ow);
  BAR();
  tables_a4(2, cp);
  unit_steps<0, 2, 0, 3, 0>(pH1, pH2, xf, wf, ac2, acc, v, t, ow, csa, csa, cs_hook); store_a(1, ow);
  BAR();
  tables_h(3, 1);
  unit_steps<0, 1, -1, 3, 0>(pH2, pH2, xf, wf, acc, ac2, v, t, ow, rs.scale, rs.center, [&](auto g) {
    if constexpr (decltype(g)::value == 14) { WAIT_V(0); BAR(); }
  });
  store_h(1, ow);
  BAR();
  tables_h(4, 2);
  {
#pragma unroll
    for (int e = 0; e < 8; ++e) {
      f32x2 vv = (f32x2){v[2 * e], v[2 * e + 1]} - (f32x2){t[2 * e], t[2 * e + 1]} * rs.center;
      vv = (f32x2){acc[2 * e], acc[2 * e + 1]} * rs.scale + vv;
      const f32x2 tt = ((vv * vv) * (-0.1029432397f) + (-2.302208198f)) * vv;
      const f32x2 d = (f32x2){__builtin_amdgcn_exp2f(tt.x), __builtin_amdgcn_exp2f(tt.y)} + 1.f;
      vv = vv * (f32x2){__builtin_amdgcn_rcpf(d.x), __builtin_amdgcn_rcpf(d.y)};
      ow[e] = pk_bf16(vv.x, vv.y);
    }
  }
  store_h(2, ow);
  WAIT_V(0);
}

__device__ __forceinline__ void phase2(const u16* __restrict__ x, const u16* __restrict__ cre, const u16* __restrict__ cim,
                                       const u16* __restrict__ b2, char* __restrict__ ws, u16* __restrict__ out, char* lds, const Loader& ld, const unsigned long long tag) {
  const int tid = threadIdx.x, lane = tid & 63, wid = __builtin_amdgcn_readfirstlane(tid >> 6);
  const int b = blockIdx.x, xcd = b & 7, idx = b >> 3;
  const int c = xcd >> 1, rt = (xcd & 1) * 8 + (idx >> 2), j = idx & 3;

#define SB() __builtin_amdgcn_sched_barrier(0)

  if (wid >= 4) {
    gu64* f16 = (gu64*)(ws + WS_CNT) + ((4 * (c >> 1) + j) * 2 + (c & 1)) * 16 + (lane & 15);
    for (unsigned spins = 0;; ++spins) {
      const unsigned long long f = __hip_atomic_load(f16, __ATOMIC_RELAXED, __HIP_MEMORY_SCOPE_AGENT);
      if (__all(f == tag) || spins > (1u << 20)) break;
      __builtin_amdgcn_s_sleep(1);
    }
    asm volatile("" ::: "memory");
    ld.issue_b(0); ld.issue_b(1); ld.issue_b(2);
    for (int u = 0; u < 27; ++u) {
      if (u == 0) WAIT_V(8); else if (u == 1) WAIT_V(12); else WAIT_V(16);
      BAR();
      ld.issue_a(u + 3); ld.issue_b(u + 3);
    }
    WAIT_V(0);
    return;
  }

  const int r31 = lane & 31, hh = lane >> 5;
  const int wm = wid >> 1, wn = wid & 1;
  unsigned ab[4];
#pragma unroll
  for (int s = 0; s < 4; ++s) ab[s] = wm * 8192 + (unsigned)(r31 * 128 + (((2 * s + hh) ^ ((r31 >> 1) & 7)) * 16));
  const unsigned bb = 16384 + wn * 8192 + lane * 16;
  const unsigned ao = wm * 8192 + lane * 16;

  f32x16 acc[2][2] = {};
  f32x16 acco = {};
  bf16x8 F0[16], F1[16];
#define RD(F, s) do { F[4 * (s) + 0] = *(const bf16x8*)(sl + ab[s]); F[4 * (s) + 1] = *(const bf16x8*)(sl + ab[s] + 4096); \
                      F[4 * (s) + 2] = *(const bf16x8*)(sl + bb + (s) * 1024); F[4 * (s) + 3] = *(const bf16x8*)(sl + bb + (s) * 1024 + 4096); } while (0)
#define RDO(F, s) do { F[4 * (s) + 0] = *(const bf16x8*)(sl + ao + (s) * 2048); F[4 * (s) + 1] = *(const bf16x8*)(sl + bb + (s) * 2048); \
                       F[4 * (s) + 2] = *(const bf16x8*)(sl + ao + (s) * 2048 + 1024); F[4 * (s) + 3] = *(const bf16x8*)(sl + bb + (s) * 2048 + 1024); } while (0)
#define MM0(F, s) acc[0][0] = __builtin_amdgcn_mfma_f32_32x32x16_bf16(F[4 * (s) + 2], F[4 * (s) + 0], acc[0][0], 0, 0, 0)
#define MM1(F, s) acc[0][1] = __builtin_amdgcn_mfma_f32_32x32x16_bf16(F[4 * (s) + 3], F[4 * (s) + 0], acc[0][1], 0, 0, 0)
#define MM2(F, s) acc[1][0] = __builtin_amdgcn_mfma_f32_32x32x16_bf16(F[4 * (s) + 2], F[4 * (s) + 1], acc[1][0], 0, 0, 0)
#define MM3(F, s) acc[1][1] = __builtin_amdgcn_mfma_f32_32x32x16_bf16(F[4 * (s) + 3], F[4 * (s) + 1], acc[1][1], 0, 0, 0)
#define MO(F, s) do { acco = __builtin_amdgcn_mfma_f32_32x32x16_bf16(F[4 * (s) + 1], F[4 * (s) + 0], acco, 0, 0, 0); \
                      acco = __builtin_amdgcn_mfma_f32_32x32x16_bf16(F[4 * (s) + 3], F[4 * (s) + 2], acco, 0, 0, 0); } while (0)
#define PERIOD(u, RDX, FR, FM) do { \
    const char* sl = lds + ring_off(u); \
    BAR(); SB(); \
    MM0(FM, 0); SB(); RDX(FR, 0); SB(); MM1(FM, 0); SB(); MM2(FM, 0); SB(); RDX(FR, 1); SB(); MM3(FM, 0); SB(); \
    MM0(FM, 1); SB(); RDX(FR, 2); SB(); MM1(FM, 1); SB(); MM2(FM, 1); SB(); RDX(FR, 3); SB(); MM3(FM, 1); SB(); \
    MM0(FM, 2); MM1(FM, 2); MM2(FM, 2); MM3(FM, 2); \
    MM0(FM, 3); MM1(FM, 3); MM2(FM, 3); SB(); WAIT_L0(); SB(); MM3(FM, 3); SB(); \
  } while (0)
#define PERIODO(u, FR, FM) do { \
    const char* sl = lds + ring_off(u); \
    BAR(); SB(); \
    RDO(FR, 0); SB(); MO(FM, 0); SB(); RDO(FR, 1); SB(); MO(FM, 1); SB(); RDO(FR, 2); SB(); MO(FM, 2); SB(); RDO(FR, 3); SB(); MO(FM, 3); SB(); \
    WAIT_L0(); SB(); \
  } while (0)
  gu64* fl = (gu64*)(ws + WS_CNT + 2048);
  {
    const char* sl = lds;
    BAR(); SB();
    if (tid == 0) __hip_atomic_store(fl + blockIdx.x, tag, __ATOMIC_RELAXED, __HIP_MEMORY_SCOPE_AGENT);

    RD(F0, 0); RD(F0, 1); RD(F0, 2); RD(F0, 3); SB();
    WAIT_L0(); SB();
  }
  for (int u = 1; u < 15; u += 2) {
    PERIOD(u, RD, F1, F0);
    PERIOD(u + 1, RD, F0, F1);
    if (u == 11 && wid == 0) {
      for (unsigned spins = 0;; ++spins) {
        const unsigned long long f0 = __hip_atomic_load(fl + lane * 4, __ATOMIC_RELAXED, __HIP_MEMORY_SCOPE_AGENT), f1 = __hip_atomic_load(fl + lane * 4 + 1, __ATOMIC_RELAXED, __HIP_MEMORY_SCOPE_AGENT),
                                 f2 = __hip_atomic_load(fl + lane * 4 + 2, __ATOMIC_RELAXED, __HIP_MEMORY_SCOPE_AGENT), f3 = __hip_atomic_load(fl + lane * 4 + 3, __ATOMIC_RELAXED, __HIP_MEMORY_SCOPE_AGENT);
        if (__all(f0 == tag && f1 == tag && f2 == tag && f3 == tag) || spins > (1u << 20)) break;
        __builtin_amdgcn_s_sleep(1);
      }
      asm volatile("" ::: "memory");
    }
  }
  PERIOD(15, RD, F1, F0);
  PERIOD(16, RDO, F0, F1);
  const int tile = rt * 4 + j;
  const auto rs = __builtin_amdgcn_make_buffer_rsrc(ws + WS_SLAB + ((size_t)tile << 17), 0, 1 << 17, 0x00020000);
  const unsigned lo = wid * 2048 + lane * 16;
#pragma unroll
  for (int q = 0; q < 4; ++q) {
    if (q != c) {
      const f32x16 v = acc[q >> 1][q & 1];
#pragma unroll
      for (int hf = 0; hf < 2; ++hf) {
        const u32x4 w = {pk_bf16(v[hf * 8], v[hf * 8 + 1]), pk_bf16(v[hf * 8 + 2], v[hf * 8 + 3]), pk_bf16(v[hf * 8 + 4], v[hf * 8 + 5]), pk_bf16(v[hf * 8 + 6], v[hf * 8 + 7])};
        __builtin_amdgcn_raw_buffer_store_b128(w, rs, (q * 4 + c) * 8192 + lo + hf * 1024, 0, 16);
      }
    }
  }
  const f32x16 own = c == 0 ? acc[0][0] : (c == 1 ? acc[0][1] : (c == 2 ? acc[1][0] : acc[1][1]));
  const int trow = rt * 128 + wm * 64 + (c >> 1) * 32 + r31;
  const int ecol = j * 128 + wn * 64 + (c & 1) * 32 + 8 * hh;
  u32x4 xv[2], bv[2];
#pragma unroll
  for (int half = 0; half < 2; ++half) { xv[half] = *(const u32x4*)(x + (size_t)trow * D_ + ecol + 16 * half); bv[half] = *(const u32x4*)(b2 + ecol + 16 * half); }
  SB();
  gu64* xfl = (gu64*)(ws + WS_CNT + 4096 + tile * 128);
  u32x4 pv[4][2];
#pragma unroll
  for (int s = 0; s < 4; ++s) { pv[s][0] = (u32x4){}; pv[s][1] = (u32x4){}; }
#pragma unroll 1
  for (int u = 17; u < 27; u += 2) {
    PERIODO(u, F1, F0);
    if (u == 19) { WAIT_V(4); }
    if (u == 23) {
      asm volatile("" ::: "memory");
#pragma unroll
      for (int s = 0; s < 4; ++s)
#pragma unroll
        for (int hf = 0; hf < 2; ++hf)
          pv[s][hf] = __builtin_amdgcn_raw_buffer_load_b128(rs, (c * 4 + s) * 8192 + lo + hf * 1024, 0, 16);
      SB();
    }
    PERIODO(u + 1, F0, F1);
    if (u == 19 && tid == 0) __hip_atomic_store(xfl + c, tag, __ATOMIC_RELAXED, __HIP_MEMORY_SCOPE_AGENT);
    if (u == 21) {
      if (wid == 0) {
        for (unsigned spins = 0;; ++spins) {
          const unsigned long long f = __hip_atomic_load(xfl + (lane & 3), __ATOMIC_RELAXED, __HIP_MEMORY_SCOPE_AGENT);
          if (__all(f == tag) || spins > (1u << 20)) break;
          __builtin_amdgcn_s_sleep(1);
        }
      }
      asm volatile("" ::: "memory");
    }
  }
  MO(F0, 0); MO(F0, 1); MO(F0, 2); MO(F0, 3);
#pragma unroll
  for (int half = 0; half < 2; ++half) {
    float o[8];
#pragma unroll
    for (int e = 0; e < 8; ++e) {
      const int rg = half * 8 + e;
      float p[4];
#pragma unroll
      for (int s = 0; s < 4; ++s) { const unsigned w = pv[s][half][e >> 1]; p[s] = c == s ? own[rg] : ((e & 1) ? bfhi(w) : bflo(w)); }
      const float s = (p[0] + p[1]) + (p[2] + p[3]);
      const unsigned xw = xv[half][e >> 1], bw = bv[half][e >> 1];
      const float xx = (e & 1) ? bfhi(xw) : bflo(xw), bb2 = (e & 1) ? bfhi(bw) : bflo(bw);
      const float v = acco[rg] + bb2;
      const float g = v * __builtin_amdgcn_rcpf(1.f + __builtin_amdgcn_exp2f(-1.442695041f * v));
      o[e] = xx + s * g;
    }
    u32x4 ov = {pk_bf16(o[0], o[1]), pk_bf16(o[2], o[3]), pk_bf16(o[4], o[5]), pk_bf16(o[6], o[7])};
    *(u32x4*)(out + (size_t)trow * D_ + ecol + 16 * half) = ov;
  }
}

extern "C" __device__ unsigned long long amd_dispatch_id(void) __asm("llvm.amdgcn.dispatch.id");

__global__ __launch_bounds__(512, 2) void fused(const u16* __restrict__ x, const u16* __restrict__ cre, const u16* __restrict__ cim,
                                                const u16* __restrict__ wre, const u16* __restrict__ wim, const u16* __restrict__ w1,
                                                const u16* __restrict__ b1, const u16* __restrict__ w2, const u16* __restrict__ b2,
                                                char* __restrict__ ws, u16* __restrict__ out) {
  extern __shared__ __attribute__((aligned(16))) char lds[];
  const unsigned long long tag = ((amd_dispatch_id() + 1ull) * 0x9E3779B97F4A7C15ull) ^ ((unsigned long long)__builtin_amdgcn_dispatch_ptr() * 0xC2B2AE3D27D4EB4Full) ^ 0x5851F42D4C957F2Dull;
  Loader ld;
  {
    const int b = blockIdx.x, xcd = b & 7, idx = b >> 3;
    ld.init(cre, cim, ws, lds, __builtin_amdgcn_readfirstlane(threadIdx.x >> 6) & 3, threadIdx.x & 63, xcd >> 1, (xcd & 1) * 8 + (idx >> 2), idx & 3);
  }
  unsigned cpf = 0;
  if (threadIdx.x < 64) {
    const char* pc = (const char*)(__builtin_amdgcn_s_getpc() & ~127ull);
    const int line = (blockIdx.x >> 3) + 32 * (threadIdx.x & 15);
    if (line < 270) asm volatile("global_load_dword %0, %1, off\n\ts_waitcnt vmcnt(0)" : "=v"(cpf) : "v"(pc + line * 128) : "memory");
  }
  phase1(x, wre, wim, w1, b1, w2, ws, lds, ld, tag);
  phase2(x, cre, cim, b2, ws, out, lds, ld, tag);
}

extern "C" void kernel_launch(void* const* d_in, const int* in_sizes, int n_in, void* d_out, int out_size, void* d_ws, size_t ws_size,
                              hipStream_t stream) {
  (void)hipFuncSetAttribute((const void*)fused, hipFuncAttributeMaxDynamicSharedMemorySize, 163840);
  fused<<<256, 512, 163840, stream>>>((const u16*)d_in[0], (const u16*)d_in[1], (const u16*)d_in[2], (const u16*)d_in[3], (const u16*)d_in[4],
                                      (const u16*)d_in[6], (const u16*)d_in[7], (const u16*)d_in[8], (const u16*)d_in[9], (char*)d_ws, (u16*)d_out);
}
```

```cpp
#include <hip/hip_runtime.h>
#include <hip/hip_bf16.h>
#include <stdint.h>
#include <type_traits>

typedef unsigned short u16;
typedef short v4s __attribute__((ext_vector_type(4)));
typedef short bf16x8 __attribute__((ext_vector_type(8)));
typedef __bf16 v2bf __attribute__((ext_vector_type(2)));
typedef float f32x4 __attribute__((ext_vector_type(4)));
typedef float f32x16 __attribute__((ext_vector_type(16)));
typedef unsigned u32x4 __attribute__((ext_vector_type(4)));
typedef unsigned u32x2 __attribute__((ext_vector_type(2)));
#define LAS __attribute__((address_space(3)))

constexpr int S_ = 2048, D_ = 512, F_ = 1376;
constexpr int NKK_B = 64;
constexpr int NSUB = 88;
constexpr size_t WS_B    = 0;
constexpr size_t WS_W2   = 4u << 20;
constexpr size_t WS_H    = 8u << 20;
constexpr size_t WS_SLAB = 16u << 20;
constexpr size_t WS_CNT  = 40u << 20;

__device__ __forceinline__ float bflo(unsigned v) { return __uint_as_float(v << 16); }
__device__ __forceinline__ float bfhi(unsigned v) { return __uint_as_float(v & 0xffff0000u); }
__device__ __forceinline__ unsigned pk_bf16(float lo, float hi) {
  typedef float f32x2_ __attribute__((ext_vector_type(2)));
  v2bf r = __builtin_convertvector((f32x2_){lo, hi}, v2bf);
  return __builtin_bit_cast(unsigned, r);
}
__device__ __forceinline__ float rbf(float f) { return bflo(pk_bf16(f, 0.f)); }
__device__ __forceinline__ int swap23(int r) { return (r & ~12) | ((r & 4) << 1) | ((r & 8) >> 1); }
typedef int i32x4 __attribute__((ext_vector_type(4)));
__device__ __forceinline__ i32x4 mk_rsrc(const void* p) {
  const unsigned long long a = (unsigned long long)p;
  return (i32x4){(int)(unsigned)a, (int)((unsigned)(a >> 32) & 0xffffu), 0x7fffffff, 0x00020000};
}
__device__ __forceinline__ void dma16(i32x4 rsrc, unsigned lds_dst, int voff, int soff) {
  unsigned keep;
  asm volatile("s_mov_b32 %0, m0\n\ts_mov_b32 m0, %1\n\ts_nop 3\n\tbuffer_load_dwordx4 %2, %3, %4 offen lds\n\ts_mov_b32 m0, %0"
               : "=&s"(keep) : "s"(lds_dst), "v"(voff), "s"(rsrc), "s"(soff) : "memory");
}
__device__ __forceinline__ void touch4(i32x4 rsrc, unsigned lds_dst, int voff, int soff) {
  unsigned keep;
  asm volatile("s_mov_b32 %0, m0\n\ts_mov_b32 m0, %1\n\ts_nop 3\n\tbuffer_load_dword %2, %3, %4 offen lds\n\ts_mov_b32 m0, %0"
               : "=&s"(keep) : "s"(lds_dst), "v"(voff), "s"(rsrc), "s"(soff) : "memory");
}
template <int OFF> __device__ __forceinline__ void dma16x_sc1(i32x4 rsrc, unsigned lds_dst, int voff, int soff) {
  unsigned keep;
  asm volatile("s_mov_b32 %0, m0\n\ts_mov_b32 m0, %1\n\ts_nop 3\n\tbuffer_load_dwordx4 %2, %3, %4 offen offset:%5 sc1 lds\n\ts_mov_b32 m0, %0"
               : "=&s"(keep) : "s"(lds_dst), "v"(voff), "s"(rsrc), "s"(soff), "n"(OFF) : "memory");
}
template <int OFF> __device__ __forceinline__ void dma16x_nt(i32x4 rsrc, unsigned lds_dst, int voff, int soff) {
  unsigned keep;
  asm volatile("s_mov_b32 %0, m0\n\ts_mov_b32 m0, %1\n\ts_nop 3\n\tbuffer_load_dwordx4 %2, %3, %4 offen offset:%5 nt lds\n\ts_mov_b32 m0, %0"
               : "=&s"(keep) : "s"(lds_dst), "v"(voff), "s"(rsrc), "s"(soff), "n"(OFF) : "memory");
}
#define WAIT_V(n) asm volatile("s_waitcnt vmcnt(%0)" ::"n"(n) : "memory")
#define WAIT_L0() asm volatile("s_waitcnt lgkmcnt(0)" ::: "memory")
#define BAR() asm volatile("s_barrier" ::: "memory")
constexpr int AUX_ST = 16;

typedef float f32x2 __attribute__((ext_vector_type(2)));
constexpr int SA0 = 0, SA1 = 33280, SH1 = 98304, SLP = 131072, AROW = 1040;
constexpr int SCR = 66560;
constexpr int SC_PART = SCR;
constexpr int SC_RS   = SCR + 3072;
constexpr int SC_MRS  = SCR + 3584;
constexpr int SC_CS   = SCR + 4096;
constexpr int SC_B1   = SCR + 7168;
constexpr int SC_DUM  = SCR + 8192;
#define PIN(x) asm volatile("" : "+v"(x))
#define SBAR() __builtin_amdgcn_sched_barrier(0)

template <int K> __device__ __forceinline__ void eh_op(const f32x16& ap, float (&v)[16], float (&t)[16], unsigned (&ow)[8], float scale, float center) {
  constexpr int st = K / 16, e = K % 16;
  if constexpr (st == 0) { v[e] = fmaf(-center, t[e], v[e]); PIN(v[e]); }
  else if constexpr (st == 1) { v[e] = fmaf(ap[e], scale, v[e]); PIN(v[e]); }
  else if constexpr (st == 2) { t[e] = v[e] * v[e]; PIN(t[e]); }
  else if constexpr (st == 3) { t[e] = fmaf(t[e], -0.1029432397f, -2.302208198f); PIN(t[e]); }
  else if constexpr (st == 4) { t[e] = t[e] * v[e]; PIN(t[e]); }
  else if constexpr (st == 5) { t[e] = __builtin_amdgcn_exp2f(t[e]); PIN(t[e]); }
  else if constexpr (st == 6) { t[e] = 1.f + t[e]; PIN(t[e]); }
  else if constexpr (st == 7) { t[e] = __builtin_amdgcn_rcpf(t[e]); PIN(t[e]); }
  else if constexpr (st == 8) { v[e] = v[e] * t[e]; PIN(v[e]); }
  else if constexpr (K < 152) { ow[e] = pk_bf16(v[2 * e], v[2 * e + 1]); PIN(ow[e]); }
}
template <int K> __device__ __forceinline__ void ea_op(const f32x16& ap, float (&v)[16], float (&t)[16], unsigned (&ow)[8], float cs) {
  constexpr int st = K / 16, e = K % 16;
  if constexpr (st == 0) { t[e] = ap[e] * t[e]; PIN(t[e]); }
  else if constexpr (st == 1) { t[e] = fmaf(-v[e], cs, t[e]); PIN(t[e]); }
  else if constexpr (K < 40) { ow[e] = pk_bf16(t[2 * e], t[2 * e + 1]); PIN(ow[e]); }
}
template <int ET, int K0, int K1> __device__ __forceinline__ void e_ops(const f32x16& ap, float (&v)[16], float (&t)[16], unsigned (&ow)[8], float s0, float s1) {
  if constexpr (K0 < K1) {
    if constexpr (ET == 1) eh_op<K0>(ap, v, t, ow, s0, s1); else if constexpr (ET == 2) ea_op<K0>(ap, v, t, ow, s0);
    e_ops<ET, K0 + 1, K1>(ap, v, t, ow, s0, s1);
  }
}
template <int MT> __device__ __forceinline__ bf16x8 wread(const char* pan, int ks) {
  if constexpr (MT == 1) { return *(const bf16x8*)(pan + ks * 32); }
  else {
    v4s t0 = __builtin_amdgcn_ds_read_tr16_b64_v4i16((LAS v4s*)(pan + ks * 1024));
    v4s t1 = __builtin_amdgcn_ds_read_tr16_b64_v4i16((LAS v4s*)(pan + ks * 1024 + 256));
    return (bf16x8)__builtin_shufflevector(t0, t1, 0, 1, 2, 3, 4, 5, 6, 7);
  }
}
__device__ __forceinline__ void row_stats(float sum, float sq, float& mean, float& inv, float& m_bf, float& scale, float& center) {
  mean = sum * (1.f / 512.f);
  const float var = fmaxf(sq * (1.f / 512.f) - mean * mean, 0.f);
  m_bf = rbf(mean);
  const float sd = rbf(sqrtf(rbf(rbf(var) + 1e-5f)));
  inv = 1.f / sd;
  const float m2 = rbf((mean - m_bf) * inv);
  const float v2 = rbf(var * inv * inv);
  const float sd2 = rbf(sqrtf(rbf(v2 + 1e-5f)));
  scale = inv / sd2; center = (m_bf + m2 * sd) * scale;
}
constexpr int WR = 8, WD = 3;
struct RowStat { float mean, var, m_bf, sd, inv, m2, v2, sd2, scale, center; };
template <int S> __device__ __forceinline__ void rs_stage(RowStat& r) {
  if constexpr (S == 0) { r.mean *= (1.f / 512.f); r.var = fmaxf(r.var * (1.f / 512.f) - r.mean * r.mean, 0.f); r.m_bf = rbf(r.mean); PIN(r.var); PIN(r.m_bf); }
  else if constexpr (S == 1) { r.sd = rbf(rbf(r.var) + 1e-5f); PIN(r.sd); }
  else if constexpr (S == 2) { r.sd = rbf(sqrtf(r.sd)); PIN(r.sd); }
  else if constexpr (S == 3) { r.inv = 1.f / r.sd; PIN(r.inv); }
  else if constexpr (S == 4) { r.m2 = rbf((r.mean - r.m_bf) * r.inv); r.v2 = rbf(rbf(r.var * r.inv * r.inv) + 1e-5f); PIN(r.m2); PIN(r.v2); }
  else if constexpr (S == 5) { r.sd2 = rbf(sqrtf(r.v2)); PIN(r.sd2); }
  else if constexpr (S == 6) { r.scale = r.inv / r.sd2; PIN(r.scale); }
  else if constexpr (S == 7) { r.center = (r.m_bf + r.m2 * r.sd) * r.scale; PIN(r.center); }
}
template <int MT, int ET, int NT, int G0, int KS, class Hook>
__device__ __forceinline__ void unit_steps(const char* pan, const char* pnx, const bf16x8 (&xf)[32], bf16x8 (&wf)[WR], f32x16& aw, const f32x16& ar,
                                           float (&v)[16], float (&t)[16], unsigned (&ow)[8], const float& s0, const float& s1, Hook&& hook) {
  if constexpr (KS < 32) {
    constexpr int NOPS = ET == 1 ? 152 : 40;
    if constexpr (KS == 0) {
      if constexpr (MT == 1) aw = __builtin_amdgcn_mfma_f32_32x32x16_bf16(xf[KS], wf[KS % WR], (f32x16){}, 0, 0, 0);
      else aw = __builtin_amdgcn_mfma_f32_32x32x16_bf16(wf[KS % WR], xf[KS], (f32x16){}, 0, 0, 0);
    } else {
      if constexpr (MT == 1) aw = __builtin_amdgcn_mfma_f32_32x32x16_bf16(xf[KS], wf[KS % WR], aw, 0, 0, 0);
      else aw = __builtin_amdgcn_mfma_f32_32x32x16_bf16(wf[KS % WR], xf[KS], aw, 0, 0, 0);
    }
    PIN(aw);
    SBAR();
    if constexpr (KS + WD < 32) wf[(KS + WD) % WR] = wread<MT>(pan, KS + WD);
    else if constexpr (NT >= 0) wf[(KS + WD) % WR] = wread<NT>(pnx, KS + WD - 32);
    asm volatile("" :: "v"(wf[KS % WR]), "v"(wf[(KS + WR - 1) % WR]));
    SBAR();
    hook(std::integral_constant<int, KS>{});
    constexpr int k0 = KS < G0 ? 0 : (KS - G0) * NOPS / (32 - G0), k1 = KS < G0 ? 0 : (KS + 1 - G0) * NOPS / (32 - G0);
    e_ops<ET, k0, k1>(ar, v, t, ow, s0, s1);
    SBAR();
    unit_steps<MT, ET, NT, G0, KS + 1>(pan, pnx, xf, wf, aw, ar, v, t, ow, s0, s1, hook);
  }
}

typedef __attribute__((address_space(1))) unsigned long long gu64;
__device__ __forceinline__ unsigned ring_off(int t) { return (t & 1) * 32768 + (t & 2) * 49152; }
struct Loader {
  i32x4 ra_s, rb_s, ra_o, rb_o; int aoff[4], voff; unsigned ldsw;
  __device__ __forceinline__ void init(const u16* cre, const u16* cim, char* ws, char* lds, int lw, int lane, int c, int rt, int j) {
#pragma unroll
    for (int i = 0; i < 4; ++i) { const int row = lw * 32 + i * 8 + (lane >> 3); aoff[i] = row * 4096 + (((lane & 7) ^ ((row >> 1) & 7)) * 16) - i * 1024 + 4096; }
    ldsw = (unsigned)(size_t)(LAS char*)lds + lw * 4096;
    ra_s = mk_rsrc((const char*)(c < 2 ? cre : cim) + (size_t)rt * 128 * 4096 + (c & 1) * 2048 - 4096);
    rb_s = mk_rsrc(ws + WS_B + ((size_t)(j * NKK_B + c * 16) << 14) + lw * 4096);
    ra_o = mk_rsrc(ws + WS_H + ((size_t)((rt * 4 + 2 * (lw >> 1) + (c >> 1)) * NSUB) << 10) + (lw & 1) * 4096 - (16 << 13));
    rb_o = mk_rsrc(ws + WS_W2 + ((size_t)((j * 4 + 2 * (lw >> 1) + (c & 1)) * NSUB) << 10) + (lw & 1) * 4096 - (16 << 13));
    voff = lane * 16;
  }
  __device__ __forceinline__ void issue_a(int T) const {
    const unsigned dst = __builtin_amdgcn_readfirstlane(ldsw + ring_off(T));
    if (T < 16) { const int so = __builtin_amdgcn_readfirstlane(T * 128);
                  dma16x_nt<0>(ra_s, dst, aoff[0], so); dma16x_nt<1024>(ra_s, dst, aoff[1], so); dma16x_nt<2048>(ra_s, dst, aoff[2], so); dma16x_nt<3072>(ra_s, dst, aoff[3], so); }
    else { i32x4 r = ra_o; r.z = __builtin_amdgcn_readfirstlane(T < 27 ? 0x7fffffff : 0); const int so = __builtin_amdgcn_readfirstlane(T << 13);
           dma16x_sc1<0>(r, dst, voff, so); dma16x_sc1<1024>(r, dst, voff, so); dma16x_sc1<2048>(r, dst, voff, so); dma16x_sc1<3072>(r, dst, voff, so); }
  }
  __device__ __forceinline__ void issue_b(int T) const {
    const unsigned dst = __builtin_amdgcn_readfirstlane(ldsw + ring_off(T) + 16384);
    i32x4 r = T < 16 ? rb_s : rb_o; r.z = T < 27 ? 0x7fffffff : 0;
    const int so = __builtin_amdgcn_readfirstlane(T < 16 ? T << 14 : T << 13);
    dma16x_sc1<0>(r, dst, voff, so); dma16x_sc1<1024>(r, dst, voff, so); dma16x_sc1<2048>(r, dst, voff, so); dma16x_sc1<3072>(r, dst, voff, so);
  }
};

__device__ __forceinline__ void phase1(const u16* __restrict__ x, const u16* __restrict__ wre, const u16* __restrict__ wim,
                                       const u16* __restrict__ w1, const u16* __restrict__ b1, const u16* __restrict__ w2,
                                       char* __restrict__ ws, char* lds, const Loader& ld, const unsigned long long tag) {
  const int tid = threadIdx.x, lane = tid & 63, wid = __builtin_amdgcn_readfirstlane(tid >> 6);
  const int b = blockIdx.x, xcd = b & 7, idx = b >> 3;
  const int rt = (xcd >> 1) * 4 + (idx & 3);
  const int cg = (xcd & 1) * 8 + (idx >> 2);
  const int h0 = cg < 11 ? cg * 3 : 33 + (cg - 11) * 2;
  const bool h3 = cg < 11;
  const int a0 = cg * 2;
  const int m = wid & 3;
  const bool helper = wid >= 4;
  const int r31 = lane & 31, hh = lane >> 5;
  const unsigned lds0 = (unsigned)(size_t)(LAS char*)lds;
  const int prot = idx & 3;

  const int hoff = (lane >> 2) * (F_ * 2) + (lane & 3) * 16;
  const i32x4 rw1 = mk_rsrc(w1);
  auto piece_h = [&](int u, int q, int slot_off) { dma16(rw1, lds0 + slot_off + q * 1024, hoff, q * 16 * (F_ * 2) + u * 64); };
  const i32x4 rx = mk_rsrc(x + (size_t)(rt * 128 + m * 32) * D_);
  int xo[8], xs[8];
#pragma unroll
  for (int i = 0; i < 8; ++i) {
    const int ii = (i + (idx >> 2)) & 7, r = (ii & 3) * 4 + (lane >> 4);
    xo[i] = r * 1024 + (((lane & 15) ^ r) * 16); xs[i] = ii;
  }
  auto quarter = [&](int q) {
#pragma unroll
    for (int i = 0; i < 8; ++i) dma16(rx, lds0 + q * 32768 + m * 8192 + xs[i] * 1024, xo[i], q * 256 + (xs[i] >> 2) * 16384);
    piece_h(h0, q * 8 + ((m * 2 + 2 * prot) & 7), SLP); piece_h(h0, q * 8 + ((m * 2 + 1 + 2 * prot) & 7), SLP);
  };
  if (helper) { quarter(0); quarter(1); }

  const auto rws = __builtin_amdgcn_make_buffer_rsrc((void*)ws, 0, 0x7fffffff, 0x00020000);
  const int xrow = m * 32 + swap23(r31);
  const unsigned xbase = xrow * 256 + ((hh ^ (xrow & 15)) * 16);

  if (helper) {
    const auto rw2t = __builtin_amdgcn_make_buffer_rsrc((void*)(ws + WS_W2), 0, 16 * NSUB * 1024, 0x00020000);
    const i32x4 rwa = mk_rsrc((a0 < 16 ? wre : wim) + (size_t)(a0 & 15) * 32 * D_);
    auto panel_h = [&](int u, int slot_off) {
#pragma unroll
      for (int k = 0; k < 8; ++k) piece_h(u, m * 8 + ((k + 2 * prot) & 7), slot_off);
    };
    auto panel_a = [&](int w, int slot_off) {
#pragma unroll
      for (int k = 0; k < 8; ++k) { const int q = m * 8 + ((k + 2 * prot) & 7); dma16(rwa, lds0 + slot_off + q * AROW, lane * 16, (w * 32 + q) * 1024); }
    };
    const v4s ones = {0x3f80, 0x3f80, 0x3f80, 0x3f80};
    const char* pzh = lds + ((lane >> 2) & 15) * 64 + (m & 1) * 32 + (lane & 3) * 8;
    auto cs_h_part = [&](f32x4 (&c)[4], int slot_off, int j0, int n) {
#pragma unroll
      for (int j = 0; j < n; ++j)
        c[j & 3] = __builtin_amdgcn_mfma_f32_4x4x4bf16_1k(ones, __builtin_amdgcn_ds_read_tr16_b64_v4i16((LAS v4s*)(pzh + slot_off + (j0 + j) * 1024)), c[j & 3], 0, 0, 0);
    };
    auto cs_h_fin = [&](f32x4 (&c)[4], int i) {
      float r = (c[0][0] + c[1][0]) + (c[2][0] + c[3][0]);
      r += __shfl_xor(r, 16); r += __shfl_xor(r, 32);
      if (lane < 16) *(float*)(lds + SC_CS + i * 512 + (m & 1) * 64 + lane * 4) = r;
    };
    auto cs_h = [&](int i, int slot_off) {
      if ((m >> 1) == (i & 1)) { f32x4 c[4] = {}; cs_h_part(c, slot_off, 0, 32); cs_h_fin(c, i); }
    };
    auto cs_a = [&](int i, int slot_off) {
      const char* pz = lds + slot_off + swap23(r31) * AROW + hh * 16 + m * 256;
      f32x4 c0 = {}, c1 = {};
#pragma unroll
      for (int j = 0; j < 8; ++j) {
        const bf16x8 w = *(const bf16x8*)(pz + j * 32);
        c0 = __builtin_amdgcn_mfma_f32_4x4x4bf16_1k(ones, __builtin_shufflevector(w, w, 0, 1, 2, 3), c0, 0, 0, 0);
        c1 = __builtin_amdgcn_mfma_f32_4x4x4bf16_1k(ones, __builtin_shufflevector(w, w, 4, 5, 6, 7), c1, 0, 0, 0);
      }
      float r = c0[0] + c1[0]; r += __shfl_xor(r, 32);
      if (lane < 32) *(float*)(lds + SC_CS + i * 512 + m * 128 + lane * 4) = r;
    };
    __builtin_amdgcn_s_setprio(2);
    {
      f32x4 c[4] = {};
      WAIT_V(10); BAR();
      quarter(2);
      if (m < 2) cs_h_part(c, SLP, 0, 8);
      WAIT_V(10); BAR();
      quarter(3);
      if (m < 2) cs_h_part(c, SLP, 8, 8);
      WAIT_V(10); BAR();
      panel_a(0, SA0);
      unsigned w2v[2][8], b1v;
      int w2dst[2];
      {
        i32x4 rw2 = mk_rsrc(w2); rw2.z = F_ * D_ * 2;
#pragma unroll
        for (int r = 0; r < 2; ++r) {
          const int hl = r * 256 + (tid - 256), cc = b * 352 + hl;
          const int ln = cc & 63, rest = cc >> 6, ct = rest / NSUB, sub = rest - ct * NSUB;
          const int off = hl < 352 ? ((16 * sub + 4 * (ln >> 5)) * D_ + 32 * ct + swap23(ln & 31)) * 2 : 0x7ff00000;
          w2dst[r] = hl < 352 ? cc * 16 : 0x7ffffff0;
          asm volatile("buffer_load_ushort %0, %8, %10, 0 offen\n\tbuffer_load_ushort %1, %8, %10, 0 offen offset:1024\n\t"
                       "buffer_load_ushort %2, %8, %10, 0 offen offset:2048\n\tbuffer_load_ushort %3, %8, %10, 0 offen offset:3072\n\t"
                       "buffer_load_ushort %4, %9, %10, 0 offen\n\tbuffer_load_ushort %5, %9, %10, 0 offen offset:1024\n\t"
                       "buffer_load_ushort %6, %9, %10, 0 offen offset:2048\n\tbuffer_load_ushort %7, %9, %10, 0 offen offset:3072"
                       : "=&v"(w2v[r][0]), "=&v"(w2v[r][1]), "=&v"(w2v[r][2]), "=&v"(w2v[r][3]), "=&v"(w2v[r][4]), "=&v"(w2v[r][5]), "=&v"(w2v[r][6]), "=&v"(w2v[r][7])
                       : "v"(off), "v"(off + 8192), "s"(rw2) : "memory");
        }
        const int bo = h0 * 32 + lane * 2;
        asm volatile("buffer_load_dword %0, %1, %2, 0 offen" : "=&v"(b1v) : "v"((bo < F_ - 2 ? bo : F_ - 2) * 2), "s"(mk_rsrc(b1)) : "memory");
      }
      if (m < 2) cs_h_part(c, SLP, 16, 8);
      WAIT_V(25); BAR();
      panel_a(1, SA1);
      if (m < 2) { cs_h_part(c, SLP, 24, 8); cs_h_fin(c, 0); }
      WAIT_V(8);
      asm volatile("" : "+v"(w2v[0][0]), "+v"(w2v[0][1]), "+v"(w2v[0][2]), "+v"(w2v[0][3]), "+v"(w2v[0][4]), "+v"(w2v[0][5]), "+v"(w2v[0][6]), "+v"(w2v[0][7]), "+v"(b1v));
      asm volatile("" : "+v"(w2v[1][0]), "+v"(w2v[1][1]), "+v"(w2v[1][2]), "+v"(w2v[1][3]), "+v"(w2v[1][4]), "+v"(w2v[1][5]), "+v"(w2v[1][6]), "+v"(w2v[1][7]));
      if (m == 3 && lane < 48) *(f32x2*)(lds + SC_B1 + lane * 8) = (f32x2){bflo(b1v), bfhi(b1v)};
#pragma unroll
      for (int r = 0; r < 2; ++r) {
        const u32x4 w = {w2v[r][0] | (w2v[r][1] << 16), w2v[r][2] | (w2v[r][3] << 16), w2v[r][4] | (w2v[r][5] << 16), w2v[r][6] | (w2v[r][7] << 16)};
        __builtin_amdgcn_raw_buffer_store_b128(w, rw2t, w2dst[r], 0, AUX_ST);
      }
    }
    WAIT_V(2); WAIT_L0(); BAR();
    panel_h(h0 + 1, SH1);
    panel_h(h3 ? h0 + 2 : h0, SLP);
    {
      const int j2 = idx & 3;
      const int st1 = 3 + 2 * m + hh, st2 = st1 + 8 < 15 ? st1 + 8 : 15;
      const int vb = (j2 * 32 + r31) * 4096 + 4096;
      touch4(ld.ra_s, lds0 + SC_DUM + m * 256, vb + st1 * 128, 0);
    }
    cs_a(1, SA0);
    {
      float mean, inv, m_bf, sc_, ce_;
      const f32x2 pr = *(const f32x2*)(lds + SC_PART + (m * 32 + r31) * 8);
      row_stats(pr.x, pr.y, mean, inv, m_bf, sc_, ce_);
      const float sg = a0 < 16 ? inv : -inv;
      if (lane < 32) { *(float*)(lds + SC_RS + (m * 32 + lane) * 4) = sg; *(float*)(lds + SC_MRS + (m * 32 + lane) * 4) = m_bf * sg; }
    }
    WAIT_V(9); WAIT_L0(); BAR();
    cs_a(2, SA1);
    WAIT_V(1);
    {
      const int j2 = idx & 3;
      const int st1 = 3 + 2 * m + hh, st2 = st1 + 8 < 15 ? st1 + 8 : 15;
      const int vb = (j2 * 32 + r31) * 4096 + 4096;
      touch4(ld.ra_s, lds0 + SC_DUM + m * 256, vb + st2 * 128, 0);
    }
    WAIT_L0(); BAR();
    cs_h(3, SH1);
    ld.issue_a(0); ld.issue_a(1);
    WAIT_L0(); BAR();
    cs_h(4, SLP);
    ld.issue_a(2);
    WAIT_L0(); BAR();
    if (tid == 256) __hip_atomic_store((gu64*)(ws + WS_CNT) + ((cg >> 1) * 2 + (rt >> 3)) * 16 + (rt & 7) * 2 + (cg & 1), tag, __ATOMIC_RELAXED, __HIP_MEMORY_SCOPE_AGENT);
    BAR();
    __builtin_amdgcn_s_setprio(0);
    asm volatile("fused_mark_c:");
    return;
  }

  bf16x8 xf[32];
  const int wrow = swap23(r31);
  const unsigned wlA = (unsigned)(wrow * AROW + hh * 16);
  const unsigned wlH = (unsigned)((8 * hh + ((lane & 15) >> 2)) * 64 + ((lane >> 4) & 1) * 32 + (lane & 3) * 8);
  f32x16 acc, ac2;
  bf16x8 wf[WR];
  float v[16], t[16]; unsigned ow[8];
  auto nohook = [](auto) {};
  RowStat rs;
  const int hstore = (swap23(r31) + 32 * hh) * 16;
  const int s0r = rt * 128 + m * 32;

  auto tables_h = [&](int i, int hu) {
#pragma unroll
    for (int g4 = 0; g4 < 4; ++g4) {
      const f32x4 c = *(const f32x4*)(lds + SC_CS + i * 512 + (8 * g4 + 4 * hh) * 4), bq = *(const f32x4*)(lds + SC_B1 + hu * 128 + (8 * g4 + 4 * hh) * 4);
#pragma unroll
      for (int e = 0; e < 4; ++e) { t[g4 * 4 + e] = c[e]; v[g4 * 4 + e] = bq[e]; }
    }
  };
  auto tables_a = [&](int i) -> float {
#pragma unroll
    for (int g4 = 0; g4 < 4; ++g4) {
      const f32x4 c = *(const f32x4*)(lds + SC_RS + (m * 32 + 8 * g4 + 4 * hh) * 4), bq = *(const f32x4*)(lds + SC_MRS + (m * 32 + 8 * g4 + 4 * hh) * 4);
#pragma unroll
      for (int e = 0; e < 4; ++e) { t[g4 * 4 + e] = c[e]; v[g4 * 4 + e] = bq[e]; }
    }
    const char* cq = lds + SC_CS + i * 512 + r31 * 4;
    return (*(const float*)cq + *(const float*)(cq + 128)) + (*(const float*)(cq + 256) + *(const float*)(cq + 384));
  };
  auto tables_a4 = [&](int i, float (&cp)[4]) {
#pragma unroll
    for (int g4 = 0; g4 < 4; ++g4) {
      const f32x4 c = *(const f32x4*)(lds + SC_RS + (m * 32 + 8 * g4 + 4 * hh) * 4), bq = *(const f32x4*)(lds + SC_MRS + (m * 32 + 8 * g4 + 4 * hh) * 4);
#pragma unroll
      for (int e = 0; e < 4; ++e) { t[g4 * 4 + e] = c[e]; v[g4 * 4 + e] = bq[e]; }
      cp[g4] = *(const float*)(lds + SC_CS + i * 512 + g4 * 128 + r31 * 4);
    }
  };
  auto store_h = [&](int hu, const unsigned (&ow)[8]) {
    const int dst = (int)WS_H + (((rt * 4 + m) * NSUB + 2 * (h0 + hu)) << 10);
    if (hu < 2 || h3) {
      __builtin_amdgcn_raw_buffer_store_b128((u32x4){ow[0], ow[1], ow[2], ow[3]}, rws, hstore, dst, AUX_ST);
      __builtin_amdgcn_raw_buffer_store_b128((u32x4){ow[4], ow[5], ow[6], ow[7]}, rws, hstore + 1024, dst, AUX_ST);
    }
    if (h0 + hu == 42 && (hu < 2 || h3)) { u32x4 z = {0u, 0u, 0u, 0u}; __builtin_amdgcn_raw_buffer_store_b128(z, rws, lane * 16 + 2048, dst, AUX_ST); __builtin_amdgcn_raw_buffer_store_b128(z, rws, lane * 16 + 3072, dst, AUX_ST); }
  };
  auto store_a = [&](int w, const unsigned (&ow)[8]) {
    const int u = a0 + w, et = u & 15, j = et >> 2, nt = et & 3;
    const int kk = (u >> 4) * 32 + (s0r >> 6), sub = (s0r & 63) >> 4;
    const int dst = (int)WS_B + (((j * NKK_B + kk) * 16 + nt * 4 + sub) << 10);
    __builtin_amdgcn_raw_buffer_store_b128((u32x4){ow[0], ow[1], ow[2], ow[3]}, rws, lane * 16, dst, AUX_ST);
    __builtin_amdgcn_raw_buffer_store_b128((u32x4){ow[4], ow[5], ow[6], ow[7]}, rws, lane * 16 + 1024, dst, AUX_ST);
  };
  {
    f32x4 ss0 = {}, ss1 = {}, sq0 = {}, sq1 = {};
    const v4s ones4 = {0x3f80, 0x3f80, 0x3f80, 0x3f80};
    acc = (f32x16){};
    const char* pan = lds + SLP + wlH;
#pragma unroll
    for (int q = 0; q < 4; ++q) {
      BAR();
      bf16x8 w8[8];
#pragma unroll
      for (int k = 0; k < 8; ++k) xf[q * 8 + k] = *(const bf16x8*)(lds + q * 32768 + (xbase ^ (k * 32)));
#pragma unroll
      for (int k = 0; k < 8; ++k) w8[k] = wread<0>(pan, q * 8 + k);
      SBAR();
#pragma unroll
      for (int k = 0; k < 8; ++k) {
        const v4s lo4 = __builtin_shufflevector(xf[q * 8 + k], xf[q * 8 + k], 0, 1, 2, 3), hi4 = __builtin_shufflevector(xf[q * 8 + k], xf[q * 8 + k], 4, 5, 6, 7);
        ss0 = __builtin_amdgcn_mfma_f32_4x4x4bf16_1k(ones4, lo4, ss0, 0, 0, 0); sq0 = __builtin_amdgcn_mfma_f32_4x4x4bf16_1k(lo4, lo4, sq0, 0, 0, 0);
        ss1 = __builtin_amdgcn_mfma_f32_4x4x4bf16_1k(ones4, hi4, ss1, 0, 0, 0); sq1 = __builtin_amdgcn_mfma_f32_4x4x4bf16_1k(hi4, hi4, sq1, 0, 0, 0);
      }
      asm volatile("" : "+v"(ss0), "+v"(ss1), "+v"(sq0), "+v"(sq1));
      SBAR();
#pragma unroll
      for (int k = 0; k < 8; ++k) { acc = __builtin_amdgcn_mfma_f32_32x32x16_bf16(w8[k], xf[q * 8 + k], acc, 0, 0, 0); PIN(acc); }
      WAIT_L0(); SBAR();
    }
    const f32x4 sqv = sq0 + sq1;
    float sum = ss0[0] + ss1[0], sq = (lane & 2) ? ((lane & 1) ? sqv[3] : sqv[2]) : ((lane & 1) ? sqv[1] : sqv[0]);
    sum += __shfl_xor(sum, 32); sq += __shfl_xor(sq, 32);
    if (lane < 32) *(f32x2*)(lds + SC_PART + (m * 32 + lane) * 8) = (f32x2){sum, sq};
    rs.mean = sum; rs.var = sq;
  }
  WAIT_L0(); BAR();
  const char* pA0 = lds + SA0 + wlA; const char* pA1 = lds + SA1 + wlA;
  const char* pH1 = lds + SH1 + wlH; const char* pH2 = lds + SLP + wlH;
#pragma unroll
  for (int e = 0; e < WD; ++e) wf[e] = wread<1>(pA0, e);
  wf[WR - 1] = wf[0];
  tables_h(0, 0);
  float csa = 0.f, cp[4];
  auto cs_hook = [&](auto g) { if constexpr (decltype(g)::value == 2) { csa = (cp[0] + cp[1]); PIN(csa); float c2 = cp[2] + cp[3]; PIN(c2); csa += c2; PIN(csa); } };
  unit_steps<1, 1, 1, 8, 0>(pA0, pA1, xf, wf, ac2, acc, v, t, ow, rs.scale, rs.center, [&](auto g) {
    constexpr int G = decltype(g)::value;
    if constexpr (G < 8) rs_stage<G>(rs);
  });
  store_h(0, ow);
  BAR();
  tables_a4(1, cp);
  unit_steps<1, 2, 0, 3, 0>(pA1, pH1, xf, wf, acc, ac2, v, t, ow, csa, csa, cs_hook); store_a(0, ow);
  BAR();
  tables_a4(2, cp);
  unit_steps<0, 2, 0, 3, 0>(pH1, pH2, xf, wf, ac2, acc, v, t, ow, csa, csa, cs_hook); store_a(1, ow);
  BAR();
  tables_h(3, 1);
  unit_steps<0, 1, -1, 3, 0>(pH2, pH2, xf, wf, acc, ac2, v, t, ow, rs.scale, rs.center, [&](auto g) {
    if constexpr (decltype(g)::value == 14) { WAIT_V(0); BAR(); }
  });
  store_h(1, ow);
  BAR();
  tables_h(4, 2);
  {
#pragma unroll
    for (int e = 0; e < 8; ++e) {
      f32x2 vv = (f32x2){v[2 * e], v[2 * e + 1]} - (f32x2){t[2 * e], t[2 * e + 1]} * rs.center;
      vv = (f32x2){acc[2 * e], acc[2 * e + 1]} * rs.scale + vv;
      const f32x2 tt = ((vv * vv) * (-0.1029432397f) + (-2.302208198f)) * vv;
      const f32x2 d = (f32x2){__builtin_amdgcn_exp2f(tt.x), __builtin_amdgcn_exp2f(tt.y)} + 1.f;
      vv = vv * (f32x2){__builtin_amdgcn_rcpf(d.x), __builtin_amdgcn_rcpf(d.y)};
      ow[e] = pk_bf16(vv.x, vv.y);
    }
  }
  store_h(2, ow);
  WAIT_V(0);
}

__device__ __forceinline__ void phase2(const u16* __restrict__ x, const u16* __restrict__ cre, const u16* __restrict__ cim,
                                       const u16* __restrict__ b2, char* __restrict__ ws, u16* __restrict__ out, char* lds, const Loader& ld, const unsigned long long tag) {
  const int tid = threadIdx.x, lane = tid & 63, wid = __builtin_amdgcn_readfirstlane(tid >> 6);
  const int b = blockIdx.x, xcd = b & 7, idx = b >> 3;
  const int c = xcd >> 1, rt = (xcd & 1) * 8 + (idx >> 2), j = idx & 3;

#define SB() __builtin_amdgcn_sched_barrier(0)

  if (wid >= 4) {
    gu64* f16 = (gu64*)(ws + WS_CNT) + ((4 * (c >> 1) + j) * 2 + (c & 1)) * 16 + (lane & 15);
    for (unsigned spins = 0;; ++spins) {
      const unsigned long long f = __hip_atomic_load(f16, __ATOMIC_RELAXED, __HIP_MEMORY_SCOPE_AGENT);
      if (__all(f == tag) || spins > (1u << 20)) break;
      __builtin_amdgcn_s_sleep(1);
    }
    asm volatile("" ::: "memory");
    ld.issue_b(0); ld.issue_b(1); ld.issue_b(2);
    for (int u = 0; u < 27; ++u) {
      if (u == 0) WAIT_V(8); else if (u == 1) WAIT_V(12); else WAIT_V(16);
      BAR();
      ld.issue_a(u + 3); ld.issue_b(u + 3);
    }
    WAIT_V(0);
    asm volatile("fused_mark_a:");
    return;
  }

  const int r31 = lane & 31, hh = lane >> 5;
  const int wm = wid >> 1, wn = wid & 1;
  unsigned ab[4];
#pragma unroll
  for (int s = 0; s < 4; ++s) ab[s] = wm * 8192 + (unsigned)(r31 * 128 + (((2 * s + hh) ^ ((r31 >> 1) & 7)) * 16));
  const unsigned bb = 16384 + wn * 8192 + lane * 16;
  const unsigned ao = wm * 8192 + lane * 16;

  f32x16 acc[2][2] = {};
  f32x16 acco = {};
  bf16x8 F0[16], F1[16];
#define RD(F, s) do { F[4 * (s) + 0] = *(const bf16x8*)(sl + ab[s]); F[4 * (s) + 1] = *(const bf16x8*)(sl + ab[s] + 4096); \
                      F[4 * (s) + 2] = *(const bf16x8*)(sl + bb + (s) * 1024); F[4 * (s) + 3] = *(const bf16x8*)(sl + bb + (s) * 1024 + 4096); } while (0)
#define RDO(F, s) do { F[4 * (s) + 0] = *(const bf16x8*)(sl + ao + (s) * 2048); F[4 * (s) + 1] = *(const bf16x8*)(sl + bb + (s) * 2048); \
                       F[4 * (s) + 2] = *(const bf16x8*)(sl + ao + (s) * 2048 + 1024); F[4 * (s) + 3] = *(const bf16x8*)(sl + bb + (s) * 2048 + 1024); } while (0)
#define MM0(F, s) acc[0][0] = __builtin_amdgcn_mfma_f32_32x32x16_bf16(F[4 * (s) + 2], F[4 * (s) + 0], acc[0][0], 0, 0, 0)
#define MM1(F, s) acc[0][1] = __builtin_amdgcn_mfma_f32_32x32x16_bf16(F[4 * (s) + 3], F[4 * (s) + 0], acc[0][1], 0, 0, 0)
#define MM2(F, s) acc[1][0] = __builtin_amdgcn_mfma_f32_32x32x16_bf16(F[4 * (s) + 2], F[4 * (s) + 1], acc[1][0], 0, 0, 0)
#define MM3(F, s) acc[1][1] = __builtin_amdgcn_mfma_f32_32x32x16_bf16(F[4 * (s) + 3], F[4 * (s) + 1], acc[1][1], 0, 0, 0)
#define MO(F, s) do { acco = __builtin_amdgcn_mfma_f32_32x32x16_bf16(F[4 * (s) + 1], F[4 * (s) + 0], acco, 0, 0, 0); \
                      acco = __builtin_amdgcn_mfma_f32_32x32x16_bf16(F[4 * (s) + 3], F[4 * (s) + 2], acco, 0, 0, 0); } while (0)
#define PERIOD(u, RDX, FR, FM) do { \
    const char* sl = lds + ring_off(u); \
    BAR(); SB(); \
    MM0(FM, 0); SB(); RDX(FR, 0); SB(); MM1(FM, 0); SB(); MM2(FM, 0); SB(); RDX(FR, 1); SB(); MM3(FM, 0); SB(); \
    MM0(FM, 1); SB(); RDX(FR, 2); SB(); MM1(FM, 1); SB(); MM2(FM, 1); SB(); RDX(FR, 3); SB(); MM3(FM, 1); SB(); \
    MM0(FM, 2); MM1(FM, 2); MM2(FM, 2); MM3(FM, 2); \
    MM0(FM, 3); MM1(FM, 3); MM2(FM, 3); SB(); WAIT_L0(); SB(); MM3(FM, 3); SB(); \
  } while (0)
#define PERIODO(u, FR, FM) do { \
    const char* sl = lds + ring_off(u); \
    BAR(); SB(); \
    RDO(FR, 0); SB(); MO(FM, 0); SB(); RDO(FR, 1); SB(); MO(FM, 1); SB(); RDO(FR, 2); SB(); MO(FM, 2); SB(); RDO(FR, 3); SB(); MO(FM, 3); SB(); \
    WAIT_L0(); SB(); \
  } while (0)
  gu64* fl = (gu64*)(ws + WS_CNT + 2048);
  {
    const char* sl = lds;
    BAR(); SB();
    if (tid == 0) __hip_atomic_store(fl + blockIdx.x, tag, __ATOMIC_RELAXED, __HIP_MEMORY_SCOPE_AGENT);

    RD(F0, 0); RD(F0, 1); RD(F0, 2); RD(F0, 3); SB();
    WAIT_L0(); SB();
  }
  for (int u = 1; u < 15; u += 2) {
    PERIOD(u, RD, F1, F0);
    PERIOD(u + 1, RD, F0, F1);
    if (u == 11 && wid == 0) {
      for (unsigned spins = 0;; ++spins) {
        const unsigned long long f0 = __hip_atomic_load(fl + lane * 4, __ATOMIC_RELAXED, __HIP_MEMORY_SCOPE_AGENT), f1 = __hip_atomic_load(fl + lane * 4 + 1, __ATOMIC_RELAXED, __HIP_MEMORY_SCOPE_AGENT),
                                 f2 = __hip_atomic_load(fl + lane * 4 + 2, __ATOMIC_RELAXED, __HIP_MEMORY_SCOPE_AGENT), f3 = __hip_atomic_load(fl + lane * 4 + 3, __ATOMIC_RELAXED, __HIP_MEMORY_SCOPE_AGENT);
        if (__all(f0 == tag && f1 == tag && f2 == tag && f3 == tag) || spins > (1u << 20)) break;
        __builtin_amdgcn_s_sleep(1);
      }
      asm volatile("" ::: "memory");
    }
  }
  PERIOD(15, RD, F1, F0);
  PERIOD(16, RDO, F0, F1);
  const int tile = rt * 4 + j;
  const auto rs = __builtin_amdgcn_make_buffer_rsrc(ws + WS_SLAB + ((size_t)tile << 17), 0, 1 << 17, 0x00020000);
  const unsigned lo = wid * 2048 + lane * 16;
#pragma unroll
  for (int q = 0; q < 4; ++q) {
    if (q != c) {
      const f32x16 v = acc[q >> 1][q & 1];
#pragma unroll
      for (int hf = 0; hf < 2; ++hf) {
        const u32x4 w = {pk_bf16(v[hf * 8], v[hf * 8 + 1]), pk_bf16(v[hf * 8 + 2], v[hf * 8 + 3]), pk_bf16(v[hf * 8 + 4], v[hf * 8 + 5]), pk_bf16(v[hf * 8 + 6], v[hf * 8 + 7])};
        __builtin_amdgcn_raw_buffer_store_b128(w, rs, (q * 4 + c) * 8192 + lo + hf * 1024, 0, 16);
      }
    }
  }
  const f32x16 own = c == 0 ? acc[0][0] : (c == 1 ? acc[0][1] : (c == 2 ? acc[1][0] : acc[1][1]));
  const int trow = rt * 128 + wm * 64 + (c >> 1) * 32 + r31;
  const int ecol = j * 128 + wn * 64 + (c & 1) * 32 + 8 * hh;
  u32x4 xv[2], bv[2];
#pragma unroll
  for (int half = 0; half < 2; ++half) { xv[half] = *(const u32x4*)(x + (size_t)trow * D_ + ecol + 16 * half); bv[half] = *(const u32x4*)(b2 + ecol + 16 * half); }
  SB();
  gu64* xfl = (gu64*)(ws + WS_CNT + 4096 + tile * 128);
  u32x4 pv[4][2];
#pragma unroll
  for (int s = 0; s < 4; ++s) { pv[s][0] = (u32x4){}; pv[s][1] = (u32x4){}; }
#pragma unroll 1
  for (int u = 17; u < 27; u += 2) {
    PERIODO(u, F1, F0);
    if (u == 19) { WAIT_V(4); }
    if (u == 23) {
      asm volatile("" ::: "memory");
#pragma unroll
      for (int s = 0; s < 4; ++s)
#pragma unroll
        for (int hf = 0; hf < 2; ++hf)
          pv[s][hf] = __builtin_amdgcn_raw_buffer_load_b128(rs, (c * 4 + s) * 8192 + lo + hf * 1024, 0, 16);
      SB();
    }
    PERIODO(u + 1, F0, F1);
    if (u == 19 && tid == 0) __hip_atomic_store(xfl + c, tag, __ATOMIC_RELAXED, __HIP_MEMORY_SCOPE_AGENT);
    if (u == 21) {
      if (wid == 0) {
        for (unsigned spins = 0;; ++spins) {
          const unsigned long long f = __hip_atomic_load(xfl + (lane & 3), __ATOMIC_RELAXED, __HIP_MEMORY_SCOPE_AGENT);
          if (__all(f == tag) || spins > (1u << 20)) break;
          __builtin_amdgcn_s_sleep(1);
        }
      }
      asm volatile("" ::: "memory");
    }
  }
  MO(F0, 0); MO(F0, 1); MO(F0, 2); MO(F0, 3);
#pragma unroll
  for (int half = 0; half < 2; ++half) {
    float o[8];
#pragma unroll
    for (int e = 0; e < 8; ++e) {
      const int rg = half * 8 + e;
      float p[4];
#pragma unroll
      for (int s = 0; s < 4; ++s) { const unsigned w = pv[s][half][e >> 1]; p[s] = c == s ? own[rg] : ((e & 1) ? bfhi(w) : bflo(w)); }
      const float s = (p[0] + p[1]) + (p[2] + p[3]);
      const unsigned xw = xv[half][e >> 1], bw = bv[half][e >> 1];
      const float xx = (e & 1) ? bfhi(xw) : bflo(xw), bb2 = (e & 1) ? bfhi(bw) : bflo(bw);
      const float v = acco[rg] + bb2;
      const float g = v * __builtin_amdgcn_rcpf(1.f + __builtin_amdgcn_exp2f(-1.442695041f * v));
      o[e] = xx + s * g;
    }
    u32x4 ov = {pk_bf16(o[0], o[1]), pk_bf16(o[2], o[3]), pk_bf16(o[4], o[5]), pk_bf16(o[6], o[7])};
    *(u32x4*)(out + (size_t)trow * D_ + ecol + 16 * half) = ov;
  }
  asm volatile("fused_mark_b:");
}

extern "C" __device__ unsigned long long amd_dispatch_id(void) __asm("llvm.amdgcn.dispatch.id");

__global__ __launch_bounds__(512, 2) void fused(const u16* __restrict__ x, const u16* __restrict__ cre, const u16* __restrict__ cim,
                                                const u16* __restrict__ wre, const u16* __restrict__ wim, const u16* __restrict__ w1,
                                                const u16* __restrict__ b1, const u16* __restrict__ w2, const u16* __restrict__ b2,
                                                char* __restrict__ ws, u16* __restrict__ out) {
  extern __shared__ __attribute__((aligned(16))) char lds[];
  if (__builtin_amdgcn_readfirstlane(threadIdx.x) < 64) {
    unsigned long long pc; int la, lb, lc;
    asm volatile("s_getpc_b64 %0\nfused_mark_0:\n\ts_mov_b32 %1, fused_mark_a-fused_mark_0\n\ts_mov_b32 %2, fused_mark_b-fused_mark_0\n\ts_mov_b32 %3, fused_mark_c-fused_mark_0"
                 : "=s"(pc), "=s"(la), "=s"(lb), "=s"(lc));
    const int len = max(la, max(lb, lc));
    const int off = ((blockIdx.x >> 3) + 32 * (threadIdx.x & 15)) * 128;
    unsigned cpf;
    asm volatile("global_load_dword %0, %1, off\n\ts_waitcnt vmcnt(0)" : "=v"(cpf) : "v"((const char*)(pc & ~3ull) + min(off, len - 4)) : "memory");
  }
  const unsigned long long tag = ((amd_dispatch_id() + 1ull) * 0x9E3779B97F4A7C15ull) ^ ((unsigned long long)__builtin_amdgcn_dispatch_ptr() * 0xC2B2AE3D27D4EB4Full) ^ 0x5851F42D4C957F2Dull;
  Loader ld;
  {
    const int b = blockIdx.x, xcd = b & 7, idx = b >> 3;
    ld.init(cre, cim, ws, lds, __builtin_amdgcn_readfirstlane(threadIdx.x >> 6) & 3, threadIdx.x & 63, xcd >> 1, (xcd & 1) * 8 + (idx >> 2), idx & 3);
  }
  phase1(x, wre, wim, w1, b1, w2, ws, lds, ld, tag);
  phase2(x, cre, cim, b2, ws, out, lds, ld, tag);
}

extern "C" void kernel_launch(void* const* d_in, const int* in_sizes, int n_in, void* d_out, int out_size, void* d_ws, size_t ws_size,
                              hipStream_t stream) {
  (void)hipFuncSetAttribute((const void*)fused, hipFuncAttributeMaxDynamicSharedMemorySize, 163840);
  fused<<<256, 512, 163840, stream>>>((const u16*)d_in[0], (const u16*)d_in[1], (const u16*)d_in[2], (const u16*)d_in[3], (const u16*)d_in[4],
                                      (const u16*)d_in[6], (const u16*)d_in[7], (const u16*)d_in[8], (const u16*)d_in[9], (char*)d_ws, (u16*)d_out);
}
```
